```python
import torch

def _softmax(x, axis=-1):
    return torch.softmax(x, dim=axis)
def _gelu(x):
    return torch.nn.functional.gelu(x, approximate="tanh")
def _silu(x):
    return torch.nn.functional.silu(x)
def _sigmoid(x):
    return torch.sigmoid(x)
def _layernorm(x, axis=-1, eps=1e-5):
    m = x.mean(dim=axis, keepdim=True)
    v = x.var(dim=axis, keepdim=True, unbiased=False)
    return (x - m) / torch.sqrt(v + eps)
def _scatter_add(tgt, idx, src, axis=0):
    return tgt.clone().index_add_(axis, idx, src)
def _segment_sum(src, seg, n):
    out = torch.zeros((n,) + src.shape[1:], dtype=src.dtype)
    return out.index_add_(0, seg, src)
def _pad(x, pw):
    flat = tuple(v for pair in reversed(pw) for v in pair)
    return torch.nn.functional.pad(x, flat)

kernel_name = 'interleave_layers__linear_kernel_hedgehog__sliding_block__prompt'

def setup_inputs():

    _w = (lambda a: (lambda t: t.to(torch.bfloat16) if t.is_floating_point() else t)(torch.as_tensor(a)))
    def _A_setup():
        B, S, D, H = 1, 2048, 576, 32
        Dh = D // H
        return {
            "x": _w((lambda *s: torch.randn(*s, dtype=torch.bfloat16) * 0.05)(B, S, D)),
            "wqkv": _w((lambda *s: torch.randn(*s, dtype=torch.bfloat16) * 0.2)(D, 3 * D)),
            "w_phi": _w((lambda *s: torch.randn(*s, dtype=torch.bfloat16) * 0.2)(H, Dh, Dh) * (Dh ** -0.5)),
            "wo": _w((lambda *s: torch.randn(*s, dtype=torch.bfloat16) * 0.05)(D, D)),
        }
    _A_in = _A_setup()

    def _B_setup():
        B, S, D = 1, 2048, 576
        return {
            "x": _w((lambda *s: torch.randn(*s, dtype=torch.bfloat16) * 0.05)(B, S, D)),
            "wqkv": _w((lambda *s: torch.randn(*s, dtype=torch.bfloat16) * 0.1)(D, 3 * D)),
            "wo": _w((lambda *s: torch.randn(*s, dtype=torch.bfloat16) * 0.05)(D, D)),
        }
    _B_in = _B_setup()

    return {
        "A_x": _A_in["x"],
        "A_wqkv": _A_in["wqkv"],
        "A_w_phi": _A_in["w_phi"],
        "A_wo": _A_in["wo"],
        "B_x": _B_in["x"],
        "B_wqkv": _B_in["wqkv"],
        "B_wo": _B_in["wo"],
    }

def reference(A_x, A_wqkv, A_w_phi, A_wo, B_x, B_wqkv, B_wo):
    def _A_ref(x, wqkv, w_phi, wo):
        B, S, D = x.shape
        H = 32; Dh = D // H
        qkv = (lambda x, *d: x.permute(*d))((x @ wqkv).reshape(B, S, 3, H, Dh), 2, 0, 3, 1, 4)
        q, k, v = qkv[0], qkv[1], qkv[2]
        pq = torch.einsum("bhsd,hdf->bhsf", q, w_phi)
        pk = torch.einsum("bhsd,hdf->bhsf", k, w_phi)
        phi_q = torch.concatenate([_softmax(pq, axis=-1), _softmax(-pq, axis=-1)], axis=-1)
        phi_k = torch.concatenate([_softmax(pk, axis=-1), _softmax(-pk, axis=-1)], axis=-1)
        kv = torch.einsum("bhsf,bhsd->bhfd", phi_k, v)
        z  = torch.einsum("bhsf->bhf", phi_k)
        num = torch.einsum("bhqf,bhfd->bhqd", phi_q, kv)
        den = torch.einsum("bhqf,bhf->bhq", phi_q, z)[..., None] + 1e-6
        o = num / den
        return (lambda x, *d: x.permute(*d))(o, 0, 2, 1, 3).reshape(B, S, D) @ wo

    def _B_ref(x, wqkv, wo):
        B, S, D = x.shape
        H = 32; Dh = D // H; W = 256
        qkv = (lambda x, *d: x.permute(*d))((x @ wqkv).reshape(B, S, 3, H, Dh), 2, 0, 3, 1, 4)
        q, k, v = qkv[0], qkv[1], qkv[2]
        scores = torch.einsum("bhqd,bhkd->bhqk", q, k) * (Dh ** -0.5)
        idx = torch.arange(S)
        mask = (idx[:, None] - idx[None, :] >= 0) & (idx[:, None] - idx[None, :] < W)
        scores = torch.where(mask[None, None], scores, -1e9)
        p = _softmax(scores, axis=-1)
        o = torch.einsum("bhqk,bhkd->bhqd", p, v)
        return (lambda x, *d: x.permute(*d))(o, 0, 2, 1, 3).reshape(B, S, D) @ wo

    x = A_x
    x = x + _A_ref(_layernorm(x, axis=-1), A_wqkv, A_w_phi, A_wo)
    x = x + _B_ref(_layernorm(x, axis=-1), B_wqkv, B_wo)
    x = x + _A_ref(_layernorm(x, axis=-1), A_wqkv, A_w_phi, A_wo)
    x = x + _B_ref(_layernorm(x, axis=-1), B_wqkv, B_wo)
    return x
```

```cpp
#include <hip/hip_runtime.h>
#include <hip/hip_bf16.h>
#include <stdint.h>

typedef unsigned short u16;
__device__ __forceinline__ float bf2f(u16 v) { return __uint_as_float(((unsigned)v) << 16); }
__device__ __forceinline__ u16 f2bf(float f) { __hip_bfloat16 b = __float2bfloat16(f); return *(u16*)&b; }

constexpr int S = 2048, D = 576, H = 32, DH = 18;

__global__ void ln_kernel(const float* x, float* y) {
    int r = blockIdx.x, t = threadIdx.x;
    float v[9]; float s = 0;
    for (int i = 0; i < 9; i++) { v[i] = x[r * D + t + 64 * i]; s += v[i]; }
    for (int m = 32; m >= 1; m >>= 1) s += __shfl_xor(s, m);
    float mean = s / D; float q = 0;
    for (int i = 0; i < 9; i++) { float d = v[i] - mean; q += d * d; }
    for (int m = 32; m >= 1; m >>= 1) q += __shfl_xor(q, m);
    float rs = rsqrtf(q / D + 1e-5f);
    for (int i = 0; i < 9; i++) y[r * D + t + 64 * i] = (v[i] - mean) * rs;
}
__global__ void cvt_in(const u16* x, float* y, int n) { int i = blockIdx.x * 256 + threadIdx.x; if (i < n) y[i] = bf2f(x[i]); }
__global__ void cvt_out(const float* x, u16* y, int n) { int i = blockIdx.x * 256 + threadIdx.x; if (i < n) y[i] = f2bf(x[i]); }

__global__ void gemm_kernel(const float* A, const u16* W, float* C, const float* R, int N, int K) {
    __shared__ float As[16][17], Ws[16][17];
    int tx = threadIdx.x & 15, ty = threadIdx.x >> 4;
    int row = blockIdx.y * 16 + ty, col = blockIdx.x * 16 + tx;
    float acc = 0;
    for (int k0 = 0; k0 < K; k0 += 16) {
        As[ty][tx] = A[row * K + k0 + tx];
        Ws[ty][tx] = bf2f(W[(k0 + ty) * N + col]);
        __syncthreads();
        for (int k = 0; k < 16; k++) acc += As[ty][k] * Ws[k][tx];
        __syncthreads();
    }
    if (R) acc += R[row * N + col];
    C[row * N + col] = acc;
}
__global__ void phi_kernel(const float* qkv, const u16* wphi, float* phi) {
    int i = blockIdx.x * 256 + threadIdx.x;
    if (i >= S * H * 2) return;
    int which = i & 1, h = (i >> 1) % H, s = i / (2 * H);
    float q[DH], p[DH];
    for (int d = 0; d < DH; d++) q[d] = qkv[s * 3 * D + which * D + h * DH + d];
    float mx = -1e30f, mn = 1e30f;
    for (int f = 0; f < DH; f++) { float a = 0; for (int d = 0; d < DH; d++) a += q[d] * bf2f(wphi[(h * DH + d) * DH + f]); p[f] = a; mx = fmaxf(mx, a); mn = fminf(mn, a); }
    float s1 = 0, s2 = 0;
    for (int f = 0; f < DH; f++) { s1 += expf(p[f] - mx); s2 += expf(mn - p[f]); }
    float* o = phi + ((size_t)(which * S + s) * H + h) * 36;
    for (int f = 0; f < DH; f++) { o[f] = expf(p[f] - mx) / s1; o[DH + f] = expf(mn - p[f]) / s2; }
}
__global__ void kv_kernel(const float* qkv, const float* phi, float* kv) {
    int h = blockIdx.x, t = threadIdx.x;
    if (t >= 36 * 19) return;
    int f = t / 19, d = t % 19;
    float acc = 0;
    for (int s = 0; s < S; s++) {
        float pk = phi[((size_t)(S + s) * H + h) * 36 + f];
        float v = d < DH ? qkv[s * 3 * D + 2 * D + h * DH + d] : 1.f;
        acc += pk * v;
    }
    kv[(h * 36 + f) * 19 + d] = acc;
}
__global__ void lin_o_kernel(const float* phi, const float* kv, float* o) {
    int i = blockIdx.x * 256 + threadIdx.x;
    if (i >= S * D) return;
    int s = i / D, c = i % D, h = c / DH, d = c % DH;
    float num = 0, den = 0;
    for (int f = 0; f < 36; f++) { float p = phi[((size_t)s * H + h) * 36 + f]; num += p * kv[(h * 36 + f) * 19 + d]; den += p * kv[(h * 36 + f) * 19 + 18]; }
    o[i] = num / (den + 1e-6f);
}
__global__ void swa_kernel(const float* qkv, float* o) {
    int i = blockIdx.x * 64 + threadIdx.x;
    int s = i % S, h = i / S;
    float q[DH], acc[DH];
    for (int d = 0; d < DH; d++) { q[d] = qkv[s * 3 * D + h * DH + d]; acc[d] = 0; }
    float m = -1e30f, l = 0; const float sc = rsqrtf((float)DH);
    int j0 = s - 255; if (j0 < 0) j0 = 0;
    for (int j = j0; j <= s; j++) {
        const float* kp = qkv + j * 3 * D + D + h * DH; const float* vp = qkv + j * 3 * D + 2 * D + h * DH;
        float a = 0; for (int d = 0; d < DH; d++) a += q[d] * kp[d];
        a *= sc; float mn = fmaxf(m, a); float al = expf(m - mn), p = expf(a - mn);
        l = l * al + p; for (int d = 0; d < DH; d++) acc[d] = acc[d] * al + p * vp[d];
        m = mn;
    }
    for (int d = 0; d < DH; d++) o[s * D + h * DH + d] = acc[d] / l;
}

extern "C" void kernel_launch(void* const* d_in, const int* in_sizes, int n_in, void* d_out, int out_size, void* d_ws, size_t ws_size, hipStream_t stream) {
    const u16* Ax = (const u16*)d_in[0]; const u16* Awqkv = (const u16*)d_in[1]; const u16* Aphi = (const u16*)d_in[2]; const u16* Awo = (const u16*)d_in[3];
    const u16* Bwqkv = (const u16*)d_in[5]; const u16* Bwo = (const u16*)d_in[6];
    float* ws = (float*)d_ws;
    float* x = ws; float* xn = x + S * D; float* qkv = xn + S * D; float* phi = qkv + S * 3 * D; float* kv = phi + 2 * S * H * 36; float* o = kv + H * 36 * 19 + 64; float* x2 = o + S * D;
    cvt_in<<<(S * D + 255) / 256, 256, 0, stream>>>(Ax, x, S * D);
    for (int l = 0; l < 4; l++) {
        bool isA = (l & 1) == 0;
        ln_kernel<<<S, 64, 0, stream>>>(x, xn);
        gemm_kernel<<<dim3(3 * D / 16, S / 16), 256, 0, stream>>>(xn, isA ? Awqkv : Bwqkv, qkv, nullptr, 3 * D, D);
        if (isA) {
            phi_kernel<<<(S * H * 2 + 255) / 256, 256, 0, stream>>>(qkv, Aphi, phi);
            kv_kernel<<<H, 704, 0, stream>>>(qkv, phi, kv);
            lin_o_kernel<<<(S * D + 255) / 256, 256, 0, stream>>>(phi, kv, o);
        } else {
            swa_kernel<<<S * H / 64, 64, 0, stream>>>(qkv, o);
        }
        gemm_kernel<<<dim3(D / 16, S / 16), 256, 0, stream>>>(o, isA ? Awo : Bwo, x2, x, D, D);
        float* t = x; x = x2; x2 = t;
    }
    cvt_out<<<(S * D + 255) / 256, 256, 0, stream>>>(x, (u16*)d_out, S * D);
}
```

```cpp
#include <hip/hip_runtime.h>
#include <stdint.h>

typedef __attribute__((ext_vector_type(8))) __bf16 bf16x8;
typedef __attribute__((ext_vector_type(4))) __bf16 bf16x4;
typedef __attribute__((ext_vector_type(2))) __bf16 bf16x2;
typedef __attribute__((ext_vector_type(4))) short s16x4;
typedef __attribute__((ext_vector_type(4))) float f32x4;
typedef __attribute__((ext_vector_type(2))) float f32x2;
typedef __attribute__((ext_vector_type(16))) float f32x16;
typedef __attribute__((ext_vector_type(4))) unsigned u32x4;
typedef __attribute__((ext_vector_type(2))) unsigned u32x2;
typedef unsigned short u16;
#define LAS __attribute__((address_space(3)))
#define GAS __attribute__((address_space(1)))
#define RLX_AGENT __ATOMIC_RELAXED, __HIP_MEMORY_SCOPE_AGENT
#define DEVI __device__ __forceinline__

constexpr int SEQ = 2048, DM = 576, NQKV = 1728;
constexpr unsigned TKT = 18 * 1024;
constexpr unsigned ACT_BYTES = 128 * TKT;
constexpr unsigned IMG_BYTES = 7 * TKT;
constexpr unsigned WS_FLAGS = 4096;
constexpr unsigned FL_X = 0, FL_O = 3, FL_K = 7, FL_W1 = 11, FL_N = 12;
constexpr unsigned WS_XB = WS_FLAGS + FL_N * 256 * 128;
constexpr unsigned WS_OB = WS_XB + 3 * ACT_BYTES;
constexpr unsigned WS_KVH = WS_OB + 4 * ACT_BYTES;
constexpr unsigned WS_KVP = WS_KVH + 2 * 256 * 20480;
constexpr unsigned WS_IMA = WS_KVP + 2 * 256 * 3072;
constexpr unsigned WS_IMB = WS_IMA + 16 * IMG_BYTES;
constexpr unsigned WS_WOI = WS_IMB + 16 * IMG_BYTES;
constexpr unsigned WS_END = WS_WOI + 2 * 36 * TKT;
constexpr unsigned WS_DBG = WS_END;
constexpr unsigned L_OST = 5 * TKT;
constexpr unsigned L_STG = IMG_BYTES;
constexpr unsigned L_KT = L_STG - 40960;
constexpr unsigned L_QS = L_STG + 20480;
constexpr unsigned L_PK = L_STG, L_VV = L_STG + 20480;
constexpr unsigned L_CS = L_STG + 32768;
constexpr unsigned L_PST = 131072;
constexpr unsigned L_PHI = L_PST + 21504;
constexpr unsigned L_KVS = L_STG;
constexpr unsigned LDS_BYTES = 163840;
constexpr unsigned SPIN_CAP = 1u << 17;
constexpr float LOG2E = 1.4426950408889634f;

struct Params {
    const u16* x; const u16* wqkvA; const u16* wphi; const u16* woA; const u16* wqkvB; const u16* woB;
    u16* out; unsigned char* ws;
};

DEVI f32x4 mfma32(u32x4 a, u32x4 b, f32x4 c) { return __builtin_amdgcn_mfma_f32_16x16x32_bf16(__builtin_bit_cast(bf16x8, a), __builtin_bit_cast(bf16x8, b), c, 0, 0, 0); }
DEVI f32x16 mfmaL(u32x4 a, u32x4 b, f32x16 c) { return __builtin_amdgcn_mfma_f32_32x32x16_bf16(__builtin_bit_cast(bf16x8, a), __builtin_bit_cast(bf16x8, b), c, 0, 0, 0); }
DEVI f32x4 mfma16(u32x2 a, u32x2 b, f32x4 c) { return mfma32((u32x4){a.x, a.y, 0u, 0u}, (u32x4){b.x, b.y, 0u, 0u}, c); }
DEVI unsigned pk2(float lo, float hi) { f32x2 f = {lo, hi}; return __builtin_bit_cast(unsigned, __builtin_convertvector(f, bf16x2)); }
DEVI float dot2(unsigned a, unsigned b, float c) { return __builtin_amdgcn_fdot2_f32_bf16(__builtin_bit_cast(bf16x2, a), __builtin_bit_cast(bf16x2, b), c, false); }
DEVI float ex2(float x) { return __builtin_amdgcn_exp2f(x); }
DEVI float rcp(float x) { return __builtin_amdgcn_rcpf(x); }
DEVI float f_of(unsigned u) { return __builtin_bit_cast(float, u); }
DEVI unsigned u_of(float f) { return __builtin_bit_cast(unsigned, f); }
DEVI float x16(float v, int lane) { auto r = __builtin_amdgcn_permlane16_swap(u_of(v), u_of(v), false, false); return f_of((lane & 16) ? r[0] : r[1]); }
DEVI float x32(float v, int lane) { auto r = __builtin_amdgcn_permlane32_swap(u_of(v), u_of(v), false, false); return f_of((lane & 32) ? r[0] : r[1]); }
DEVI float red_add(float v) { auto r = __builtin_amdgcn_permlane16_swap(u_of(v), u_of(v), false, false); v = f_of(r[0]) + f_of(r[1]);
    auto q = __builtin_amdgcn_permlane32_swap(u_of(v), u_of(v), false, false); return f_of(q[0]) + f_of(q[1]); }
DEVI float red_max(float v) { auto r = __builtin_amdgcn_permlane16_swap(u_of(v), u_of(v), false, false); v = fmaxf(f_of(r[0]), f_of(r[1]));
    auto q = __builtin_amdgcn_permlane32_swap(u_of(v), u_of(v), false, false); return fmaxf(f_of(q[0]), f_of(q[1])); }
DEVI float r16_add(float v) { auto r = __builtin_amdgcn_permlane16_swap(u_of(v), u_of(v), false, false); return f_of(r[0]) + f_of(r[1]); }
DEVI float r16_max(float v) { auto r = __builtin_amdgcn_permlane16_swap(u_of(v), u_of(v), false, false); return fmaxf(f_of(r[0]), f_of(r[1])); }
DEVI float r16_min(float v) { auto r = __builtin_amdgcn_permlane16_swap(u_of(v), u_of(v), false, false); return fminf(f_of(r[0]), f_of(r[1])); }

DEVI float max3f(float a, float b, float c) {
    return __builtin_fmaxf(__builtin_fmaxf(a, b), c);
}
#define BARRIER() asm volatile("s_waitcnt lgkmcnt(0)\n\ts_barrier" ::: "memory")
#define DRAIN() asm volatile("s_waitcnt vmcnt(0)" ::: "memory")

extern __shared__ __attribute__((aligned(16))) unsigned char smem[];
DEVI u32x4 lds_r128(unsigned off) { return *(const LAS u32x4*)((LAS unsigned char*)smem + off); }
DEVI u32x2 lds_r64(unsigned off) { return *(const LAS u32x2*)((LAS unsigned char*)smem + off); }
DEVI unsigned lds_r32(unsigned off) { return *(const LAS unsigned*)((LAS unsigned char*)smem + off); }
DEVI unsigned lds_r16(unsigned off) { return *(const LAS u16*)((LAS unsigned char*)smem + off); }
DEVI void lds_w128(unsigned off, u32x4 v) { *(LAS u32x4*)((LAS unsigned char*)smem + off) = v; }
DEVI void lds_w64(unsigned off, u32x2 v) { *(LAS u32x2*)((LAS unsigned char*)smem + off) = v; }
DEVI void lds_w32(unsigned off, unsigned v) { *(LAS unsigned*)((LAS unsigned char*)smem + off) = v; }
DEVI u32x2 lds_tr(unsigned off) { return __builtin_bit_cast(u32x2, __builtin_amdgcn_ds_read_tr16_b64_v4i16((LAS s16x4*)((LAS unsigned char*)smem + off))); }

typedef __amdgpu_buffer_rsrc_t rsrc_t;
DEVI rsrc_t mk_rsrc(const void* p, unsigned bytes) { return __builtin_amdgcn_make_buffer_rsrc((void*)p, 0, bytes, 0x00020000); }
DEVI u32x4 ld128(rsrc_t r, unsigned voff, unsigned soff) { return __builtin_amdgcn_raw_buffer_load_b128(r, voff, soff, 0); }
DEVI u32x2 ld64(rsrc_t r, unsigned voff, unsigned soff) { return __builtin_amdgcn_raw_buffer_load_b64(r, voff, soff, 0); }
DEVI u32x4 ld128c(rsrc_t r, unsigned voff, unsigned soff) { return __builtin_amdgcn_raw_buffer_load_b128(r, voff, soff, 16); }
DEVI void st128c(rsrc_t r, unsigned voff, unsigned soff, u32x4 v) { __builtin_amdgcn_raw_buffer_store_b128(v, r, voff, soff, 16); }
DEVI void st32c(rsrc_t r, unsigned voff, unsigned soff, unsigned v) { __builtin_amdgcn_raw_buffer_store_b32(v, r, voff, soff, 16); }
DEVI void st64c(rsrc_t r, unsigned voff, unsigned soff, u32x2 v) { __builtin_amdgcn_raw_buffer_store_b64(v, r, voff, soff, 16); }
DEVI void dma1k(const unsigned char* gsrc, unsigned lds_dst) {
    unsigned keep;
    asm volatile("s_mov_b32 %0, m0\n\ts_mov_b32 m0, %2\n\ts_nop 0\n\tglobal_load_lds_dwordx4 %1, off sc1\n\ts_mov_b32 m0, %0" : "=&s"(keep) : "v"(gsrc), "s"(lds_dst) : "memory");
}
DEVI void ld128_raw(u32x4& d, const void* base, unsigned voff) {
    asm volatile("global_load_dwordx4 %0, %1, %2" : "=v"(d) : "v"(voff), "s"(base) : "memory");
}
DEVI void wait_flags(const unsigned char* ws, unsigned kind, unsigned idx, unsigned tag) {
    const GAS unsigned* f = (const GAS unsigned*)(ws + WS_FLAGS + (kind * 256 + idx) * 128);
#pragma unroll 1
    for (unsigned sp = 0; sp < SPIN_CAP; ++sp) {
        const unsigned v = __hip_atomic_load(f, RLX_AGENT);
        if (__all(v == tag)) break;
        __builtin_amdgcn_s_sleep(1);
    }
    asm volatile("" ::: "memory");
}
DEVI void set_flag(unsigned char* ws, unsigned kind, unsigned idx, unsigned tag) {
    __hip_atomic_store((GAS unsigned*)(ws + WS_FLAGS + (kind * 256 + idx) * 128), tag, RLX_AGENT);
}

#define STAMP(i) do { } while (0)

DEVI u32x4 onehot8(int j) {
    const unsigned v = (j & 1) ? 0x3F800000u : 0x3F80u; const int w = j >> 1;
    return (u32x4){w == 0 ? v : 0u, w == 1 ? v : 0u, w == 2 ? v : 0u, w == 3 ? v : 0u};
}
DEVI u32x4 sel_frag(int gq, int src) {
    const int og = src >> 3, oj = (src & 7) + (src >= 16 ? 6 : 0);
    return onehot8(og == gq ? oj : -2);
}
DEVI unsigned img_slot(bool sets, int which, int h, int f) {
    const int c = (f >= 5) + (f >= 10) + (f >= 14);
    const int kst = f + (c == 0 ? 0 : c == 3 ? -9 : -5);
    const int gv = f < 16 ? (f >> 2) : 2 * h + f - 16, sv = f < 16 ? 4 * h + (f & 3) : 8;
    const int gt = sets ? (c ^ (2 * h)) : gv, st = (sets ? kst : sv) + (which == 0 ? 19 : which == 2 ? 9 : 0);
    return (unsigned)(st >> 2) * 1024u + (unsigned)(4 * gt + (st & 3)) * 16u;
}
DEVI int wo_krow(int mat, int s, int gq, int j) {
    const int a = 36 * s + (mat ? 18 * (gq >> 1) + 8 * (j >> 2) + 4 * (gq & 1) : 18 * (j >> 2) + 4 * gq) + (j & 3);
    const int c = 36 * (8 * (s - 16) + 2 * gq + (j >> 2)) + 18 * ((j >> 1) & 1) + 16 + (j & 1);
    return s < 16 ? a : c;
}
DEVI int fresh(int v) { asm volatile("" : "+v"(v)); return v; }
DEVI int sfresh(int v) { asm volatile("" : "+s"(v)); return v; }

#define SB() __builtin_amdgcn_sched_barrier(0)
template <int T0, int NT, int S0, int S1, bool SSQ, int D, class F>
DEVI void gemm_img(f32x4 (&acc)[7], const u32x4 (&xf)[18], unsigned lb, f32x4& sq, F side) {
    u32x4 a[D][NT];
#pragma unroll
    for (int s = S0; s < S0 + D; ++s)
#pragma unroll
        for (int t = 0; t < NT; ++t) a[s % D][t] = lds_r128(lb + (T0 + t) * TKT + s * 1024);
    SB();
#pragma unroll
    for (int s = S0; s < S1; ++s) {
        if (SSQ) { sq = mfma32(xf[s], xf[s], sq); SB(); }
        side(s);
        SB();
#pragma unroll
        for (int t = 0; t < NT; ++t) {
            acc[T0 + t] = mfma32(a[s % D][t], xf[s], acc[T0 + t]);
            SB();
            if (s + D < S1) { a[s % D][t] = lds_r128(lb + (T0 + t) * TKT + (s + D) * 1024); SB(); }
        }
    }
}
DEVI void colsum_tile(int t, unsigned csb, int lane) {
    f32x4 c = {0.f, 0.f, 0.f, 0.f}; const u32x4 ones = {0x3F803F80u, 0x3F803F80u, 0x3F803F80u, 0x3F803F80u};
    u32x4 a[18];
#pragma unroll
    for (int s = 0; s < 18; ++s) a[s] = lds_r128(t * TKT + s * 1024 + lane * 16);
    SB();
#pragma unroll
    for (int s = 0; s < 18; ++s) c = mfma32(a[s], ones, c);
    if ((lane & 15) == 0) lds_w128(csb + ((lane >> 4) * 28 + 4 * t) * 4, __builtin_bit_cast(u32x4, c));
}
DEVI void dual_softmax(const float (&q)[9], int lane, u32x4& fx, u32x4& fy, unsigned& fe) {
    float mxX = fmaxf(fmaxf(fmaxf(q[0], q[1]), fmaxf(q[2], q[3])), q[4]), mnX = fminf(fminf(fminf(q[0], q[1]), fminf(q[2], q[3])), q[4]);
    float mxY = fmaxf(fmaxf(q[5], q[6]), fmaxf(q[7], q[8])), mnY = fminf(fminf(q[5], q[6]), fminf(q[7], q[8]));
    mxX = r16_max(mxX); mnX = r16_min(mnX); mxY = r16_max(mxY); mnY = r16_min(mnY);
    { const float a = x32(mxY, lane), c = x32(mnY, lane), d = x32(mxX, lane), e = x32(mnX, lane);
      mxX = fmaxf(mxX, a); mnX = fminf(mnX, c); mxY = fmaxf(mxY, d); mnY = fminf(mnY, e); }
    float ep[9], en[9];
#pragma unroll
    for (int i = 0; i < 5; ++i) { ep[i] = ex2(q[i] - mxX); en[i] = ex2(mnX - q[i]); }
#pragma unroll
    for (int i = 5; i < 9; ++i) { ep[i] = ex2(q[i] - mxY); en[i] = ex2(mnY - q[i]); }
    float spX = (ep[0] + ep[1]) + (ep[2] + ep[3]) + ep[4], snX = (en[0] + en[1]) + (en[2] + en[3]) + en[4];
    float spY = (ep[5] + ep[6]) + (ep[7] + ep[8]), snY = (en[5] + en[6]) + (en[7] + en[8]);
    spX = r16_add(spX); snX = r16_add(snX); spY = r16_add(spY); snY = r16_add(snY);
    { const float a = x32(spY, lane), c = x32(snY, lane), d = x32(spX, lane), e = x32(snX, lane);
      spX = rcp(spX + a); snX = rcp(snX + c); spY = rcp(spY + d); snY = rcp(snY + e); }
#pragma unroll
    for (int i = 0; i < 5; ++i) { ep[i] *= spX; en[i] *= snX; }
#pragma unroll
    for (int i = 5; i < 9; ++i) { ep[i] *= spY; en[i] *= snY; }
    fx = (u32x4){pk2(ep[0], ep[1]), pk2(ep[2], ep[3]), pk2(ep[4], en[0]), pk2(en[1], en[2])};
    fy = (u32x4){pk2(ep[5], ep[6]), pk2(ep[7], ep[8]), pk2(en[5], en[6]), pk2(en[7], en[8])};
    fe = pk2(en[3], en[4]);
}

struct KVFrag { u32x4 km; unsigned kl; u32x2 v[4]; };
DEVI void att_load(KVFrag& f, unsigned ka, unsigned la, unsigned va) {
    f.km = lds_r128(ka); f.kl = lds_r32(la);
#pragma unroll
    for (int e = 0; e < 4; ++e) f.v[e] = lds_tr(va + e * 384);
}
DEVI f32x16 att_scores(u32x4 km, unsigned kl, u32x4 q0, u32x4 q1, f32x16 c, int mode, int dl) {
    f32x16 s = mfmaL(km, q0, c);
    s = mfmaL((u32x4){kl, 0u, 0u, 0u}, q1, s);
    if (mode) {
        const bool dg = mode == 1;
#pragma unroll
        for (int v = 0; v < 16; ++v) { const int cv = (v & 3) + 8 * (v >> 2); if ((cv > dl) == dg) s[v] = -1e30f; }
    }
    return s;
}
DEVI void att_accum(const f32x16& s, const u32x2 (&v)[4], f32x16& o) {
    const u32x4 p0 = {pk2(ex2(s[0]), ex2(s[1])), pk2(ex2(s[2]), ex2(s[3])), pk2(ex2(s[4]), ex2(s[5])), pk2(ex2(s[6]), ex2(s[7]))};
    o = mfmaL((u32x4){v[0].x, v[0].y, v[1].x, v[1].y}, p0, o);
    const u32x4 p1 = {pk2(ex2(s[8]), ex2(s[9])), pk2(ex2(s[10]), ex2(s[11])), pk2(ex2(s[12]), ex2(s[13])), pk2(ex2(s[14]), ex2(s[15]))};
    o = mfmaL((u32x4){v[2].x, v[2].y, v[3].x, v[3].y}, p1, o);
}
DEVI float max16(const f32x16& s, float m) {
#pragma unroll
    for (int v = 0; v < 16; v += 2) m = max3f(m, s[v], s[v + 1]);
    return m;
}
DEVI float r32_max(float v) { auto q = __builtin_amdgcn_permlane32_swap(u_of(v), u_of(v), false, false); return fmaxf(f_of(q[0]), f_of(q[1])); }
DEVI f32x16 splat16(float v) { f32x16 r;
#pragma unroll
    for (int i = 0; i < 16; ++i) r[i] = v;
    return r; }
#define GD1 1
struct Ctx { int wave, g, p, cu; unsigned tag; };

template <bool ISB>
DEVI void layer_fn(const Params& P, const rsrc_t wsr, const int lane_in, const Ctx& C, const int it, u32x4 (&xf)[18], f32x4 (&res)[3]) {
    const int wave = sfresh(C.wave), g = sfresh(C.g), p = sfresh(C.p), cu = g * 16 + p;
    const unsigned tag = C.tag;
    const int layer = 2 * it + (ISB ? 1 : 0);
    const int tp = p >> 2, cg = p & 3;
    const f32x4 Z4 = {0.f, 0.f, 0.f, 0.f};
    constexpr unsigned csb = L_CS + (ISB ? 448 : 0);
    const bool first = !ISB && it == 0, last = ISB && it == 1;
    u32x4 wof[18];
    f32x4 acc[7];
    float mean, rstd;
    const unsigned wob = WS_WOI + ((ISB ? 36 : 0) + 9 * cg + wave) * TKT;
    const unsigned nimo = (ISB ? WS_IMA : WS_IMB) + p * IMG_BYTES;
    {
        const int lane = fresh(lane_in), gq = lane >> 4, tk = lane & 15;
        if (it == 0 && wave < 5) colsum_tile(wave, csb, lane);
        if (!first) {
            wait_flags(P.ws, FL_X + layer - 1, g * 16 + (wave >> 1) * 4 + (lane & 3), tag);
            STAMP(layer * 12 + 2);
            const unsigned xb = WS_XB + (layer - 1) * ACT_BYTES + (g * 8 + wave) * TKT;
#pragma unroll
            for (int s = 0; s < 18; ++s) xf[s] = ld128c(wsr, lane * 16 + s * 1024, xb);
        }
#pragma unroll
        for (int t = 0; t < 7; ++t) acc[t] = Z4;
        f32x4 sq = Z4;
        {
            const unsigned char* cimg = P.ws + (ISB ? WS_IMB : WS_IMA) + p * IMG_BYTES + lane * 16;
            gemm_img<0, 5, 0, 18, true, GD1>(acc, xf, lane * 16, sq, [&](int s) {
                if (s >= 8 && s < 13 && !first && (s < 12 || wave < 4)) dma1k(cimg + (90 + wave + 8 * (s - 8)) * 1024, (90 + wave + 8 * (s - 8)) * 1024);
                if (!ISB && s >= 9 && !first) { wof[2 * (s - 9)] = ld128c(wsr, lane * 16 + (2 * (s - 9)) * 1024, wob); wof[2 * (s - 9) + 1] = ld128c(wsr, lane * 16 + (2 * (s - 9) + 1) * 1024, wob); }
            });
        }
        STAMP(layer * 12 + 3);
        const float dg = (tk & 2) ? ((tk & 1) ? sq[3] : sq[2]) : ((tk & 1) ? sq[1] : sq[0]);
        const float ssq = red_add(gq == (tk >> 2) ? dg : 0.f);
        mean = acc[4][2] * (1.f / DM);
        rstd = __builtin_amdgcn_rsqf(fmaxf(ssq * (1.f / DM) - mean * mean, 0.f) + 1e-5f);
        if (it == 0) BARRIER();
    }
    {
        const int lane = fresh(lane_in), gq = lane >> 4, tk = lane & 15;
        float y[18];
        {
            const float ks = ISB ? rstd : rstd * LOG2E;
#pragma unroll
            for (int t = 0; t < 5; ++t) {
                const f32x4 cs = __builtin_bit_cast(f32x4, lds_r128(csb + (gq * 28 + 4 * t) * 4));
#pragma unroll
                for (int i = 0; i < 4; ++i) if (4 * t + i < 18) y[4 * t + i] = (acc[t][i] - mean * cs[i]) * (4 * t + i < 9 ? ks : rstd);
            }
        }
        const auto sa = __builtin_amdgcn_permlane16_swap(pk2(y[9], y[10]), pk2(y[13], y[14]), false, false);
        const auto sb = __builtin_amdgcn_permlane16_swap(pk2(y[11], y[12]), pk2(y[15], y[16]), false, false);
        const u32x4 vm = {sa[0], sb[0], sa[1], sb[1]};
        const auto sl = __builtin_amdgcn_permlane16_swap(u_of(y[17]), u_of(y[17]), false, false);
        const int hx = gq >> 1, od = gq & 1;
        if (ISB) {
            const auto ka = __builtin_amdgcn_permlane16_swap(pk2(y[0], y[1]), pk2(y[4], y[5]), false, false);
            const auto kb = __builtin_amdgcn_permlane16_swap(pk2(y[2], y[3]), pk2(y[6], y[7]), false, false);
            const u32x4 km = {ka[0], kb[0], ka[1], kb[1]};
            const auto kl = __builtin_amdgcn_permlane16_swap(u_of(y[8]), u_of(y[8]), false, false);
            const u32x4 lr = {pk2(f_of(kl[0]), f_of(kl[1])), 0u, pk2(f_of(sl[0]), f_of(sl[1])), 0x3F80u};
            const int kk = (wave & 1) * 16 + tk;
            const unsigned tb = (wave >> 1) * 5120, oc = 1024 + (kk >> 3) * 384;
            const unsigned a0 = tb + od * 2560 + hx * 512 + kk * 16, a1 = tb + od * 2560 + oc + hx * 128 + (kk & 7) * 16, a2 = tb + hx * 2560 + oc + 256 + ((kk & 7) ^ 4) * 16;
            const unsigned hb = WS_KVH + (it * 256 + cu) * 20480;
            st128c(wsr, a0, hb, km); st128c(wsr, a1, hb, vm);
            lds_w128(L_STG + a0, km); lds_w128(L_STG + a1, vm);
            if (od == 0) { st128c(wsr, a2, hb, lr); lds_w128(L_STG + a2, lr); }
            asm volatile("s_waitcnt vmcnt(3)" ::: "memory");
            BARRIER();
        } else {
            float kz[9];
#pragma unroll
            for (int i = 0; i < 9; ++i) kz[i] = y[i];
            u32x4 fx, fy; unsigned fe;
            dual_softmax(kz, lane, fx, fy, fe);
            const unsigned tok = wave * 16 + tk;
            const unsigned pk = L_PK + tok * 80, vv = L_VV + tok * 48;
            lds_w128(pk + hx * 10240 + od * 16, fx); lds_w32(pk + hx * 10240 + 64 + od * 4, fe); lds_w128(pk + (hx ^ 1) * 10240 + 32 + od * 16, fy);
            lds_w128(vv + od * 6144 + hx * 16, vm);
            lds_w128(vv + hx * 6144 + 32, (u32x4){pk2(f_of(sl[0]), f_of(sl[1])), 0x3F80u, 0u, 0u});
            STAMP(53 + it);
            asm volatile("s_waitcnt vmcnt(10)" ::: "memory");
            BARRIER();
            STAMP(55 + it);
            if (wave < 6) {
                const int h = wave >= 3, ft = wave - 3 * h;
                const unsigned trow = (unsigned)(lane >> 2), tcol = (unsigned)(lane & 3) * 8u;
                const unsigned pa = L_PK + h * 10240 + trow * 80 + ft * 32 + tcol, pb = L_VV + h * 6144 + trow * 48 + tcol;
                u32x2 ta[8], tb[8], td[8];
#pragma unroll
                for (int s = 0; s < 8; ++s) { ta[s] = lds_tr(pa + (16 * s) * 80); tb[s] = lds_tr(pb + (16 * s) * 48); td[s] = lds_tr(pb + (16 * s) * 48 + 32); }
                SB();
                f32x4 k0 = Z4, k1 = Z4;
#pragma unroll
                for (int s = 0; s < 4; ++s) {
                    const u32x4 af = {ta[2 * s].x, ta[2 * s].y, ta[2 * s + 1].x, ta[2 * s + 1].y};
                    k0 = mfma32(af, (u32x4){tb[2 * s].x, tb[2 * s].y, tb[2 * s + 1].x, tb[2 * s + 1].y}, k0);
                    k1 = mfma32(af, (u32x4){td[2 * s].x, td[2 * s].y, td[2 * s + 1].x, td[2 * s + 1].y}, k1);
                }
                const unsigned kb = WS_KVP + ((it * 16 + p) * 16 + g) * 3072;
                const unsigned so = h * 1520 + (16 * ft + 4 * gq) * 2;
                if (16 * ft + 4 * gq < 40) {
                    st64c(wsr, so + tk * 80, kb, (u32x2){pk2(k0[0], k0[1]), pk2(k0[2], k0[3])});
                    if (tk < 3) st64c(wsr, so + (16 + tk) * 80, kb, (u32x2){pk2(k1[0], k1[1]), pk2(k1[2], k1[3])});
                }
            }
        }
        STAMP(layer * 12 + 0 + 12 * (layer == 0 ? 4 : 0) + (layer == 0 ? 4 : 0));
        if (it == 0 && wave >= 6) colsum_tile(wave - 1, csb, lane);
        { f32x4 dummy; gemm_img<5, 2, 0, 9, false, 3>(acc, xf, lane * 16, dummy, [](int) {}); }
        DRAIN();
        BARRIER();
        if (wave == 0 && lane == 0) set_flag(P.ws, FL_K + layer, cu, tag);
        STAMP(layer * 12 + 4);
    }
    f32x4 oa[2][2];
    {
        const int lane = fresh(lane_in), gq = lane >> 4, tk = lane & 15;
        const unsigned char* nimg1 = P.ws + nimo + lane * 16;
        { f32x4 dummy; gemm_img<5, 2, 9, 18, false, 3>(acc, xf, lane * 16, dummy, [&](int s) {
              if (!ISB && s < 16 && (s < 15 || wave < 6)) dma1k(nimg1 + (wave + 8 * (s - 9)) * 1024, (wave + 8 * (s - 9)) * 1024); }); }
        if (first) {
#pragma unroll 1
            for (int i = 0; i < 4; ++i) wait_flags(P.ws, FL_W1, i * 64 + lane, tag);
            const rsrc_t rx = mk_rsrc(P.x, SEQ * DM * 2);
#pragma unroll
            for (int i = 0; i < 3; ++i) {
                const int tt = 2 * tp + (i < 2 ? i : (wave & 1)), ct = 9 * cg + (i < 2 ? wave : 8);
                const u32x2 r = ld64(rx, ((unsigned)tk * DM + 4u * gq) * 2u, ((unsigned)(g * 128 + tt * 16) * DM + 16u * ct) * 2u);
                res[i] = (f32x4){f_of(r.x << 16), f_of(r.x & 0xffff0000u), f_of(r.y << 16), f_of(r.y & 0xffff0000u)};
            }
        }
        float q[9];
        {
            const float qs = ISB ? rstd * (0.23570226039551584f * LOG2E) : rstd * LOG2E;
            const float c19 = f_of(lds_r32(csb + (gq * 28 + 19) * 4));
            const f32x4 c5 = __builtin_bit_cast(f32x4, lds_r128(csb + (gq * 28 + 20) * 4)), c6 = __builtin_bit_cast(f32x4, lds_r128(csb + (gq * 28 + 24) * 4));
            q[0] = (acc[4][3] - mean * c19) * qs;
#pragma unroll
            for (int i = 0; i < 4; ++i) { q[1 + i] = (acc[5][i] - mean * c5[i]) * qs; q[5 + i] = (acc[6][i] - mean * c6[i]) * qs; }
        }
        STAMP(layer * 12 + 5);
        if (ISB) {
            {
                const int hx = gq >> 1, od = gq & 1, kk = (wave & 1) * 16 + tk;
                const auto qa = __builtin_amdgcn_permlane16_swap(pk2(q[0], q[1]), pk2(q[4], q[5]), false, false);
                const auto qb = __builtin_amdgcn_permlane16_swap(pk2(q[2], q[3]), pk2(q[6], q[7]), false, false);
                const auto ql = __builtin_amdgcn_permlane16_swap(u_of(q[8]), u_of(q[8]), false, false);
                lds_w128(L_QS + ((wave >> 1) * 2 + od) * 1152 + hx * 512 + kk * 16, (u32x4){qa[0], qb[0], qa[1], qb[1]});
                if (od == 0) lds_w32(L_QS + ((wave >> 1) * 2 + hx) * 1152 + 1024 + kk * 4, pk2(f_of(ql[0]), f_of(ql[1])));
            }
            BARRIER();
            const int h = wave & 1, jq = wave < 4 ? (wave >> 1) : 3 - ((wave - 4) >> 1);
            const int r = lane & 31, hh = lane >> 5, dl = r - 4 * hh;
            u32x4 hv[5];
            const int src = g - 2 + (wave >> 2);
            if (src >= 0) {
                wait_flags(P.ws, FL_K + layer, src * 16 + p, tag);
                const unsigned hb = WS_KVH + (it * 256 + src * 16 + p) * 20480 + (wave & 3) * 5120;
#pragma unroll
                for (int i = 0; i < 5; ++i) hv[i] = ld128c(wsr, lane * 16 + i * 1024, hb);
            }
            STAMP(layer * 12 + 1);
            const u32x4 q0 = lds_r128(L_QS + (jq * 2 + h) * 1152 + lane * 16);
            const unsigned q1w = lds_r32(L_QS + (jq * 2 + h) * 1152 + 1024 + r * 4);
            const u32x4 q1 = {hh ? 0u : q1w, 0u, 0u, 0u};
            const unsigned kb = L_KT + h * 2560;
            const unsigned ka = kb + lane * 16, la = kb + 1280 + (r >> 3) * 384 + ((r & 7) ^ 4) * 16;
            const unsigned k8 = 4 * hh + ((lane >> 2) & 3);
            const unsigned va = kb + 1024 + ((lane & 16) ? 256 + (k8 ^ 4) * 16 + 8 : (lane & 2) * 64 + k8 * 16 + (lane & 1) * 8);
            const f32x16 Z16 = splat16(0.f);
            f32x16 negm;
            {
                const unsigned o = (jq + 8) * 5120;
                const f32x16 sd = att_scores(lds_r128(ka + o), lds_r32(la + o), q0, q1, Z16, 1, dl);
                negm = splat16(-r32_max(max16(sd, -1e30f)));
            }
            f32x16 o16 = Z16;
            KVFrag fr[2];
            att_load(fr[0], ka + 8 * 5120, la + 8 * 5120, va + 8 * 5120);
#pragma unroll
            for (int i = 0; i < 4; ++i) {
                if (i < 3) att_load(fr[(i + 1) & 1], ka + (9 + i) * 5120, la + (9 + i) * 5120, va + (9 + i) * 5120);
                wof[2 * i] = ld128c(wsr, lane * 16 + (2 * i) * 1024, wob); wof[2 * i + 1] = ld128c(wsr, lane * 16 + (2 * i + 1) * 1024, wob);
                SB();
                if (i <= jq) {
                    const f32x16 sc = att_scores(fr[i & 1].km, fr[i & 1].kl, q0, q1, negm, i == jq ? 1 : 0, dl);
                    att_accum(sc, fr[i & 1].v, o16);
                }
                SB();
            }
            if (src >= 0) {
#pragma unroll
                for (int i = 0; i < 5; ++i) lds_w128(L_KT + wave * 5120 + i * 1024 + lane * 16, hv[i]);
            }
            BARRIER();
            STAMP(layer * 12 + 6);
            const int kmin = g >= 2 ? 0 : (g == 1 ? 4 : 8), k0 = jq > kmin ? jq : kmin;
            att_load(fr[1], ka + 7 * 5120, la + 7 * 5120, va + 7 * 5120);
#pragma unroll
            for (int i = 7; i >= 0; --i) {
                if (i > 0) att_load(fr[(i - 1) & 1], ka + (i - 1) * 5120, la + (i - 1) * 5120, va + (i - 1) * 5120);
                if (i >= 3) { wof[22 - 2 * i] = ld128c(wsr, lane * 16 + (22 - 2 * i) * 1024, wob); wof[23 - 2 * i] = ld128c(wsr, lane * 16 + (23 - 2 * i) * 1024, wob); }
                if (i >= 1 && !last && (i > 1 || wave < 6)) dma1k(nimg1 + (wave + 8 * (7 - i)) * 1024, (wave + 8 * (7 - i)) * 1024);
                SB();
                if (i >= k0) {
                    const f32x16 sc = att_scores(fr[i & 1].km, fr[i & 1].kl, q0, q1, negm, i == jq ? 2 : 0, dl);
                    att_accum(sc, fr[i & 1].v, o16);
                }
                SB();
            }
            asm volatile("s_nop 7\n\ts_nop 7" : "+v"(o16));
            {
                float t = 0.f;
#pragma unroll
                for (int v = 0; v < 11; ++v) t += __builtin_fabsf(o16[v]);
                if (__any(!(t < 3.0e38f))) {
                    float m = -1e30f;
#pragma unroll 1
                    for (int i = k0; i <= jq + 8; ++i)
                        m = max16(att_scores(lds_r128(ka + i * 5120), lds_r32(la + i * 5120), q0, q1, Z16, i == jq + 8 ? 1 : (i == jq ? 2 : 0), dl), m);
                    negm = splat16(-r32_max(m));
                    o16 = Z16;
#pragma unroll 1
                    for (int i = k0; i <= jq + 8; ++i) {
                        KVFrag f; att_load(f, ka + i * 5120, la + i * 5120, va + i * 5120);
                        const f32x16 sc = att_scores(f.km, f.kl, q0, q1, negm, i == jq + 8 ? 1 : (i == jq ? 2 : 0), dl);
                        att_accum(sc, f.v, o16);
                    }
                    asm volatile("s_nop 7\n\ts_nop 7" : "+v"(o16));
                }
            }
            STAMP(layer * 12 + 7);
            const float inv = rcp(o16[10]);
            const unsigned ob = WS_OB + layer * ACT_BYTES + (g * 8 + 2 * jq) * TKT;
            const unsigned vo = (r >> 4) * TKT + (r & 15) * 16;
            st128c(wsr, vo + hh * 256, ob + p * 1024 + h * 512, (u32x4){pk2(o16[0] * inv, o16[1] * inv), pk2(o16[2] * inv, o16[3] * inv), pk2(o16[4] * inv, o16[5] * inv), pk2(o16[6] * inv, o16[7] * inv)});
            if (hh == 0) st32c(wsr, vo, ob + (16 + (p >> 3)) * 1024 + ((p >> 1) & 3) * 256 + (p & 1) * 8 + h * 4, pk2(o16[8] * inv, o16[9] * inv));
        } else {
            u32x4 fx, fy; unsigned fe1;
            dual_softmax(q, lane, fx, fy, fe1);
            const u32x2 fe = {fe1, 0u};
            wait_flags(P.ws, FL_K + layer, (lane & 15) * 16 + p, tag);
            STAMP(layer * 12 + 6);
            if (wave < 6) {
                const unsigned c = wave * 32 + (lane & 31), hf = lane >> 5;
                const unsigned kb = WS_KVP + ((it * 16 + p) * 16) * 3072;
                u32x4 pv[8];
#pragma unroll
                for (int i = 0; i < 8; ++i) pv[i] = ld128c(wsr, c * 16 + hf * (8 * 3072), kb + i * 3072);
                float sm[8];
#pragma unroll
                for (int e = 0; e < 8; ++e) sm[e] = 0.f;
#pragma unroll
                for (int i = 0; i < 8; ++i) {
#pragma unroll
                    for (int w = 0; w < 4; ++w) { sm[2 * w] += f_of(pv[i][w] << 16); sm[2 * w + 1] += f_of(pv[i][w] & 0xffff0000u); }
                }
#pragma unroll
                for (int e = 0; e < 8; ++e) { const auto r = __builtin_amdgcn_permlane32_swap(u_of(sm[e]), u_of(sm[e]), false, false); sm[e] = f_of(r[0]) + f_of(r[1]); }
                if (hf == 0 && c < 190) lds_w128(L_KVS + c * 16, (u32x4){pk2(sm[0], sm[1]), pk2(sm[2], sm[3]), pk2(sm[4], sm[5]), pk2(sm[6], sm[7])});
            }
            if (first) {
#pragma unroll
                for (int s = 0; s < 18; ++s) wof[s] = ld128c(wsr, lane * 16 + s * 1024, wob);
            }
            STAMP(57 + it);
            BARRIER();
            const unsigned r1 = tk == 1 ? 16 : tk == 2 ? 17 : 18;
#pragma unroll
            for (int h = 0; h < 2; ++h) {
                const bool mine = (gq >> 1) == h;
                const u32x4 fm = mine ? fx : fy;
                const unsigned ch = (unsigned)(gq ^ (2 * h)) * 16u, ex = 64u + (unsigned)(gq & 1) * 4u;
                const unsigned ra = L_KVS + h * 1520 + tk * 80, rb = L_KVS + h * 1520 + r1 * 80;
                const unsigned ea = lds_r32(ra + ex), eb = lds_r32(rb + ex);
                f32x4 o0 = mfma32(lds_r128(ra + ch), fm, Z4), o1 = mfma32(lds_r128(rb + ch), fm, Z4);
                o0 = mfma16((u32x2){mine ? ea : 0u, 0u}, fe, o0);
                o1 = mfma16((u32x2){mine ? eb : 0u, 0u}, fe, o1);
                oa[h][0] = o0; oa[h][1] = o1;
            }
            STAMP(layer * 12 + 7);
            const float ia = rcp(oa[0][1][0] + 1e-6f), ib = rcp(oa[1][1][0] + 1e-6f);
            const unsigned ob = WS_OB + layer * ACT_BYTES + (g * 8 + wave) * TKT;
            st128c(wsr, lane * 16, ob + p * 1024, (u32x4){pk2(oa[0][0][0] * ia, oa[0][0][1] * ia), pk2(oa[0][0][2] * ia, oa[0][0][3] * ia),
                                                          pk2(oa[1][0][0] * ib, oa[1][0][1] * ib), pk2(oa[1][0][2] * ib, oa[1][0][3] * ib)});
            if (gq == 0) st64c(wsr, tk * 16, ob + (16 + (p >> 3)) * 1024 + ((p >> 1) & 3) * 256 + (p & 1) * 8,
                               (u32x2){pk2(oa[0][1][1] * ia, oa[0][1][2] * ia), pk2(oa[1][1][1] * ib, oa[1][1][2] * ib)});
        }
    }
    {
        const int lane = fresh(lane_in), gq = lane >> 4, tk = lane & 15;
        {
            DRAIN();
            BARRIER();
            if (wave == 0 && lane == 0) set_flag(P.ws, FL_O + layer, cu, tag);
            STAMP(59 + layer);
        }
        const unsigned char* nimg = P.ws + nimo + lane * 16;
        u32x4 wox[18];
        if (wave < 2) {
            const unsigned wb = WS_WOI + ((ISB ? 36 : 0) + 9 * cg + 8) * TKT;
#pragma unroll
            for (int s = 0; s < 18; ++s) wox[s] = ld128c(wsr, lane * 16 + s * 1024, wb);
        }
        wait_flags(P.ws, FL_O + layer, g * 16 + (lane & 15), tag);
        STAMP(layer * 12 + 8);
        if (first && blockIdx.x == 0 && wave == 0 && lane == 0) __hip_atomic_store((GAS unsigned*)P.ws, tag, RLX_AGENT);
        {
            const unsigned ob = WS_OB + layer * ACT_BYTES + (g * 8 + 2 * tp) * TKT;
            u32x4 ov[5];
#pragma unroll
            for (int i = 0; i < 5; ++i) if (i < 4 || wave < 4) ov[i] = ld128c(wsr, lane * 16 + i * 8192, ob + wave * 1024);
#pragma unroll
            for (int i = 0; i < 5; ++i) if (i < 4 || wave < 4) lds_w128(L_OST + (wave + 8 * i) * 1024 + lane * 16, ov[i]);
        }

        BARRIER();
        STAMP(layer * 12 + 9);
        {
            f32x4 c0 = res[0], c1 = res[1], c2 = res[2];
            const unsigned lo = L_OST + lane * 16;
            if (wave < 2) {
                u32x4 ox[18];
#pragma unroll
                for (int s = 0; s < 18; ++s) ox[s] = lds_r128(lo + wave * TKT + s * 1024);
                SB();
#pragma unroll
                for (int s = 0; s < 18; ++s) c2 = mfma32(wox[s], ox[s], c2);
            }
            SB();
            u32x4 of[4][2];
#pragma unroll
            for (int s = 0; s < 3; ++s) { of[s][0] = lds_r128(lo + s * 1024); of[s][1] = lds_r128(lo + TKT + s * 1024); }
#pragma unroll
            for (int s = 0; s < 18; ++s) {
                SB();
                c0 = mfma32(wof[s], of[s & 3][0], c0);
                SB();
                if (s + 3 < 18) of[(s + 3) & 3][0] = lds_r128(lo + (s + 3) * 1024);
                SB();
                c1 = mfma32(wof[s], of[s & 3][1], c1);
                SB();
                if (s + 3 < 18) of[(s + 3) & 3][1] = lds_r128(lo + TKT + (s + 3) * 1024);
                if ((s & 1) == 0 && s < 10 && !last && (s < 8 || wave < 4)) dma1k(nimg + (54 + wave + 4 * s) * 1024, (54 + wave + 4 * s) * 1024);
            }
            res[0] = c0; res[1] = c1; res[2] = c2;
            STAMP(layer * 12 + 10);
            const auto s0 = __builtin_amdgcn_permlane16_swap(pk2(c0[0], c0[1]), pk2(c1[0], c1[1]), false, false);
            const auto s1 = __builtin_amdgcn_permlane16_swap(pk2(c0[2], c0[3]), pk2(c1[2], c1[3]), false, false);
            const u32x4 v = {s0[0], s1[0], s0[1], s1[1]};
            const u32x2 v2 = {pk2(c2[0], c2[1]), pk2(c2[2], c2[3])};
            const int ct = 9 * cg + wave, cx = 9 * cg + 8;
            const int tx = 2 * tp + wave;
            if (!last) {
                const unsigned xb = WS_XB + layer * ACT_BYTES + g * 8 * TKT;
                st128c(wsr, (gq & 1) * TKT + ((gq >> 1) * 16 + tk) * 16, xb + 2 * tp * TKT + (ct >> 1) * 1024 + (ct & 1) * 512, v);
                if (wave < 2) st64c(wsr, ((gq >> 1) * 16 + tk) * 16 + (gq & 1) * 8, xb + tx * TKT + (cx >> 1) * 1024 + (cx & 1) * 512, v2);
                DRAIN();
                BARRIER();
                if (wave == 0 && lane == 0) set_flag(P.ws, FL_X + layer, cu, tag);

            } else {
                const int tt = 2 * tp + (gq & 1);
                *(u32x4*)(P.out + (size_t)(g * 128 + tt * 16 + tk) * DM + 16 * ct + 8 * (gq >> 1)) = v;
                if (wave < 2) *(u32x2*)(P.out + (size_t)(g * 128 + tx * 16 + tk) * DM + 16 * cx + 4 * gq) = v2;
            }
        }
        STAMP(layer * 12 + 11);
    }
    if (ISB) {
#pragma unroll
        for (int s = 0; s < 18; ++s) asm volatile("" : "=v"(xf[s]));
    }
}

__global__ void __launch_bounds__(512, 2) fused_kernel(Params P) {
    const int tid = threadIdx.x, lane = tid & 63, wave = __builtin_amdgcn_readfirstlane(tid >> 6);
    const int b = blockIdx.x, g = (b & 7) * 2 + (b >> 7), p = (b >> 3) & 15;
    const rsrc_t wsr = mk_rsrc(P.ws, WS_END);
    STAMP(0);
    u32x4 xf[18];
    f32x4 res[3];
    Ctx C; C.wave = wave; C.g = g; C.p = p; C.cu = g * 16 + p;
    {
        const int gq = lane >> 4, tk = lane & 15;
        const f32x4 Z4 = {0.f, 0.f, 0.f, 0.f};
        unsigned phv, epv;
        asm volatile("global_load_dword %0, %1, %2 sc1" : "=v"(epv) : "v"(0u), "s"(P.ws) : "memory");
        asm volatile("global_load_dword %0, %1, %2" : "=v"(phv) : "v"((unsigned)(p * 324 + (tid < 324 ? tid : 0)) * 4u), "s"(P.wphi) : "memory");
        u32x4 raw[6];
#pragma unroll
        for (int i = 0; i < 6; ++i) asm volatile("" : "=v"(raw[i]));
        const bool xa = g < 2, xb = g == 2 || g == 3;
        const int sx = xa ? 16 + g : 14 + g;
        const int u0 = b + 256 * (wave - 6), u1 = b + 512;
        const int m0 = u0 >= 324, r0 = u0 - 324 * m0, cp0 = r0 / 18, s0 = r0 - 18 * cp0;
        const int r1 = u1 - 324, cp1 = r1 / 18, s1 = r1 - 18 * cp1;
        const bool two = wave == 6 && u1 < 648;
        if (wave < 6) {
            const unsigned wv = ((unsigned)tk * NQKV + (unsigned)(p * 36 + (wave >> 1) * 576 + (wave & 1) * 18 + (gq == 1 ? 8 : gq == 2 ? 10 : 0))) * 2u;
            ld128_raw(raw[0], P.wqkvA, wv + (unsigned)(2 * g) * (16 * NQKV * 2)); ld128_raw(raw[1], P.wqkvA, wv + (unsigned)(2 * g + 1) * (16 * NQKV * 2));
            ld128_raw(raw[2], P.wqkvB, wv + (unsigned)(2 * g) * (16 * NQKV * 2)); ld128_raw(raw[3], P.wqkvB, wv + (unsigned)(2 * g + 1) * (16 * NQKV * 2));
            if (g < 4) { const u16* w = xa ? P.wqkvA : P.wqkvB;
                ld128_raw(raw[4], w, wv + (unsigned)(2 * sx) * (16 * NQKV * 2)); ld128_raw(raw[5], w, wv + (unsigned)(2 * sx + 1) * (16 * NQKV * 2)); }
        } else {
            const u16* w = m0 ? P.woB : P.woA;
#pragma unroll
            for (int hf = 0; hf < 2; ++hf) ld128_raw(raw[hf], w, (unsigned)(wo_krow(m0, s0, tk >> 2, 4 * hf + (tk & 3)) * DM + 32 * cp0 + 8 * gq) * 2u);
            if (two) {
#pragma unroll
                for (int hf = 0; hf < 2; ++hf) ld128_raw(raw[2 + hf], P.woB, (unsigned)(wo_krow(1, s1, tk >> 2, 4 * hf + (tk & 3)) * DM + 32 * cp1 + 8 * gq) * 2u);
            }
        }
        if (tid < 252) { const int row = tid / 7, j = tid - 7 * row; lds_w32(L_PHI + row * 64 + (j < 3 ? 32 + 4 * j : 36 + 4 * j), 0u); }
        if (wave == 7 && lane < 48) lds_w128(L_PST + (lane >> 4) * 7168 + 4096 + (tk >> 2) * 256 + (4 * (tk & 3) + 2) * 16, (u32x4){0x3F803F80u, 0x3F803F80u, 0x3F803F80u, 0x3F803F80u});
        asm volatile("s_waitcnt vmcnt(0)" : "+v"(epv), "+v"(phv), "+v"(raw[0]), "+v"(raw[1]), "+v"(raw[2]), "+v"(raw[3]), "+v"(raw[4]), "+v"(raw[5]) :: "memory");
        C.tag = __builtin_amdgcn_readfirstlane(epv) * 0x9E3779B1u + 0x7F4A7C15u;
        STAMP(48);
        if (tid < 324) {
            const int e = 2 * tid, hh = e >= 324, r = e - 324 * hh, d = r / 18, f = r - 18 * d;
            const unsigned a = L_PHI + (unsigned)(hh * 18 + f) * 64u + (unsigned)(d < 16 ? d : d + 6) * 2u;
            *(LAS u16*)((LAS unsigned char*)smem + a) = (u16)phv; *(LAS u16*)((LAS unsigned char*)smem + a + 64) = (u16)(phv >> 16);
        }
        if (wave >= 6) {
            const u32x4 e0 = onehot8((tk >> 3) == gq ? (tk & 7) : -2), e1 = onehot8((tk >> 3) + 2 == gq ? (tk & 7) : -2);
#pragma unroll
            for (int i = 0; i < 2; ++i) {
                if (i == 0 || two) {
                    const f32x4 a0 = mfma32(raw[2 * i], e0, Z4), a1 = mfma32(raw[2 * i + 1], e0, Z4), c0 = mfma32(raw[2 * i], e1, Z4), c1 = mfma32(raw[2 * i + 1], e1, Z4);
                    const unsigned o = WS_WOI + (unsigned)(i == 0 ? (m0 * 36 + 2 * cp0) * 18 + s0 : (36 + 2 * cp1) * 18 + s1) * 1024u;
                    st128c(wsr, lane * 16, o, (u32x4){pk2(a0[0], a0[1]), pk2(a0[2], a0[3]), pk2(a1[0], a1[1]), pk2(a1[2], a1[3])});
                    st128c(wsr, lane * 16, o + TKT, (u32x4){pk2(c0[0], c0[1]), pk2(c0[2], c0[3]), pk2(c1[0], c1[1]), pk2(c1[2], c1[3])});
                }
            }
        }
        BARRIER();
        if (wave < 6) {
            const int wh = wave >> 1, h = wave & 1;
            const u32x4 im = sel_frag(gq, tk), il = sel_frag(gq, 16 + (tk & 1));
            u32x4 am = im, al = il;
            if (wave < 4) { am = lds_r128(L_PHI + (h * 18 + tk) * 64 + gq * 16); al = lds_r128(L_PHI + (h * 18 + 16 + (tk & 1)) * 64 + gq * 16); }
            const unsigned ko = L_PST + (gq >> 1) * 256 + (gq & 1) * 8;
            const unsigned dmA = ko + img_slot(wave < 4, wh, h, tk), dlA = ko + img_slot(wave < 4, wh, h, 16 + (tk & 1));
            const unsigned dmB = ko + img_slot(false, wh, h, tk), dlB = ko + img_slot(false, wh, h, 16 + (tk & 1));
#pragma unroll
            for (int i = 0; i < 6; ++i) {
                if (i < 4 || g < 4) {
                    const bool fold = i < 2 || (i >= 4 && xa);
                    const f32x4 cm = mfma32(raw[i], fold ? am : im, Z4), cl = mfma32(raw[i], fold ? al : il, Z4);
                    lds_w64((fold ? dmA : dmB) + (i >> 1) * 7168 + (i & 1) * 512, (u32x2){pk2(cm[0], cm[1]), pk2(cm[2], cm[3])});
                    if (tk < 2) lds_w64((fold ? dlA : dlB) + (i >> 1) * 7168 + (i & 1) * 512, (u32x2){pk2(cl[0], cl[1]), pk2(cl[2], cl[3])});
                }
            }
        }
        BARRIER();
#pragma unroll
        for (int i = 0; i < 3; ++i) {
            const int f = wave + 8 * i, u = f / 7, t = f - 7 * u;
            if (f < 14 || (f < 21 && g < 4)) {
                const unsigned o = ((u == 0 || (u == 2 && xa)) ? WS_IMA : WS_IMB) + p * IMG_BYTES + t * TKT + (u == 2 ? sx : g) * 1024;
                st128c(wsr, lane * 16, o, lds_r128(L_PST + f * 1024 + lane * 16));
            }
        }
        STAMP(49);
        {
            const unsigned char* xs = (const unsigned char*)P.x + ((size_t)(g * 128 + wave * 16 + (lane >> 3)) * DM + 8 * ((lane & 7) ^ (lane >> 3))) * 2;
#pragma unroll
            for (int i = 0; i < 16; ++i) dma1k(xs + (i & 1) * (8 * DM * 2) + (i >> 1) * 128, wave * 16384 + i * 1024);
        }
        asm volatile("s_waitcnt vmcnt(16)" ::: "memory");
        BARRIER();
        if (tid == 0) set_flag(P.ws, FL_W1, g * 16 + p, C.tag);
        {
            const rsrc_t rx = mk_rsrc(P.x, SEQ * DM * 2);
            const unsigned xo = ((unsigned)(g * 128 + wave * 16 + tk) * DM + 8u * gq) * 2u;
            xf[16] = ld128(rx, xo, 16 * 64); xf[17] = ld128(rx, xo, 17 * 64);
        }
        DRAIN();
        STAMP(50);
#pragma unroll
        for (int s = 0; s < 16; ++s) xf[s] = lds_r128(wave * 16384 + (s >> 1) * 2048 + (tk >> 3) * 1024 + (tk & 7) * 128 + (((4 * (s & 1) + gq) ^ (tk & 7)) * 16));
        wait_flags(P.ws, FL_W1, tk * 16 + p, C.tag);
        STAMP(51);
        BARRIER();
        const unsigned char* img = P.ws + WS_IMA + p * IMG_BYTES + lane * 16;
#pragma unroll 1
        for (int f = wave; f < 126; f += 8) dma1k(img + f * 1024, f * 1024);
        DRAIN();
        BARRIER();
    }
    STAMP(1);
#pragma unroll 1
    for (int it = 0; it < 2; ++it) {
        layer_fn<false>(P, wsr, lane, C, it, xf, res);
        layer_fn<true>(P, wsr, lane, C, it, xf, res);
    }
}

extern "C" void kernel_launch(void* const* d_in, const int* in_sizes, int n_in, void* d_out, int out_size, void* d_ws, size_t ws_size, hipStream_t stream) {
    Params P;
    P.x = (const u16*)d_in[0]; P.wqkvA = (const u16*)d_in[1]; P.wphi = (const u16*)d_in[2]; P.woA = (const u16*)d_in[3];
    P.wqkvB = (const u16*)d_in[5]; P.woB = (const u16*)d_in[6];
    P.out = (u16*)d_out; P.ws = (unsigned char*)d_ws;
    (void)hipFuncSetAttribute((const void*)fused_kernel, hipFuncAttributeMaxDynamicSharedMemorySize, LDS_BYTES);
    hipLaunchKernelGGL(fused_kernel, dim3(256), dim3(512), LDS_BYTES, stream, P);
}
```

```cpp
#include <hip/hip_runtime.h>
#include <stdint.h>
#define ODRAIN 0
#define XSLEEP 4
#define RSLEEP 6
#define NIT 2
#define ONEPOLL 0

typedef __attribute__((ext_vector_type(8))) __bf16 bf16x8;
typedef __attribute__((ext_vector_type(2))) __bf16 bf16x2;
typedef __attribute__((ext_vector_type(4))) short s16x4;
typedef __attribute__((ext_vector_type(4))) float f32x4;
typedef __attribute__((ext_vector_type(2))) float f32x2;
typedef __attribute__((ext_vector_type(16))) float f32x16;
typedef __attribute__((ext_vector_type(4))) unsigned u32x4;
typedef __attribute__((ext_vector_type(2))) unsigned u32x2;
typedef unsigned short u16;
#define LAS __attribute__((address_space(3)))
#define GAS __attribute__((address_space(1)))
#define RLX_AGENT __ATOMIC_RELAXED, __HIP_MEMORY_SCOPE_AGENT
#define DEVI __device__ __forceinline__

constexpr int SEQ = 2048, DM = 576, NQKV = 1728;
constexpr unsigned TKT = 18 * 1024;
constexpr unsigned ACT_BYTES = 128 * TKT;
constexpr unsigned IMG_BYTES = 7 * TKT;
constexpr unsigned WS_FLAGS = 4096;
constexpr unsigned FL_W1 = 0, FL_W2 = 1, FL_W4 = 2, FL_W5 = 3, FL_IA = 4, FL_IB = 6, FL_N = 8;
constexpr unsigned KVP_BYTES = 256 * 3072, KVH_BYTES = 256 * 20480, ST_BYTES = 128 * 2304, UNIT = 256 * 1024;
constexpr unsigned SETA = KVP_BYTES + 2 * ACT_BYTES + ST_BYTES;
constexpr unsigned N0 = 23, N1 = 40, N2 = 52;
constexpr unsigned WS_S0 = 524288, WS_S1 = WS_S0 + N0 * UNIT, WS_S2 = WS_S1 + N1 * UNIT;
static_assert(WS_FLAGS + FL_N * 256 * 128 <= WS_S0 && SETA <= N0 * UNIT && KVH_BYTES + 2 * ACT_BYTES + ST_BYTES <= N1 * UNIT && SETA + KVH_BYTES + ACT_BYTES <= N2 * UNIT, "map");
__host__ __device__ constexpr unsigned ws_k(int layer) { return layer == 0 ? WS_S0 : layer == 1 ? WS_S1 : layer == 2 ? WS_S2 : WS_S2 + SETA; }
__host__ __device__ constexpr unsigned ws_o(int layer) { return ws_k(layer) + ((layer & 1) ? KVH_BYTES : KVP_BYTES); }
constexpr unsigned WS_IMA = WS_S2 + N2 * UNIT;
constexpr unsigned WS_IMB = WS_IMA + 16 * IMG_BYTES;
constexpr unsigned WS_WOI = WS_IMB + 16 * IMG_BYTES;
constexpr unsigned WS_END = WS_WOI + 2 * 36 * TKT;
constexpr unsigned WS_DBG = WS_END;
constexpr unsigned L_OST = 5 * TKT;
constexpr unsigned L_STG = IMG_BYTES;
constexpr unsigned L_KT = L_STG - 40960;
constexpr unsigned L_QS = L_STG + 20480;
constexpr unsigned L_PK = L_STG, L_VV = L_STG + 20480;
constexpr unsigned L_KVS = L_STG;
constexpr unsigned L_CS = L_STG + 32768;
constexpr unsigned L_PST = 147456;
constexpr unsigned LDS_BYTES = 163840;
constexpr unsigned SPIN_CAP = 1u << 17;
constexpr float LOG2E = 1.4426950408889634f;
constexpr float NEGBIG = -1e30f;

struct Params {
    const u16* x; const u16* wqkvA; const u16* wphi; const u16* woA; const u16* wqkvB; const u16* woB;
    u16* out; unsigned char* ws;
};

DEVI f32x4 mfma32(u32x4 a, u32x4 b, f32x4 c) { return __builtin_amdgcn_mfma_f32_16x16x32_bf16(__builtin_bit_cast(bf16x8, a), __builtin_bit_cast(bf16x8, b), c, 0, 0, 0); }
DEVI f32x16 mfmaL(u32x4 a, u32x4 b, f32x16 c) { return __builtin_amdgcn_mfma_f32_32x32x16_bf16(__builtin_bit_cast(bf16x8, a), __builtin_bit_cast(bf16x8, b), c, 0, 0, 0); }
DEVI u32x4 w1(unsigned a) { return (u32x4){a, 0u, 0u, 0u}; }
DEVI unsigned pkv(f32x2 f) { return __builtin_bit_cast(unsigned, __builtin_convertvector(f, bf16x2)); }
DEVI unsigned pk2(float lo, float hi) { return pkv((f32x2){lo, hi}); }
DEVI float dot2(unsigned a, unsigned b, float c) { return __builtin_amdgcn_fdot2_f32_bf16(__builtin_bit_cast(bf16x2, a), __builtin_bit_cast(bf16x2, b), c, false); }
DEVI float ex2(float x) { return __builtin_amdgcn_exp2f(x); }
DEVI float rcp(float x) { return __builtin_amdgcn_rcpf(x); }
DEVI float f_of(unsigned u) { return __builtin_bit_cast(float, u); }
DEVI unsigned u_of(float f) { return __builtin_bit_cast(unsigned, f); }
DEVI f32x2 fma2(f32x2 a, f32x2 b, f32x2 c) { return __builtin_elementwise_fma(a, b, c); }
DEVI f32x2 sp2(float v) { return (f32x2){v, v}; }
DEVI f32x2 sw16(float a, float b) { const auto r = __builtin_amdgcn_permlane16_swap(u_of(a), u_of(b), false, false); return (f32x2){f_of(r[0]), f_of(r[1])}; }
DEVI f32x2 sw32(float a, float b) { const auto r = __builtin_amdgcn_permlane32_swap(u_of(a), u_of(b), false, false); return (f32x2){f_of(r[0]), f_of(r[1])}; }
DEVI float vmax(f32x2 v) { float r; asm("v_max_f32 %0, %1, %2" : "=v"(r) : "v"(v.x), "v"(v.y)); return r; }
DEVI float vmaxn(f32x2 v) { float r; asm("v_max_f32_e64 %0, -%1, -%2" : "=v"(r) : "v"(v.x), "v"(v.y)); return r; }
DEVI float max3f(float a, float b, float c) { return __builtin_fmaxf(__builtin_fmaxf(a, b), c); }
DEVI float min3f(float a, float b, float c) { return __builtin_fminf(__builtin_fminf(a, b), c); }
DEVI u32x4 unit_pair(unsigned a0, unsigned a1, unsigned b0, unsigned b1) {
    const auto s = __builtin_amdgcn_permlane16_swap(a0, b0, false, false);
    const auto t = __builtin_amdgcn_permlane16_swap(a1, b1, false, false);
    return (u32x4){s[0], t[0], s[1], t[1]};
}

#define BARRIER() asm volatile("s_waitcnt lgkmcnt(0)\n\ts_barrier" ::: "memory")
#define DRAIN() asm volatile("s_waitcnt vmcnt(0)" ::: "memory")
#define SB() __builtin_amdgcn_sched_barrier(0)

extern __shared__ __attribute__((aligned(16))) unsigned char smem[];
DEVI u32x4 lds_r128(unsigned off) { return *(const LAS u32x4*)((LAS unsigned char*)smem + off); }
DEVI f32x4 lds_rf4(unsigned off) { return *(const LAS f32x4*)((LAS unsigned char*)smem + off); }
DEVI unsigned lds_r32(unsigned off) { return *(const LAS unsigned*)((LAS unsigned char*)smem + off); }
DEVI void lds_w128(unsigned off, u32x4 v) { *(LAS u32x4*)((LAS unsigned char*)smem + off) = v; }
DEVI void lds_w64(unsigned off, u32x2 v) { *(LAS u32x2*)((LAS unsigned char*)smem + off) = v; }
DEVI void lds_w32(unsigned off, unsigned v) { *(LAS unsigned*)((LAS unsigned char*)smem + off) = v; }
DEVI void lds_w16(unsigned off, unsigned v) { *(LAS u16*)((LAS unsigned char*)smem + off) = (u16)v; }
DEVI u32x2 lds_tr(unsigned off) { return __builtin_bit_cast(u32x2, __builtin_amdgcn_ds_read_tr16_b64_v4i16((LAS s16x4*)((LAS unsigned char*)smem + off))); }

typedef __amdgpu_buffer_rsrc_t rsrc_t;
DEVI rsrc_t mk_rsrc(const void* p, unsigned bytes) { return __builtin_amdgcn_make_buffer_rsrc((void*)p, 0, bytes, 0x00020000); }
DEVI u32x4 ld128c(rsrc_t r, unsigned voff, unsigned soff) { return __builtin_amdgcn_raw_buffer_load_b128(r, voff, soff, 16); }
DEVI void st128c(rsrc_t r, unsigned voff, unsigned soff, u32x4 v) {
    __builtin_amdgcn_raw_buffer_store_b128(v, r, voff, soff, 16);
    asm volatile("s_nop 1" :: "v"(v) : "memory");
}
DEVI void st64c(rsrc_t r, unsigned voff, unsigned soff, u32x2 v) { __builtin_amdgcn_raw_buffer_store_b64(v, r, voff, soff, 16); }
DEVI void st32c(rsrc_t r, unsigned voff, unsigned soff, unsigned v) { __builtin_amdgcn_raw_buffer_store_b32(v, r, voff, soff, 16); }
DEVI void dma1k(const void* sbase, unsigned voff, unsigned lds_dst) {
    unsigned keep;
    asm volatile("s_mov_b32 %0, m0\n\ts_mov_b32 m0, %3\n\ts_nop 0\n\tglobal_load_lds_dwordx4 %1, %2 sc1\n\ts_mov_b32 m0, %0" : "=&s"(keep) : "v"(voff), "s"(sbase), "s"(lds_dst) : "memory");
}
DEVI void dma1k_in(const void* sbase, unsigned voff, unsigned lds_dst) {
    unsigned keep;
    asm volatile("s_mov_b32 %0, m0\n\ts_mov_b32 m0, %3\n\ts_nop 0\n\tglobal_load_lds_dwordx4 %1, %2\n\ts_mov_b32 m0, %0" : "=&s"(keep) : "v"(voff), "s"(sbase), "s"(lds_dst) : "memory");
}
DEVI void ld128_raw(u32x4& d, const void* base, unsigned voff) { asm volatile("global_load_dwordx4 %0, %1, %2" : "=v"(d) : "v"(voff), "s"(base) : "memory"); }
DEVI void ld64_raw(u32x2& d, const void* base, unsigned voff) { asm volatile("global_load_dwordx2 %0, %1, %2" : "=v"(d) : "v"(voff), "s"(base) : "memory"); }
DEVI void wait_flags(const unsigned char* base, unsigned voff, unsigned tag) {
    const GAS unsigned* f = (const GAS unsigned*)(base + voff);
#pragma unroll 1
    for (unsigned sp = 0; sp < SPIN_CAP; ++sp) {
        const unsigned v = __hip_atomic_load(f, RLX_AGENT);
        if (__all(v == tag)) break;
        __builtin_amdgcn_s_sleep(1);
    }
    asm volatile("" ::: "memory");
}
DEVI unsigned umax3(unsigned a, unsigned b, unsigned c) { return __builtin_elementwise_max(__builtin_elementwise_max(a, b), c); }
DEVI unsigned umax4(unsigned m, u32x4 v) { return umax3(umax3(m, v.x, v.y), v.z, v.w); }
DEVI unsigned ok1(unsigned v) { return __builtin_elementwise_min(v, 0xFFFFFFFEu); }
DEVI u32x2 ok2(u32x2 v) { return (u32x2){ok1(v.x), ok1(v.y)}; }
DEVI u32x4 ok4(u32x4 v) { return (u32x4){ok1(v.x), ok1(v.y), ok1(v.z), ok1(v.w)}; }
DEVI void arm_unit(rsrc_t r, unsigned l16, unsigned unit, unsigned cu) { st128c(r, l16, WS_S0 + (unit * 256 + cu) * 1024, (u32x4){~0u, ~0u, ~0u, ~0u}); }
DEVI void wait_all256(const unsigned char* base, int lane, unsigned tag) {
    const GAS unsigned* f = (const GAS unsigned*)(base + lane * 128);
#pragma unroll 1
    for (unsigned sp = 0; sp < SPIN_CAP; ++sp) {
        const unsigned v0 = __hip_atomic_load(f, RLX_AGENT), v1 = __hip_atomic_load(f + 2048, RLX_AGENT), v2 = __hip_atomic_load(f + 4096, RLX_AGENT), v3 = __hip_atomic_load(f + 6144, RLX_AGENT);
        if (__all(v0 == tag && v1 == tag && v2 == tag && v3 == tag)) break;
        __builtin_amdgcn_s_sleep(1);
    }
    asm volatile("" ::: "memory");
}
#define RCOUNT(site) do { } while (0)
#define RETRY(site, LOADS, MAXEXPR) do { _Pragma("unroll 1") for (unsigned sp_ = 0; sp_ < SPIN_CAP; ++sp_) { if (!__any((MAXEXPR) == 0xFFFFFFFFu)) break; \
    RCOUNT(site); __builtin_amdgcn_s_sleep(RSLEEP); asm volatile("" ::: "memory"); LOADS; } } while (0)
DEVI const unsigned char* flag_at(const unsigned char* ws, unsigned kind, unsigned idx) { return ws + WS_FLAGS + (kind * 256 + idx) * 128; }
DEVI void set_flag(unsigned char* ws, unsigned kind, unsigned idx, unsigned tag) {
    __hip_atomic_store((GAS unsigned*)(ws + WS_FLAGS + (kind * 256 + idx) * 128), tag, RLX_AGENT);
}

#define STAMP(i) do { } while (0)

DEVI u32x4 onehot8(int j) {
    const unsigned v = (j & 1) ? 0x3F800000u : 0x3F80u; const int w = j >> 1;
    return (u32x4){w == 0 ? v : 0u, w == 1 ? v : 0u, w == 2 ? v : 0u, w == 3 ? v : 0u};
}
DEVI int wo_krow(int mat, int s, int gq, int j) {
    const int a = 36 * s + (mat ? 18 * (gq >> 1) + 8 * (j >> 2) + 4 * (gq & 1) : 18 * (j >> 2) + 4 * gq) + (j & 3);
    const int c = 36 * (8 * (s - 16) + 2 * gq + (j >> 2)) + 18 * ((j >> 1) & 1) + 16 + (j & 1);
    return s < 16 ? a : c;
}
DEVI int fresh(int v) { asm volatile("" : "+v"(v)); return v; }
DEVI int sfresh(int v) { asm volatile("" : "+s"(v)); return v; }

template <int T0, int NT, int S0, int S1, bool SSQ, int D, class F>
DEVI void gemm_img(f32x4 (&acc)[7], u32x4 (&xf)[18], const unsigned (&lb)[3], f32x4& sq, F side) {
    u32x4 a[D][NT];
#pragma unroll
    for (int s = S0; s < S0 + D; ++s)
#pragma unroll
        for (int t = 0; t < NT; ++t) a[s % D][t] = lds_r128(lb[s / 6] + (T0 + t) * TKT + (s % 6) * 1024);
    SB();
#pragma unroll
    for (int s = S0; s < S1; ++s) {
        side(s);
        SB();
        if (SSQ) { sq = mfma32(xf[s], xf[s], sq); SB(); }
#pragma unroll
        for (int t = 0; t < NT; ++t) {
            acc[T0 + t] = mfma32(a[s % D][t], xf[s], acc[T0 + t]);
            SB();
            if (s + D < S1) { a[s % D][t] = lds_r128(lb[(s + D) / 6] + (T0 + t) * TKT + ((s + D) % 6) * 1024); SB(); }
        }
    }
}
DEVI void colsum_tile(int t, unsigned csb, int lane) {
    f32x4 c = {0.f, 0.f, 0.f, 0.f}; const u32x4 ones = {0x3F803F80u, 0x3F803F80u, 0x3F803F80u, 0x3F803F80u};
    u32x4 a[18];
#pragma unroll
    for (int s = 0; s < 18; ++s) a[s] = lds_r128(t * TKT + s * 1024 + lane * 16);
    SB();
#pragma unroll
    for (int s = 0; s < 18; ++s) c = mfma32(a[s], ones, c);
    asm volatile("s_nop 7" : "+v"(c));
    if ((lane & 15) == 0) lds_w128(csb + ((lane >> 4) * 28 + 4 * t) * 4, __builtin_bit_cast(u32x4, c));
}
DEVI void dual_softmax(const float (&t)[9], float ks, bool lo, u32x4& fa, u32x4& fb, unsigned& fe) {
    const float zA = lo ? t[8] : t[0], zB = lo ? t[4] : t[8];
    const float mA = max3f(max3f(t[0], t[1], t[2]), t[3], zA), nA = min3f(min3f(t[0], t[1], t[2]), t[3], zA);
    const float mB = max3f(max3f(t[4], t[5], t[6]), t[7], zB), nB = min3f(min3f(t[4], t[5], t[6]), t[7], zB);
    const float T = -ks * vmax(sw32(vmax(sw16(mA, mB)), vmaxn(sw16(nA, nB))));
    const f32x2 u = sw16(T, T);
    const f32x2 sa = sw32(u.x, u.x), sb = sw32(u.y, u.y);
    const f32x2 s8 = lo ? sa : sb, k2 = {ks, -ks};
    f32x2 e[9];
#pragma unroll
    for (int i = 0; i < 9; ++i) { const f32x2 z = fma2(sp2(t[i]), k2, i < 4 ? sa : i < 8 ? sb : s8); e[i] = (f32x2){ex2(z.x), ex2(z.y)}; }
    const float wl = lo ? 1.f : 0.f;
    const f32x2 sA = fma2(e[8], sp2(wl), (e[0] + e[1]) + (e[2] + e[3])), sB = fma2(e[8], sp2(1.f - wl), (e[4] + e[5]) + (e[6] + e[7]));
    const f32x2 p = sw16(sA.x, sB.x), q = sw16(sA.y, sB.y), w = sw32(p.x + p.y, q.x + q.y);
    const float R = rcp(w.x + w.y);
    const f32x2 v = sw16(R, R);
    const f32x2 ra = sw32(v.x, v.x), rb = sw32(v.y, v.y), r8 = lo ? ra : rb;
    fa = (u32x4){pkv(e[0] * ra), pkv(e[1] * ra), pkv(e[2] * ra), pkv(e[3] * ra)};
    fb = (u32x4){pkv(e[4] * rb), pkv(e[5] * rb), pkv(e[6] * rb), pkv(e[7] * rb)};
    fe = pkv(e[8] * r8);
}

DEVI f32x16 att_scores(u32x4 km, u32x4 kl, u32x4 q0, u32x4 q1, f32x16 c) { return mfmaL(kl, q1, mfmaL(km, q0, c)); }
DEVI u32x4 att_p(const f32x16& s, int b) { return (u32x4){pk2(ex2(s[b]), ex2(s[b + 1])), pk2(ex2(s[b + 2]), ex2(s[b + 3])), pk2(ex2(s[b + 4]), ex2(s[b + 5])), pk2(ex2(s[b + 6]), ex2(s[b + 7]))}; }
DEVI void att_accum(const f32x16& s, const u32x2 (&v)[4], f32x16& o) {
    const u32x4 p0 = {pk2(ex2(s[0]), ex2(s[1])), pk2(ex2(s[2]), ex2(s[3])), pk2(ex2(s[4]), ex2(s[5])), pk2(ex2(s[6]), ex2(s[7]))};
    o = mfmaL((u32x4){v[0].x, v[0].y, v[1].x, v[1].y}, p0, o);
    const u32x4 p1 = {pk2(ex2(s[8]), ex2(s[9])), pk2(ex2(s[10]), ex2(s[11])), pk2(ex2(s[12]), ex2(s[13])), pk2(ex2(s[14]), ex2(s[15]))};
    o = mfmaL((u32x4){v[2].x, v[2].y, v[3].x, v[3].y}, p1, o);
}
DEVI void att_vload(u32x2 (&v)[4], unsigned va) {
#pragma unroll
    for (int e = 0; e < 4; ++e) v[e] = lds_tr(va + e * 384);
}
DEVI float max16(const f32x16& s, float m) {
#pragma unroll
    for (int v = 0; v < 16; v += 2) m = max3f(m, s[v], s[v + 1]);
    return m;
}
DEVI f32x16 splat16(float v) { f32x16 r;
#pragma unroll
    for (int i = 0; i < 16; ++i) r[i] = v;
    return r; }

struct Ctx { int wave, g, p; unsigned tag; };

template <bool ISB>
DEVI void layer_fn(const Params& P, const rsrc_t wsr, const int lane_in, const Ctx& C, const int it, u32x4 (&xf)[18], f32x4 (&res)[3], f32x4 (&acc)[7], float& ssq) {
    const int wave = sfresh(C.wave), g = sfresh(C.g), p = sfresh(C.p), cu = g * 16 + p;
    const unsigned tag = C.tag;
    const int layer = 2 * it + (ISB ? 1 : 0);
    const int tp = (p >> 1) & 3, cg = (p >> 3) * 2 + (p & 1);
    const f32x4 Z4 = {0.f, 0.f, 0.f, 0.f};
    constexpr unsigned csb = L_CS + (ISB ? 448 : 0);
    const bool first = !ISB && it == 0, last = ISB && it == 1;
    u32x4 wof[18];
    float mean, rstd;
    const unsigned wob = WS_WOI + ((ISB ? 36 : 0) + 9 * cg + wave) * TKT;
    const unsigned w1k = wave * 1024;
    const unsigned kbase = ISB ? (it ? ws_k(3) : ws_k(1)) : (it ? ws_k(2) : ws_k(0)), obase = kbase + (ISB ? KVH_BYTES : KVP_BYTES);
    const int rot = first ? 0 : p % 3;
    const unsigned ro0 = rot * 6144, ro1 = rot == 2 ? 0 : ro0 + 6144, ro2 = rot ? ro0 - 6144 : 12288;
    const unsigned char* nimg = P.ws + (ISB ? WS_IMA : WS_IMB) + p * IMG_BYTES + w1k;
    {
        const int lane = fresh(lane_in), gq = lane >> 4, tk = lane & 15;
        const unsigned l16 = lane * 16;
        const unsigned lb[3] = {l16 + ro0, l16 + ro1, l16 + ro2};
        if (!first) {
#pragma unroll
            for (int t = 0; t < 5; ++t) acc[t] = Z4;
            const unsigned xprev = (ISB ? (it ? ws_o(2) : ws_o(0)) : ws_o(1)) + ACT_BYTES;
            const unsigned xb = xprev + (g * 8 + wave) * TKT, sb = xprev + ACT_BYTES + (g * 8 + wave) * 2304;
            unsigned st[9];
            auto xload = [&](int s0, int s1) {
#pragma unroll
                for (int s = s0; s < s1; ++s) xf[s] = ld128c(wsr, l16 + (s % 6) * 1024, xb + (s < 6 ? ro0 : s < 12 ? ro1 : ro2));
            };
            auto xmax = [&](int s0, int s1) { unsigned m = 0u;
#pragma unroll
                for (int s = s0; s < s1; ++s) m = umax4(m, xf[s]);
                return m; };
            auto sload = [&]() {
#pragma unroll
                for (int j = 0; j < 9; ++j) st[j] = __builtin_amdgcn_raw_buffer_load_b32(wsr, lane * 4 + j * 256, sb, 16);
            };
            asm volatile("s_waitcnt vmcnt(2)" ::: "memory");
            BARRIER();
            if (ISB && it == 0) { if (wave < 5) colsum_tile(wave, csb, lane); }
            else __builtin_amdgcn_s_sleep(XSLEEP);
            sload();
            RETRY(layer * 8 + 3, sload(), umax3(umax3(umax3(umax3(st[0], st[1], st[2]), st[3], st[4]), st[5], st[6]), st[7], st[8]));
            STAMP(layer * 12 + 2);
            xload(0, 6);
            ssq = ((f_of(st[0]) + f_of(st[1])) + (f_of(st[2]) + f_of(st[3]))) + ((f_of(st[4]) + f_of(st[5])) + (f_of(st[6]) + f_of(st[7]))) + f_of(st[8]);
            if (ISB && it == 0) BARRIER();
            const unsigned char* cimg = P.ws + (ISB ? WS_IMB : WS_IMA) + p * IMG_BYTES + w1k;
            f32x4 dummy;
            gemm_img<0, 5, 0, 18, false, 2>(acc, xf, lb, dummy, [&](int s) {
                if (s == 0) RETRY(layer * 8, xload(0, 3), xmax(0, 3));
                if (s == 0) xload(6, 9);
                if (s == 1 || s == 2) xload(7 + 2 * s, 9 + 2 * s);
                if (s == 3) RETRY(layer * 8 + 1, xload(3, 9), xmax(3, 9));
                if (s == 3 || s == 4) xload(7 + 2 * s, 9 + 2 * s);
                if (s == 5) xload(17, 18);
                if (s == 9) RETRY(layer * 8 + 2, xload(9, 18), xmax(9, 18));
                if (s >= 9 && s < 14 && (s < 13 || wave < 4)) dma1k(cimg + (90 + 8 * (s - 9)) * 1024, l16, w1k + (90 + 8 * (s - 9)) * 1024);
                if (ISB && it == 0 && s >= 14 && s < 17) arm_unit(wsr, l16, 63 + (s - 14) * 8 + wave, cu);
                if (!ISB && s >= 14 && (s < 17 || wave < 4)) arm_unit(wsr, l16, 87 + (s - 14) * 8 + wave, cu);
            });
        }
        f32x4 cs[5];
#pragma unroll
        for (int t = 0; t < 5; ++t) cs[t] = lds_rf4(csb + gq * 112 + 16 * t);
        acc[5] = Z4; acc[6] = Z4;
        STAMP(layer * 12 + 3);
        const f32x2 r1 = sw16(ssq, ssq); const float h1 = r1.x + r1.y; const f32x2 r2 = sw32(h1, h1);
        asm volatile("s_nop 7" : "+v"(acc[4]));
        mean = acc[4][2] * (1.f / DM);
        rstd = __builtin_amdgcn_rsqf(__builtin_fmaxf((r2.x + r2.y) * (1.f / DM) - mean * mean, 0.f) + 1e-5f);
        const int hx = gq >> 1, od = gq & 1;
        f32x2 y[9];
        {
            const f32x2 nm = sp2(-mean);
#pragma unroll
            for (int t = 0; t < 5; ++t) {
                y[2 * t] = fma2(nm, (f32x2){cs[t][0], cs[t][1]}, (f32x2){acc[t][0], acc[t][1]});
                if (t < 4) y[2 * t + 1] = fma2(nm, (f32x2){cs[t][2], cs[t][3]}, (f32x2){acc[t][2], acc[t][3]});
            }
        }
        const u32x4 vm = unit_pair(pk2(y[4].y * rstd, y[5].x * rstd), pk2(y[5].y * rstd, y[6].x * rstd), pk2(y[6].y * rstd, y[7].x * rstd), pk2(y[7].y * rstd, y[8].x * rstd));
        const float v8 = y[8].y * rstd;
        const unsigned vl = pkv(sw16(v8, v8));
        if (ISB) {
            const f32x2 r2 = sp2(rstd);
            const u32x4 km = unit_pair(pkv(y[0] * r2), pkv(y[1] * r2), pkv(y[2] * r2), pkv(y[3] * r2));
            const float k8 = y[4].x * rstd;
            const u32x4 lr = {ok1(pkv(sw16(k8, k8))), 0u, ok1(vl), 0x3F80u};
            const unsigned tb = (wave >> 1) * 5120 + (wave & 1) * 256, tc = (wave >> 1) * 5120 + (wave & 1) * 768 + 1024;
            const unsigned oc = (tk >> 3) * 384 + (tk & 7) * 16;
            const unsigned a0 = od * 2560 + hx * 512 + tk * 16, a1 = od * 2560 + hx * 128 + oc, a2 = hx * 2560 + 256 + (oc ^ 64);
            const unsigned hb = kbase + cu * 20480;
            st128c(wsr, a0, hb + tb, ok4(km)); st128c(wsr, a1, hb + tc, ok4(vm));
            lds_w128(L_STG + tb + a0, km); lds_w128(L_STG + tc + a1, vm);
            if (od == 0) { st128c(wsr, a2, hb + tc, lr); lds_w128(L_STG + tc + a2, lr); }
            asm volatile("s_waitcnt vmcnt(3)" ::: "memory");
            BARRIER();
            if (it == 0 && wave == 7 && lane == 0) set_flag(P.ws, FL_W4, cu, tag);
        } else {
            const float kz[9] = {y[0].x, y[0].y, y[1].x, y[1].y, y[2].x, y[2].y, y[3].x, y[3].y, y[4].x};
            u32x4 fa, fb; unsigned fe;
            dual_softmax(kz, rstd * LOG2E, gq < 2, fa, fb, fe);
            const unsigned pk = L_PK + wave * 1280 + tk * 80, vv = L_VV + wave * 768 + tk * 48;
            lds_w128(pk + gq * 16, fa); lds_w128(pk + gq * 16 + 10240, fb); lds_w32(pk + hx * 10240 + 64 + od * 4, fe);
            lds_w128(vv + od * 6144 + hx * 16, vm);
            lds_w64(vv + hx * 6144 + 32, (u32x2){vl, 0x3F80u});
            STAMP(53 + it);
            if (it == 1 && wave == 3) wait_all256(flag_at(P.ws, FL_W4, 0), lane, tag);
            DRAIN();
            BARRIER();
            if (wave == 7 && lane == 0) set_flag(P.ws, it ? FL_W5 : FL_W2, cu, tag);
            STAMP(55 + it);
            if (wave < 6) {
                const int h = wave >= 3, ft = wave - 3 * h;
                const unsigned trow = (unsigned)(lane >> 2), tcol = (unsigned)(lane & 3) * 8u;
                const unsigned pa = L_PK + h * 10240 + ft * 32 + trow * 80 + tcol, pb = L_VV + h * 6144 + trow * 48 + tcol;
                u32x2 ta[8], tb[8], td[8];
#pragma unroll
                for (int s = 0; s < 8; ++s) { ta[s] = lds_tr(pa + (16 * s) * 80); tb[s] = lds_tr(pb + (16 * s) * 48); td[s] = lds_tr(pb + (16 * s) * 48 + 32); }
                SB();
                f32x4 k0 = Z4, k1 = Z4;
#pragma unroll
                for (int s = 0; s < 4; ++s) {
                    const u32x4 af = {ta[2 * s].x, ta[2 * s].y, ta[2 * s + 1].x, ta[2 * s + 1].y};
                    k0 = mfma32(af, (u32x4){tb[2 * s].x, tb[2 * s].y, tb[2 * s + 1].x, tb[2 * s + 1].y}, k0);
                    k1 = mfma32(af, (u32x4){td[2 * s].x, td[2 * s].y, td[2 * s + 1].x, td[2 * s + 1].y}, k1);
                }
                const u32x2 s0 = ok2((u32x2){pk2(k0[0], k0[1]), pk2(k0[2], k0[3])}), s1 = ok2((u32x2){pk2(k1[0], k1[1]), pk2(k1[2], k1[3])});
                const unsigned kb = kbase + (p * 16 + g) * 3072 + h * 1536 + ft * 32;
                const unsigned so = gq * 8 + tk * 80;
                if (ft < 2 || gq < 2) {
                    st64c(wsr, so, kb, s0);
                    if (tk < 3) st64c(wsr, so, kb + 1280, s1);
                }
            }
        }
        STAMP(layer * 12 + 0 + (layer == 0 ? 52 : 0));
        if (it == 0 && wave >= 6) colsum_tile(wave - 1, csb, lane);
        STAMP(layer * 12 + 4);
    }
    {
        const int lane = fresh(lane_in), gq = lane >> 4, tk = lane & 15;
        const unsigned l16 = lane * 16;
        const unsigned lb[3] = {l16 + ro0, l16 + ro1, l16 + ro2};
        { f32x4 dummy; gemm_img<5, 2, 0, 18, false, 3>(acc, xf, lb, dummy, [&](int s) {
              if (!ISB) wof[s] = ld128c(wsr, l16 + s * 1024, wob); }); }
        if (it == 0 || !ISB) BARRIER();
        float q[9];
        {
            const unsigned ca = csb + gq * 112;
            const float c19 = f_of(lds_r32(ca + 76));
            const f32x4 c5 = lds_rf4(ca + 80), c6 = lds_rf4(ca + 96);
            q[0] = acc[4][3] - mean * c19;
#pragma unroll
            for (int i = 0; i < 4; ++i) { q[1 + i] = acc[5][i] - mean * c5[i]; q[5 + i] = acc[6][i] - mean * c6[i]; }
        }
        STAMP(layer * 12 + 5);
        if (ISB) {
            u32x4 hv[5];
            const int src = g - 2 + (wave >> 2);
            auto hload = [&]() {
                const unsigned hb = kbase + (src * 16 + p) * 20480 + (wave & 3) * 5120;
#pragma unroll
                for (int i = 0; i < 5; ++i) hv[i] = ld128c(wsr, l16 + i * 1024, hb);
            };
            if (src >= 0) {
                hload();
            } else {
#pragma unroll
                for (int i = 0; i < 5; ++i) hv[i] = (u32x4){0u, (i == 0 || (i == 2 && lane >= 32) || (i == 3 && lane < 32)) ? 0u : 0xF149u, 0u, 0u};
            }
            {
                const float qs = rstd * (0.23570226039551584f * LOG2E);
                const int hx = gq >> 1, od = gq & 1;
                const unsigned qb = L_QS + (wave >> 1) * 2304;
                lds_w128(qb + (wave & 1) * 256 + od * 1152 + hx * 512 + tk * 16, unit_pair(pk2(q[0] * qs, q[1] * qs), pk2(q[2] * qs, q[3] * qs), pk2(q[4] * qs, q[5] * qs), pk2(q[6] * qs, q[7] * qs)));
                const float q8 = q[8] * qs;
                lds_w32(qb + (wave & 1) * 64 + 1024 + hx * 1152 + tk * 4, pkv(sw16(q8, q8)));
            }
            const int h = wave & 1, jq = wave < 4 ? (wave >> 1) : 3 - ((wave - 4) >> 1);
            const int r = lane & 31, hh = lane >> 5, dl = r - 4 * hh;
            f32x16 msk;
#pragma unroll
            for (int v = 0; v < 16; ++v) msk[v] = ((v & 3) + 8 * (v >> 2) > dl) ? NEGBIG : 0.f;
            const unsigned kb = L_KT + h * 2560 + jq * 5120;
            const unsigned ka = kb + l16, la = kb + 1280 + (r >> 3) * 384 + ((r & 7) ^ 4) * 16;
            const unsigned k8 = 4 * hh + ((lane >> 2) & 3);
            const unsigned va = kb + 1024 + ((lane & 16) ? 256 + (k8 ^ 4) * 16 + 8 : (lane & 2) * 64 + k8 * 16 + (lane & 1) * 8);
            BARRIER();
            const u32x4 q0 = lds_r128(L_QS + (jq * 2 + h) * 1152 + l16);
            const unsigned q1w = lds_r32(L_QS + (jq * 2 + h) * 1152 + 1024 + r * 4);
            u32x4 km = lds_r128(ka + 8 * 5120), kl = lds_r128(la + 8 * 5120);
            u32x2 vf[2][4];
            att_vload(vf[0], va + 8 * 5120);
            STAMP(layer * 12 + 1);
            const u32x4 q1 = {hh ? 0u : q1w, hh ? 0u : 0x3F80u, 0u, 0u};
            f32x16 sc[2], negm, o16 = splat16(0.f);
            sc[0] = att_scores(km, kl, q0, q1, msk);
            {
                const float m = max16(sc[0], NEGBIG);
                const float nm = -vmax(sw32(m, m));
                negm = splat16(nm);
                sc[0] += negm;
                msk = (splat16(NEGBIG) - msk) + negm;
            }
#pragma unroll
            for (int i = 8; i >= 0; --i) {
                if (i >= 5 && i == 8 - jq) {
                    RETRY(layer * 8 + 4, hload(), umax4(umax4(umax4(umax4(umax4(0u, hv[0]), hv[1]), hv[2]), hv[3]), hv[4]));
#pragma unroll
                    for (int j = 0; j < 5; ++j) lds_w128(L_KT + wave * 5120 + j * 1024 + l16, hv[j]);
                    BARRIER();
                    STAMP(layer * 12 + 6);
                }
                SB();
                if (i > 0) { km = lds_r128(ka + (i - 1) * 5120); kl = lds_r128(la + (i - 1) * 5120); att_vload(vf[(i - 1) & 1], va + (i - 1) * 5120); }
                wof[16 - 2 * i] = ld128c(wsr, l16 + (16 - 2 * i) * 1024, wob); wof[17 - 2 * i] = ld128c(wsr, l16 + (17 - 2 * i) * 1024, wob);
                SB();
                const u32x4 p0 = att_p(sc[i & 1], 0);
                SB();
                if (i > 0) sc[(i - 1) & 1] = att_scores(km, kl, q0, q1, i == 1 ? msk : negm);
                o16 = mfmaL((u32x4){vf[i & 1][0].x, vf[i & 1][0].y, vf[i & 1][1].x, vf[i & 1][1].y}, p0, o16);
                SB();
                const u32x4 p1 = att_p(sc[i & 1], 8);
                if (i < 7 && !last) {
                    if (i > 0 || wave < 6) dma1k(nimg + 8 * (6 - i) * 1024, l16, w1k + 8 * (6 - i) * 1024);
                }
                SB();
                o16 = mfmaL((u32x4){vf[i & 1][2].x, vf[i & 1][2].y, vf[i & 1][3].x, vf[i & 1][3].y}, p1, o16);
            }
            asm volatile("s_nop 7\n\ts_nop 7" : "+v"(o16));
            {
                float t = 0.f;
#pragma unroll
                for (int v = 0; v < 11; ++v) t += __builtin_fabsf(o16[v]);
                if (__any(!(t < 3.0e38f))) {
                    float m = NEGBIG;
#pragma unroll 1
                    for (int pass = 0; pass < 2; ++pass) {
                        o16 = splat16(0.f);
#pragma unroll 1
                        for (int i = 0; i <= 8; ++i) {
                            u32x2 v4[4]; att_vload(v4, va + i * 5120);
                            f32x16 s = att_scores(lds_r128(ka + i * 5120), lds_r128(la + i * 5120), q0, q1, splat16(pass ? -m : 0.f));
                            const int lim = i == 8 ? dl : 99, lim2 = i == 0 ? dl : -99;
#pragma unroll
                            for (int v = 0; v < 16; ++v) { const int cv = (v & 3) + 8 * (v >> 2); if (cv > lim || cv <= lim2) s[v] = NEGBIG; }
                            if (pass) att_accum(s, v4, o16); else m = max16(s, m);
                        }
                        if (!pass) m = vmax(sw32(m, m));
                    }
                    asm volatile("s_nop 7\n\ts_nop 7" : "+v"(o16));
                }
            }
            STAMP(layer * 12 + 7);
            const float inv = rcp(o16[10]);
            const unsigned ob = obase + (g * 8 + 2 * jq) * TKT;
            const unsigned vo = (r >> 4) * TKT + (r & 15) * 16;
            st128c(wsr, vo + hh * 256, ob + p * 1024 + h * 512, ok4((u32x4){pk2(o16[0] * inv, o16[1] * inv), pk2(o16[2] * inv, o16[3] * inv), pk2(o16[4] * inv, o16[5] * inv), pk2(o16[6] * inv, o16[7] * inv)}));
            if (hh == 0) st32c(wsr, vo, ob + (16 + (p >> 3)) * 1024 + ((p >> 1) & 3) * 256 + (p & 1) * 8 + h * 4, ok1(pk2(o16[8] * inv, o16[9] * inv)));
        } else {
            const unsigned c16 = (lane & 31) * 16;
            u32x4 pv[8];
            auto pload = [&]() {
                const unsigned kb = kbase + p * 16 * 3072 + (wave < 6 ? wave : 0) * 512;
#pragma unroll
                for (int i = 0; i < 8; ++i) pv[i] = ld128c(wsr, c16 + (lane >> 5) * (8 * 3072), kb + i * 3072);
            };
            if (wave < 6) pload();
            u32x4 fq[2]; unsigned fe;
            dual_softmax(q, rstd * LOG2E, gq < 2, fq[0], fq[1], fe);
            const unsigned r1 = tk == 1 ? 16 : tk == 2 ? 17 : 18;
            const unsigned ra = L_KVS + tk * 80, rb = L_KVS + r1 * 80, ex = 64u + (unsigned)(gq & 1) * 4u;
            if (wave < 6) {
                STAMP(layer * 12 + 6);
                const unsigned keep = (c16 == 496 && (wave == 2 || wave == 5)) ? 0u : ~0u;
                RETRY(layer * 8 + 4, pload(), keep & umax4(umax4(umax4(umax4(umax4(umax4(umax4(umax4(0u, pv[0]), pv[1]), pv[2]), pv[3]), pv[4]), pv[5]), pv[6]), pv[7]));
                float sm[8];
                const unsigned clo = (unsigned)sfresh(0x3F80), chi = (unsigned)sfresh(0x3F800000);
#pragma unroll
                for (int e = 0; e < 8; ++e) sm[e] = 0.f;
#pragma unroll
                for (int i = 0; i < 8; ++i) {
#pragma unroll
                    for (int w = 0; w < 4; ++w) { sm[2 * w] = dot2(clo, pv[i][w], sm[2 * w]); sm[2 * w + 1] = dot2(chi, pv[i][w], sm[2 * w + 1]); }
                }
                const f32x2 a0 = sw32(sm[0], sm[4]), a1 = sw32(sm[1], sm[5]), a2 = sw32(sm[2], sm[6]), a3 = sw32(sm[3], sm[7]);
                lds_w64(L_KVS + wave * 512 + c16 + (lane >> 5) * 8, (u32x2){pk2(a0.x + a0.y, a1.x + a1.y), pk2(a2.x + a2.y, a3.x + a3.y)});
            }
            else if (wave == 7) wait_all256(flag_at(P.ws, it ? FL_W5 : FL_W2, 0), lane, tag);
            STAMP(57 + it);
            BARRIER();
            f32x4 oa[2][2];
            {
                u32x4 am[2][2]; unsigned ae[2][2];
#pragma unroll
                for (int h = 0; h < 2; ++h) {
                    am[h][0] = lds_r128(ra + h * 1536 + gq * 16); am[h][1] = lds_r128(rb + h * 1536 + gq * 16);
                    ae[h][0] = lds_r32(ra + h * 1536 + ex); ae[h][1] = lds_r32(rb + h * 1536 + ex);
                }
                SB();
#pragma unroll
                for (int h = 0; h < 2; ++h) { oa[h][0] = mfma32(am[h][0], fq[h], Z4); oa[h][1] = mfma32(am[h][1], fq[h], Z4); }
#pragma unroll
                for (int h = 0; h < 2; ++h) {
                    const u32x4 fh = w1((gq >> 1) == h ? fe : 0u);
                    oa[h][1] = mfma32(w1(ae[h][1]), fh, oa[h][1]); oa[h][0] = mfma32(w1(ae[h][0]), fh, oa[h][0]);
                }
            }
            STAMP(layer * 12 + 7);
            const float ia = rcp(oa[0][1][0] + 1e-6f), ib = rcp(oa[1][1][0] + 1e-6f);
            const unsigned ob = obase + (g * 8 + wave) * TKT;
            st128c(wsr, l16, ob + p * 1024, ok4((u32x4){pk2(oa[0][0][0] * ia, oa[0][0][1] * ia), pk2(oa[0][0][2] * ia, oa[0][0][3] * ia),
                                                        pk2(oa[1][0][0] * ib, oa[1][0][1] * ib), pk2(oa[1][0][2] * ib, oa[1][0][3] * ib)}));
            if (gq == 0) st64c(wsr, l16, ob + (16 + (p >> 3)) * 1024 + ((p >> 1) & 3) * 256 + (p & 1) * 8,
                               ok2((u32x2){pk2(oa[0][1][1] * ia, oa[0][1][2] * ia), pk2(oa[1][1][1] * ib, oa[1][1][2] * ib)}));
        }
    }
    {
        const int lane = fresh(lane_in), gq = lane >> 4, tk = lane & 15;
        const unsigned l16 = lane * 16;
        BARRIER();
        STAMP(59 + layer);
        u32x4 wox[18];
        if (wave >= 6) {
            const unsigned wb = WS_WOI + ((ISB ? 36 : 0) + 9 * cg + 8) * TKT;
#pragma unroll
            for (int s = 0; s < 18; ++s) wox[s] = ld128c(wsr, l16 + s * 1024, wb);
        } else {
            const unsigned ob = obase + (g * 8 + 2 * tp) * TKT + w1k;
            u32x4 ov[6];
            auto oload = [&]() {
#pragma unroll
                for (int i = 0; i < 6; ++i) ov[i] = ld128c(wsr, l16 + i * 6144, ob);
            };
            oload();
            RETRY(layer * 8 + 5, oload(), umax4(umax4(umax4(umax4(umax4(umax4(0u, ov[0]), ov[1]), ov[2]), ov[3]), ov[4]), ov[5]));
            STAMP(layer * 12 + 8);
#pragma unroll
            for (int i = 0; i < 6; ++i) lds_w128(L_OST + w1k + i * 6144 + l16, ov[i]);
        }
        if (first && blockIdx.x == 0 && wave == 0 && lane == 0) __hip_atomic_store((GAS unsigned*)P.ws, tag, RLX_AGENT);
        BARRIER();
        STAMP(layer * 12 + 9);
        {
            f32x4 c0 = res[0], c1 = res[1], c2 = res[2];
            const unsigned lo = L_OST + l16;
            if (wave >= 6) {
                u32x4 ox[18];
#pragma unroll
                for (int s = 0; s < 18; ++s) ox[s] = lds_r128(lo + (wave & 1) * TKT + s * 1024);
                SB();
#pragma unroll
                for (int s = 0; s < 18; ++s) c2 = mfma32(wox[s], ox[s], c2);
            }
            SB();
            u32x4 of[4][2];
#pragma unroll
            for (int s = 0; s < 3; ++s) { of[s][0] = lds_r128(lo + s * 1024); of[s][1] = lds_r128(lo + TKT + s * 1024); }
#pragma unroll
            for (int s = 0; s < 18; ++s) {
                SB();
                c0 = mfma32(wof[s], of[s & 3][0], c0);
                SB();
                if (s + 3 < 18) of[(s + 3) & 3][0] = lds_r128(lo + (s + 3) * 1024);
                SB();
                c1 = mfma32(wof[s], of[s & 3][1], c1);
                SB();
                if (s + 3 < 18) of[(s + 3) & 3][1] = lds_r128(lo + TKT + (s + 3) * 1024);
                if (ISB) { if ((s & 1) == 0 && s < 10 && !last && (s < 8 || wave < 4)) dma1k(nimg + (54 + 4 * s) * 1024, l16, w1k + (54 + 4 * s) * 1024); }
                else if (s < 12 && (s < 11 || wave < 2)) dma1k(nimg + 8 * s * 1024, l16, w1k + 8 * s * 1024);
            }
            res[0] = c0; res[1] = c1; res[2] = c2;
            STAMP(layer * 12 + 10);
            const u32x4 v = unit_pair(pk2(c0[0], c0[1]), pk2(c0[2], c0[3]), pk2(c1[0], c1[1]), pk2(c1[2], c1[3]));
            const u32x2 v2 = {pk2(c2[0], c2[1]), pk2(c2[2], c2[3])};
            const int ct = 9 * cg + wave, cx = 9 * cg + 8;
            const int tx = 2 * tp + (wave & 1);
            if (!last) {
                const unsigned xb = obase + ACT_BYTES + g * 8 * TKT, sb = obase + 2 * ACT_BYTES + g * 8 * 2304;
                const unsigned vo = (gq >> 1) * 256 + tk * 16;
                st128c(wsr, (gq & 1) * TKT + vo, xb + 2 * tp * TKT + ct * 512, ok4(v));
                const f32x2 a0 = {c0[0], c0[1]}, a1 = {c0[2], c0[3]}, b0 = {c1[0], c1[1]}, b1 = {c1[2], c1[3]};
                const f32x2 qa = fma2(a1, a1, a0 * a0), qb = fma2(b1, b1, b0 * b0);
                const f32x2 u = sw16(qa.x + qa.y, qb.x + qb.y); const float u1 = u.x + u.y; const f32x2 u2 = sw32(u1, u1);
                if (gq < 2) st32c(wsr, gq * 2304 + tk * 4, sb + 2 * tp * 2304 + ct * 64, ok1(u_of(u2.x + u2.y)));
                if (wave >= 6) {
                    st64c(wsr, vo + (gq & 1) * 8, xb + tx * TKT + cx * 512, ok2(v2));
                    const f32x2 d0 = {c2[0], c2[1]}, d1 = {c2[2], c2[3]}, qd = fma2(d1, d1, d0 * d0);
                    const float e0 = qd.x + qd.y; const f32x2 e1 = sw16(e0, e0); const float e2 = e1.x + e1.y; const f32x2 e3 = sw32(e2, e2);
                    if (gq == 0) st32c(wsr, tk * 4, sb + tx * 2304 + cx * 64, ok1(u_of(e3.x + e3.y)));
                }

            } else {
                const int tt = 2 * tp + (gq & 1);
                *(u32x4*)(P.out + (size_t)(g * 128 + tt * 16 + tk) * DM + 16 * ct + 8 * (gq >> 1)) = v;
                if (wave >= 6) *(u32x2*)(P.out + (size_t)(g * 128 + tx * 16 + tk) * DM + 16 * cx + 4 * gq) = v2;
            }
        }
        STAMP(layer * 12 + 11);
    }
    if (ISB) {
#pragma unroll
        for (int s = 0; s < 18; ++s) asm volatile("" : "=v"(xf[s]));
#pragma unroll
        for (int t = 0; t < 5; ++t) asm volatile("" : "=v"(acc[t]));
        asm volatile("" : "=v"(ssq));
    }
}

__global__ void __launch_bounds__(512, 2) fused_kernel(Params P) {
    const int tid = threadIdx.x, lane = tid & 63, wave = __builtin_amdgcn_readfirstlane(tid >> 6);
    const int b = blockIdx.x, g = ((b >> 1) & 3) * 4 + (b >> 6), p = (b & 1) * 8 + ((b >> 3) & 7);
    const rsrc_t wsr = mk_rsrc(P.ws, WS_END);
    STAMP(0);
    u32x4 xf[18];
    f32x4 res[3];
    f32x4 acc[7], sq;
    float ssq;
    Ctx C; C.wave = wave; C.g = g; C.p = p;
    {
        const int gq = lane >> 4, tk = lane & 15;
        const f32x4 Z4 = {0.f, 0.f, 0.f, 0.f};
        const unsigned epv = __hip_atomic_load((const GAS unsigned*)P.ws, RLX_AGENT);
        const u16* xs = P.x + (size_t)(g * 128) * DM;
        const bool isa = wave == 0;
        int ks = g;
        u32x4 raw[12];
        const u32x4 im = onehot8((tk >> 3) == gq ? (tk & 7) : -2);
        auto unit_loads = [&]() {
            const unsigned wv = ((unsigned)tk * NQKV + (unsigned)(gq == 1 ? 8 : gq == 2 ? 10 : 0)) * 2u;
            const u16* w = (isa ? P.wqkvA : P.wqkvB) + p * 36 + ks * (32 * NQKV);
#pragma unroll
            for (int i = 0; i < 12; ++i) ld128_raw(raw[i], w + (i & 1) * (16 * NQKV) + (i >> 2) * 576 + ((i >> 1) & 1) * 18, wv);
        };
        auto unit_build = [&](const u32x4 (&bm)[2], const u32x4 (&bl)[2], const u32x4& ix, unsigned pb, unsigned tag) {
            if (lane < 16) lds_w128(pb + 4096 + (tk >> 2) * 256 + (4 * (tk & 3) + 2) * 16, (u32x4){0x3F803F80u, 0x3F803F80u, 0x3F803F80u, 0x3F803F80u});
            const unsigned ko = pb + (gq >> 1) * 256 + (gq & 1) * 8;
            const unsigned km = ko + (tk >> 2) * 64, kx = ko + (tk & 1) * 64;
            u32x2 lf[12];
#pragma unroll
            for (int i = 0; i < 12; ++i) {
                const int wh = i >> 2, h = (i >> 1) & 1, sb = wh == 0 ? 19 : wh == 2 ? 9 : 0;
                const f32x4 cm = mfma32(raw[i], wh < 2 ? bm[h] : im, Z4), cl = mfma32(raw[i], wh < 2 ? bl[h] : ix, Z4);
                const unsigned sm = sb + 4 * h + (tk & 3);
                lds_w64(km + (sm >> 2) * 1024 + (sm & 3) * 16 + (i & 1) * 512, (u32x2){pk2(cm[0], cm[1]), pk2(cm[2], cm[3])});
                lf[i] = (u32x2){pk2(cl[0], cl[1]), pk2(cl[2], cl[3])};
            }
            if (tk < 2) {
#pragma unroll
                for (int i = 0; i < 12; ++i) { const int wh = i >> 2, h = (i >> 1) & 1, sl = (wh == 0 ? 19 : wh == 2 ? 9 : 0) + 8; lds_w64(kx + (sl >> 2) * 1024 + (8 * h + (sl & 3)) * 16 + (i & 1) * 512, lf[i]); }
            }
            const unsigned o = (isa ? WS_IMA : WS_IMB) + p * IMG_BYTES + ks * 1024;
#pragma unroll
            for (int t = 0; t < 7; ++t) st128c(wsr, lane * 16, o + t * TKT, lds_r128(pb + t * 1024 + lane * 16));
            DRAIN();
            if (lane == 0) set_flag(P.ws, isa ? FL_IA : FL_IB, p * 32 + ks, tag);
        };
#define RAW_WAIT() asm volatile("s_waitcnt vmcnt(0)" : "+v"(raw[0]), "+v"(raw[1]), "+v"(raw[2]), "+v"(raw[3]), "+v"(raw[4]), "+v"(raw[5]), \
                                "+v"(raw[6]), "+v"(raw[7]), "+v"(raw[8]), "+v"(raw[9]), "+v"(raw[10]), "+v"(raw[11]) :: "memory")
        const u32x4 ix = onehot8(gq == 2 ? 6 + (tk & 1) : -2);
        u32x4 bm[2] = {im, im}, bl[2] = {ix, ix};
        const bool more = wave < 2 && (g >> 1) == wave;
        if (wave < 2) {
            if (isa) {
                unsigned ph[2][2][4], pg[2][2][4];
                const unsigned d0 = (unsigned)(gq == 1 ? 8 : gq == 2 ? 10 : 0) * 36u;
                unit_loads();
#pragma unroll
                for (int h = 0; h < 2; ++h)
#pragma unroll
                    for (int e = 0; e < 2; ++e)
#pragma unroll
                        for (int j = 0; j < 4; ++j)
                            asm volatile("global_load_ushort %0, %2, %3 offset:%4\n\tglobal_load_ushort %1, %2, %3 offset:%5" : "=&v"(ph[h][e][j]), "=&v"(pg[h][e][j])
                                         : "v"(d0 + (e ? 32u + 2u * (tk & 1) : 2u * tk)), "s"(P.wphi + p * 648), "i"(h * 648 + j * 72), "i"(h * 648 + j * 72 + 36) : "memory");
                __builtin_amdgcn_s_barrier();
                STAMP(49);
                RAW_WAIT();
                const unsigned m0 = gq < 2 ? ~0u : 0u, m3 = gq < 3 ? ~0u : 0u;
#pragma unroll
                for (int h = 0; h < 2; ++h)
#pragma unroll
                    for (int e = 0; e < 2; ++e) {
                        asm volatile("" : "+v"(ph[h][e][0]), "+v"(ph[h][e][1]), "+v"(ph[h][e][2]), "+v"(ph[h][e][3]), "+v"(pg[h][e][0]), "+v"(pg[h][e][1]), "+v"(pg[h][e][2]), "+v"(pg[h][e][3]) :: "memory");
                        u32x4 v;
#pragma unroll
                        for (int j = 0; j < 4; ++j) v[j] = (ph[h][e][j] | (pg[h][e][j] << 16)) & (j < 3 ? m0 : m3);
                        if (e) bl[h] = v; else bm[h] = v;
                    }
            } else {
                __builtin_amdgcn_s_barrier();
                __builtin_amdgcn_s_sleep(20);
                unit_loads(); RAW_WAIT();
            }
            STAMP(48);
            const unsigned tag = __builtin_amdgcn_readfirstlane(epv) * 0x9E3779B1u + 0x7F4A7C15u;
            unit_build(bm, bl, ix, L_PST + wave * 7168, tag);
            if (more) {
                ks = 16 + (g & 1);
                const rsrc_t rw = mk_rsrc(isa ? P.wqkvA : P.wqkvB, DM * NQKV * 2);
                const unsigned wv = ((unsigned)tk * NQKV + (unsigned)(gq == 1 ? 8 : gq == 2 ? 10 : 0)) * 2u;
#pragma unroll
                for (int i = 0; i < 12; ++i) raw[i] = __builtin_amdgcn_raw_buffer_load_b128(rw, wv, (p * 36 + ks * (32 * NQKV) + (i & 1) * (16 * NQKV) + (i >> 2) * 576 + ((i >> 1) & 1) * 18) * 2, 0);
            }
            STAMP(63);
        } else {
            const int u = b + 256 * (wave - 2), m = u >= 324, r = u - 324 * m, cp = r / 18, s = r - 18 * cp;
            const unsigned xo = ((unsigned)(lane >> 3) * DM + 8u * ((lane & 7) ^ (lane >> 3))) * 2u;
            const unsigned w0 = (unsigned)(wo_krow(m, s, tk >> 2, tk & 3) * DM + 8 * gq) * 2u, w1o = (unsigned)(wo_krow(m, s, tk >> 2, 4 + (tk & 3)) * DM + 8 * gq) * 2u;
            const u32x4 e1 = onehot8((tk >> 3) + 2 == gq ? (tk & 7) : -2);
            const u16* xw = xs + ((wave & 1) * 8) * DM + ((wave - 2) >> 1) * 64;
            __builtin_amdgcn_s_barrier();
            const u16* ww = u < 648 ? (m ? P.woB : P.woA) + 32 * cp : P.woA;
            ld128_raw(raw[0], ww, u < 648 ? w0 : 0u); ld128_raw(raw[1], ww, u < 648 ? w1o : 0u);
#pragma unroll 1
            for (int t0 = 0; t0 < 8; ++t0) {
                const int tt = (t0 + p) & 7;
#pragma unroll
                for (int j = 0; j < 3; ++j) dma1k_in(xw + tt * 16 * DM + j * 192, xo, tt * TKT + (wave - 2 + 6 * j) * 1024);
            }
            STAMP(49);
            asm volatile("s_waitcnt vmcnt(24)" : "+v"(raw[0]), "+v"(raw[1]) :: "memory");
            const f32x4 a0 = mfma32(raw[0], im, Z4), a1 = mfma32(raw[1], im, Z4), c0 = mfma32(raw[0], e1, Z4), c1 = mfma32(raw[1], e1, Z4);
            const u32x4 o0 = {pk2(a0[0], a0[1]), pk2(a0[2], a0[3]), pk2(a1[0], a1[1]), pk2(a1[2], a1[3])}, o1 = {pk2(c0[0], c0[1]), pk2(c0[2], c0[3]), pk2(c1[0], c1[1]), pk2(c1[2], c1[3])};
            if (u < 648) {
                const unsigned o = WS_WOI + (unsigned)((m * 36 + 2 * cp) * 18 + s) * 1024u;
                st128c(wsr, lane * 16, o, o0); st128c(wsr, lane * 16, o + TKT, o1);
            }
            if (wave >= 5) arm_unit(wsr, lane * 16, wave - 5, b);
            STAMP(48);
        }
        if (!more) DRAIN();
        C.tag = __builtin_amdgcn_readfirstlane(epv) * 0x9E3779B1u + 0x7F4A7C15u;
        BARRIER();
        if (tid == 0) set_flag(P.ws, FL_W1, g * 16 + p, C.tag);
        STAMP(50);
        {
            const unsigned xa = (tk >> 3) * 1024 + (tk & 7) * 128;
#pragma unroll
            for (int s = 0; s < 18; ++s) xf[s] = lds_r128(wave * TKT + (s >> 1) * 2048 + xa + (((4 * (s & 1) + gq) ^ (tk & 7)) * 16));
            const int tp = (p >> 1) & 3, cg = (p >> 3) * 2 + (p & 1);
#pragma unroll
            for (int i = 0; i < 3; ++i) {
                const int tt = 2 * tp + (i < 2 ? i : (wave & 1)), ct = 9 * cg + (i < 2 ? wave : 8);
                const u32x2 r = *(const LAS u32x2*)((LAS unsigned char*)smem + tt * TKT + (ct >> 2) * 2048 + xa + (((2 * (ct & 3) + (gq >> 1)) ^ (tk & 7)) * 16) + (gq & 1) * 8);
                res[i] = (f32x4){f_of(r.x << 16), f_of(r.x & 0xffff0000u), f_of(r.y << 16), f_of(r.y & 0xffff0000u)};
            }
        }
        wait_flags(flag_at(P.ws, FL_IA, p * 32), tk * 128, C.tag);
        BARRIER();
        STAMP(51);
        const unsigned char* img = P.ws + WS_IMA + p * IMG_BYTES;
        unsigned fo[4];
#pragma unroll
        for (int i = 0; i < 4; ++i) { const int j = i * 8 + wave, jt = j / 6; fo[i] = (j < 30 ? jt * 12 + j : 60 + j) * 1024; }
        const unsigned st3 = wave < 6 ? 6144 : 2048;
        auto piece = [&](int c, int i) {
            unsigned f = c < 3 ? fo[i] + c * (i < 3 ? 6144 : st3) : (96 + i * 8 + wave) * 1024;
            if (c == 3 && i == 3) f = f < 125 * 1024 ? f : 125 * 1024;
            dma1k(img + f, lane * 16, f);
        };
#pragma unroll
        for (int i = 0; i < 8; ++i) piece(i >> 2, i & 3);
        STAMP(1);
        if (more) unit_build(bm, bl, ix, L_PST + wave * 7168, C.tag);
#pragma unroll
        for (int t = 0; t < 7; ++t) acc[t] = Z4;
        sq = Z4;
        const unsigned lb0[3] = {(unsigned)lane * 16, (unsigned)lane * 16 + 6144, (unsigned)lane * 16 + 12288};
        asm volatile("s_waitcnt vmcnt(4)" ::: "memory"); BARRIER();
        STAMP(2);
        gemm_img<0, 5, 0, 6, true, 2>(acc, xf, lb0, sq, [&](int s) { if (s < 4) arm_unit(wsr, lane * 16, 3 + s * 8 + wave, b); });
        STAMP(25);
        wait_flags(flag_at(P.ws, FL_IA, p * 32 + 16), (lane & 1) * 128, C.tag);
        BARRIER();
        STAMP(26);
        gemm_img<0, 5, 6, 12, true, 2>(acc, xf, lb0, sq, [&](int s) { if (s < 10) piece(2, s - 6); if (s >= 8) piece(3, s - 8); });
        STAMP(27);
        asm volatile("s_waitcnt vmcnt(2)" ::: "memory"); BARRIER();
        STAMP(28);
        if (wave == 7) wait_all256(flag_at(P.ws, FL_W1, 0), lane, C.tag);
        gemm_img<0, 5, 12, 18, true, 2>(acc, xf, lb0, sq, [&](int s) { if (s < 15 || (s == 15 && wave < 4)) arm_unit(wsr, lane * 16, 35 + (s - 12) * 8 + wave, b); });
        STAMP(29);
        if (wave < 5) colsum_tile(wave, L_CS, lane);
        if (wave == 6) wait_flags(flag_at(P.ws, FL_IB, p * 32), (lane < 18 ? lane : 0) * 128, C.tag);
        asm volatile("s_nop 7" : "+v"(sq));
        const float d0 = (tk & 1) ? sq[1] : sq[0], d1 = (tk & 1) ? sq[3] : sq[2], dg = (tk & 2) ? d1 : d0;
        ssq = gq == (tk >> 2) ? dg : 0.f;
        BARRIER();
    }
#pragma unroll 1
    for (int it = 0; it < NIT; ++it) {
        layer_fn<false>(P, wsr, lane, C, it, xf, res, acc, ssq);
        layer_fn<true>(P, wsr, lane, C, it, xf, res, acc, ssq);
    }
}

extern "C" void kernel_launch(void* const* d_in, const int* in_sizes, int n_in, void* d_out, int out_size, void* d_ws, size_t ws_size, hipStream_t stream) {
    Params P;
    P.x = (const u16*)d_in[0]; P.wqkvA = (const u16*)d_in[1]; P.wphi = (const u16*)d_in[2]; P.woA = (const u16*)d_in[3];
    P.wqkvB = (const u16*)d_in[5]; P.woB = (const u16*)d_in[6];
    P.out = (u16*)d_out; P.ws = (unsigned char*)d_ws;
    (void)hipFuncSetAttribute((const void*)fused_kernel, hipFuncAttributeMaxDynamicSharedMemorySize, LDS_BYTES);
    hipLaunchKernelGGL(fused_kernel, dim3(256), dim3(512), LDS_BYTES, stream, P);
}
```

```cpp
#include <hip/hip_runtime.h>
#include <stdint.h>
#define ODRAIN 0
#define DSLEEP 16
#define XSLEEP 4
#define RSLEEP 6
#define NIT 2
#define ONEPOLL 0

typedef __attribute__((ext_vector_type(8))) __bf16 bf16x8;
typedef __attribute__((ext_vector_type(2))) __bf16 bf16x2;
typedef __attribute__((ext_vector_type(4))) short s16x4;
typedef __attribute__((ext_vector_type(4))) float f32x4;
typedef __attribute__((ext_vector_type(2))) float f32x2;
typedef __attribute__((ext_vector_type(16))) float f32x16;
typedef __attribute__((ext_vector_type(4))) unsigned u32x4;
typedef __attribute__((ext_vector_type(2))) unsigned u32x2;
typedef unsigned short u16;
#define LAS __attribute__((address_space(3)))
#define GAS __attribute__((address_space(1)))
#define RLX_AGENT __ATOMIC_RELAXED, __HIP_MEMORY_SCOPE_AGENT
#define DEVI __device__ __forceinline__

constexpr int SEQ = 2048, DM = 576, NQKV = 1728;
constexpr unsigned TKT = 18 * 1024;
constexpr unsigned ACT_BYTES = 128 * TKT;
constexpr unsigned IMG_BYTES = 7 * TKT;
constexpr unsigned WS_FLAGS = 4096;
constexpr unsigned FL_W1 = 0, FL_W2 = 1, FL_W4 = 2, FL_W5 = 3, FL_IA = 4, FL_IB = 6, FL_N = 8;
constexpr unsigned KVP_BYTES = 256 * 3072, KVH_BYTES = 256 * 20480, ST_BYTES = 128 * 2304, UNIT = 256 * 1024;
constexpr unsigned SETA = KVP_BYTES + 2 * ACT_BYTES + ST_BYTES;
constexpr unsigned N0 = 23, N1 = 40, N2 = 52;
constexpr unsigned WS_S0 = 524288, WS_S1 = WS_S0 + N0 * UNIT, WS_S2 = WS_S1 + N1 * UNIT;
static_assert(WS_FLAGS + FL_N * 256 * 128 <= WS_S0 && SETA <= N0 * UNIT && KVH_BYTES + 2 * ACT_BYTES + ST_BYTES <= N1 * UNIT && SETA + KVH_BYTES + ACT_BYTES <= N2 * UNIT, "map");
__host__ __device__ constexpr unsigned ws_k(int layer) { return layer == 0 ? WS_S0 : layer == 1 ? WS_S1 : layer == 2 ? WS_S2 : WS_S2 + SETA; }
__host__ __device__ constexpr unsigned ws_o(int layer) { return ws_k(layer) + ((layer & 1) ? KVH_BYTES : KVP_BYTES); }
constexpr unsigned WS_IMA = WS_S2 + N2 * UNIT;
constexpr unsigned WS_IMB = WS_IMA + 16 * IMG_BYTES;
constexpr unsigned WS_WOI = WS_IMB + 16 * IMG_BYTES;
constexpr unsigned WS_END = WS_WOI + 2 * 36 * TKT;
constexpr unsigned WS_DBG = WS_END;
constexpr unsigned L_OST = 5 * TKT;
constexpr unsigned L_STG = IMG_BYTES;
constexpr unsigned L_KT = L_STG - 40960;
constexpr unsigned L_QS = L_STG + 20480;
constexpr unsigned L_PK = L_STG, L_VV = L_STG + 20480;
constexpr unsigned L_KVS = L_STG;
constexpr unsigned L_CS = L_STG + 32768;
constexpr unsigned L_PST = 147456;
constexpr unsigned LDS_BYTES = 163840;
constexpr unsigned CODE_BYTES = 42 * 1024;
constexpr unsigned SPIN_CAP = 1u << 17;
constexpr float LOG2E = 1.4426950408889634f;
constexpr float NEGBIG = -1e30f;

struct Params {
    const u16* x; const u16* wqkvA; const u16* wphi; const u16* woA; const u16* wqkvB; const u16* woB;
    u16* out; unsigned char* ws;
};

DEVI f32x4 mfma32(u32x4 a, u32x4 b, f32x4 c) { return __builtin_amdgcn_mfma_f32_16x16x32_bf16(__builtin_bit_cast(bf16x8, a), __builtin_bit_cast(bf16x8, b), c, 0, 0, 0); }
DEVI f32x16 mfmaL(u32x4 a, u32x4 b, f32x16 c) { return __builtin_amdgcn_mfma_f32_32x32x16_bf16(__builtin_bit_cast(bf16x8, a), __builtin_bit_cast(bf16x8, b), c, 0, 0, 0); }
DEVI u32x4 w1(unsigned a) { return (u32x4){a, 0u, 0u, 0u}; }
DEVI unsigned pkv(f32x2 f) { return __builtin_bit_cast(unsigned, __builtin_convertvector(f, bf16x2)); }
DEVI unsigned pk2(float lo, float hi) { return pkv((f32x2){lo, hi}); }
DEVI float dot2(unsigned a, unsigned b, float c) { return __builtin_amdgcn_fdot2_f32_bf16(__builtin_bit_cast(bf16x2, a), __builtin_bit_cast(bf16x2, b), c, false); }
DEVI float ex2(float x) { return __builtin_amdgcn_exp2f(x); }
DEVI float rcp(float x) { return __builtin_amdgcn_rcpf(x); }
DEVI float f_of(unsigned u) { return __builtin_bit_cast(float, u); }
DEVI unsigned u_of(float f) { return __builtin_bit_cast(unsigned, f); }
DEVI f32x2 fma2(f32x2 a, f32x2 b, f32x2 c) { return __builtin_elementwise_fma(a, b, c); }
DEVI f32x2 sp2(float v) { return (f32x2){v, v}; }
DEVI f32x2 sw16(float a, float b) { const auto r = __builtin_amdgcn_permlane16_swap(u_of(a), u_of(b), false, false); return (f32x2){f_of(r[0]), f_of(r[1])}; }
DEVI f32x2 sw32(float a, float b) { const auto r = __builtin_amdgcn_permlane32_swap(u_of(a), u_of(b), false, false); return (f32x2){f_of(r[0]), f_of(r[1])}; }
DEVI float vmax(f32x2 v) { float r; asm("v_max_f32 %0, %1, %2" : "=v"(r) : "v"(v.x), "v"(v.y)); return r; }
DEVI float vmaxn(f32x2 v) { float r; asm("v_max_f32_e64 %0, -%1, -%2" : "=v"(r) : "v"(v.x), "v"(v.y)); return r; }
DEVI float max3f(float a, float b, float c) { return __builtin_fmaxf(__builtin_fmaxf(a, b), c); }
DEVI float min3f(float a, float b, float c) { return __builtin_fminf(__builtin_fminf(a, b), c); }
DEVI u32x4 unit_pair(unsigned a0, unsigned a1, unsigned b0, unsigned b1) {
    const auto s = __builtin_amdgcn_permlane16_swap(a0, b0, false, false);
    const auto t = __builtin_amdgcn_permlane16_swap(a1, b1, false, false);
    return (u32x4){s[0], t[0], s[1], t[1]};
}

#define BARRIER() asm volatile("s_waitcnt lgkmcnt(0)\n\ts_barrier" ::: "memory")
#define DRAIN() asm volatile("s_waitcnt vmcnt(0)" ::: "memory")
#define SB() __builtin_amdgcn_sched_barrier(0)

extern __shared__ __attribute__((aligned(16))) unsigned char smem[];
DEVI u32x4 lds_r128(unsigned off) { return *(const LAS u32x4*)((LAS unsigned char*)smem + off); }
DEVI f32x4 lds_rf4(unsigned off) { return *(const LAS f32x4*)((LAS unsigned char*)smem + off); }
DEVI unsigned lds_r32(unsigned off) { return *(const LAS unsigned*)((LAS unsigned char*)smem + off); }
DEVI void lds_w128(unsigned off, u32x4 v) { *(LAS u32x4*)((LAS unsigned char*)smem + off) = v; }
DEVI void lds_w64(unsigned off, u32x2 v) { *(LAS u32x2*)((LAS unsigned char*)smem + off) = v; }
DEVI void lds_w32(unsigned off, unsigned v) { *(LAS unsigned*)((LAS unsigned char*)smem + off) = v; }
DEVI void lds_w16(unsigned off, unsigned v) { *(LAS u16*)((LAS unsigned char*)smem + off) = (u16)v; }
DEVI u32x2 lds_tr(unsigned off) { return __builtin_bit_cast(u32x2, __builtin_amdgcn_ds_read_tr16_b64_v4i16((LAS s16x4*)((LAS unsigned char*)smem + off))); }

typedef __amdgpu_buffer_rsrc_t rsrc_t;
DEVI rsrc_t mk_rsrc(const void* p, unsigned bytes) { return __builtin_amdgcn_make_buffer_rsrc((void*)p, 0, bytes, 0x00020000); }
DEVI u32x4 ld128c(rsrc_t r, unsigned voff, unsigned soff) { return __builtin_amdgcn_raw_buffer_load_b128(r, voff, soff, 16); }
DEVI void st128c(rsrc_t r, unsigned voff, unsigned soff, u32x4 v) {
    __builtin_amdgcn_raw_buffer_store_b128(v, r, voff, soff, 16);
    asm volatile("s_nop 1" :: "v"(v) : "memory");
}
DEVI void st64c(rsrc_t r, unsigned voff, unsigned soff, u32x2 v) { __builtin_amdgcn_raw_buffer_store_b64(v, r, voff, soff, 16); }
DEVI void st32c(rsrc_t r, unsigned voff, unsigned soff, unsigned v) { __builtin_amdgcn_raw_buffer_store_b32(v, r, voff, soff, 16); }
DEVI void dma1k(const void* sbase, unsigned voff, unsigned lds_dst) {
    unsigned keep;
    asm volatile("s_mov_b32 %0, m0\n\ts_mov_b32 m0, %3\n\ts_nop 0\n\tglobal_load_lds_dwordx4 %1, %2 sc1\n\ts_mov_b32 m0, %0" : "=&s"(keep) : "v"(voff), "s"(sbase), "s"(lds_dst) : "memory");
}
DEVI void dma1k_in(const void* sbase, unsigned voff, unsigned lds_dst) {
    unsigned keep;
    asm volatile("s_mov_b32 %0, m0\n\ts_mov_b32 m0, %3\n\ts_nop 0\n\tglobal_load_lds_dwordx4 %1, %2\n\ts_mov_b32 m0, %0" : "=&s"(keep) : "v"(voff), "s"(sbase), "s"(lds_dst) : "memory");
}
DEVI void ld128_raw(u32x4& d, const void* base, unsigned voff) { asm volatile("global_load_dwordx4 %0, %1, %2" : "=v"(d) : "v"(voff), "s"(base) : "memory"); }
DEVI void ld64_raw(u32x2& d, const void* base, unsigned voff) { asm volatile("global_load_dwordx2 %0, %1, %2" : "=v"(d) : "v"(voff), "s"(base) : "memory"); }
DEVI void wait_flags(const unsigned char* base, unsigned voff, unsigned tag) {
    const GAS unsigned* f = (const GAS unsigned*)(base + voff);
#pragma unroll 1
    for (unsigned sp = 0; sp < SPIN_CAP; ++sp) {
        const unsigned v = __hip_atomic_load(f, RLX_AGENT);
        if (__all(v == tag)) break;
        __builtin_amdgcn_s_sleep(1);
    }
    asm volatile("" ::: "memory");
}
DEVI unsigned umax3(unsigned a, unsigned b, unsigned c) { return __builtin_elementwise_max(__builtin_elementwise_max(a, b), c); }
DEVI unsigned umax4(unsigned m, u32x4 v) { return umax3(umax3(m, v.x, v.y), v.z, v.w); }
DEVI unsigned ok1(unsigned v) { return __builtin_elementwise_min(v, 0xFFFFFFFEu); }
DEVI u32x2 ok2(u32x2 v) { return (u32x2){ok1(v.x), ok1(v.y)}; }
DEVI u32x4 ok4(u32x4 v) { return (u32x4){ok1(v.x), ok1(v.y), ok1(v.z), ok1(v.w)}; }
DEVI void arm_unit(rsrc_t r, unsigned l16, unsigned unit, unsigned cu) { st128c(r, l16, WS_S0 + (unit * 256 + cu) * 1024, (u32x4){~0u, ~0u, ~0u, ~0u}); }
DEVI void wait_all256(const unsigned char* base, int lane, unsigned tag) {
    const GAS unsigned* f = (const GAS unsigned*)(base + lane * 128);
#pragma unroll 1
    for (unsigned sp = 0; sp < SPIN_CAP; ++sp) {
        const unsigned v0 = __hip_atomic_load(f, RLX_AGENT), v1 = __hip_atomic_load(f + 2048, RLX_AGENT), v2 = __hip_atomic_load(f + 4096, RLX_AGENT), v3 = __hip_atomic_load(f + 6144, RLX_AGENT);
        if (__all(v0 == tag && v1 == tag && v2 == tag && v3 == tag)) break;
        __builtin_amdgcn_s_sleep(1);
    }
    asm volatile("" ::: "memory");
}
#define RCOUNT(site) do { } while (0)
#define RETRY(site, LOADS, MAXEXPR) do { _Pragma("unroll 1") for (unsigned sp_ = 0; sp_ < SPIN_CAP; ++sp_) { if (!__any((MAXEXPR) == 0xFFFFFFFFu)) break; \
    RCOUNT(site); __builtin_amdgcn_s_sleep(RSLEEP); asm volatile("" ::: "memory"); LOADS; } } while (0)
DEVI const unsigned char* flag_at(const unsigned char* ws, unsigned kind, unsigned idx) { return ws + WS_FLAGS + (kind * 256 + idx) * 128; }
DEVI void set_flag(unsigned char* ws, unsigned kind, unsigned idx, unsigned tag) {
    __hip_atomic_store((GAS unsigned*)(ws + WS_FLAGS + (kind * 256 + idx) * 128), tag, RLX_AGENT);
}

#define STAMP(i) do { } while (0)

DEVI u32x4 onehot8(int j) {
    const unsigned v = (j & 1) ? 0x3F800000u : 0x3F80u; const int w = j >> 1;
    return (u32x4){w == 0 ? v : 0u, w == 1 ? v : 0u, w == 2 ? v : 0u, w == 3 ? v : 0u};
}
DEVI int wo_krow(int mat, int s, int gq, int j) {
    const int a = 36 * s + (mat ? 18 * (gq >> 1) + 8 * (j >> 2) + 4 * (gq & 1) : 18 * (j >> 2) + 4 * gq) + (j & 3);
    const int c = 36 * (8 * (s - 16) + 2 * gq + (j >> 2)) + 18 * ((j >> 1) & 1) + 16 + (j & 1);
    return s < 16 ? a : c;
}
DEVI int fresh(int v) { asm volatile("" : "+v"(v)); return v; }
DEVI int sfresh(int v) { asm volatile("" : "+s"(v)); return v; }

template <int T0, int NT, int S0, int S1, bool SSQ, int D, class F>
DEVI void gemm_img(f32x4 (&acc)[7], u32x4 (&xf)[18], const unsigned (&lb)[3], f32x4& sq, F side) {
    u32x4 a[D][NT];
#pragma unroll
    for (int s = S0; s < S0 + D; ++s)
#pragma unroll
        for (int t = 0; t < NT; ++t) a[s % D][t] = lds_r128(lb[s / 6] + (T0 + t) * TKT + (s % 6) * 1024);
    SB();
#pragma unroll
    for (int s = S0; s < S1; ++s) {
        side(s);
        SB();
        if (SSQ) { sq = mfma32(xf[s], xf[s], sq); SB(); }
#pragma unroll
        for (int t = 0; t < NT; ++t) {
            acc[T0 + t] = mfma32(a[s % D][t], xf[s], acc[T0 + t]);
            SB();
            if (s + D < S1) { a[s % D][t] = lds_r128(lb[(s + D) / 6] + (T0 + t) * TKT + ((s + D) % 6) * 1024); SB(); }
        }
    }
}
DEVI void colsum_tile(int t, unsigned csb, int lane) {
    f32x4 c = {0.f, 0.f, 0.f, 0.f}; const u32x4 ones = {0x3F803F80u, 0x3F803F80u, 0x3F803F80u, 0x3F803F80u};
    u32x4 a[18];
#pragma unroll
    for (int s = 0; s < 18; ++s) a[s] = lds_r128(t * TKT + s * 1024 + lane * 16);
    SB();
#pragma unroll
    for (int s = 0; s < 18; ++s) c = mfma32(a[s], ones, c);
    asm volatile("s_nop 7" : "+v"(c));
    if ((lane & 15) == 0) lds_w128(csb + ((lane >> 4) * 28 + 4 * t) * 4, __builtin_bit_cast(u32x4, c));
}
DEVI void dual_softmax(const float (&t)[9], float ks, bool lo, u32x4& fa, u32x4& fb, unsigned& fe) {
    const float zA = lo ? t[8] : t[0], zB = lo ? t[4] : t[8];
    const float mA = max3f(max3f(t[0], t[1], t[2]), t[3], zA), nA = min3f(min3f(t[0], t[1], t[2]), t[3], zA);
    const float mB = max3f(max3f(t[4], t[5], t[6]), t[7], zB), nB = min3f(min3f(t[4], t[5], t[6]), t[7], zB);
    const float T = -ks * vmax(sw32(vmax(sw16(mA, mB)), vmaxn(sw16(nA, nB))));
    const f32x2 u = sw16(T, T);
    const f32x2 sa = sw32(u.x, u.x), sb = sw32(u.y, u.y);
    const f32x2 s8 = lo ? sa : sb, k2 = {ks, -ks};
    f32x2 e[9];
#pragma unroll
    for (int i = 0; i < 9; ++i) { const f32x2 z = fma2(sp2(t[i]), k2, i < 4 ? sa : i < 8 ? sb : s8); e[i] = (f32x2){ex2(z.x), ex2(z.y)}; }
    const float wl = lo ? 1.f : 0.f;
    const f32x2 sA = fma2(e[8], sp2(wl), (e[0] + e[1]) + (e[2] + e[3])), sB = fma2(e[8], sp2(1.f - wl), (e[4] + e[5]) + (e[6] + e[7]));
    const f32x2 p = sw16(sA.x, sB.x), q = sw16(sA.y, sB.y), w = sw32(p.x + p.y, q.x + q.y);
    const float R = rcp(w.x + w.y);
    const f32x2 v = sw16(R, R);
    const f32x2 ra = sw32(v.x, v.x), rb = sw32(v.y, v.y), r8 = lo ? ra : rb;
    fa = (u32x4){pkv(e[0] * ra), pkv(e[1] * ra), pkv(e[2] * ra), pkv(e[3] * ra)};
    fb = (u32x4){pkv(e[4] * rb), pkv(e[5] * rb), pkv(e[6] * rb), pkv(e[7] * rb)};
    fe = pkv(e[8] * r8);
}

DEVI f32x16 att_scores(u32x4 km, u32x4 kl, u32x4 q0, u32x4 q1, f32x16 c) { return mfmaL(kl, q1, mfmaL(km, q0, c)); }
DEVI u32x4 att_p(const f32x16& s, int b) { return (u32x4){pk2(ex2(s[b]), ex2(s[b + 1])), pk2(ex2(s[b + 2]), ex2(s[b + 3])), pk2(ex2(s[b + 4]), ex2(s[b + 5])), pk2(ex2(s[b + 6]), ex2(s[b + 7]))}; }
DEVI void att_accum(const f32x16& s, const u32x2 (&v)[4], f32x16& o) {
    const u32x4 p0 = {pk2(ex2(s[0]), ex2(s[1])), pk2(ex2(s[2]), ex2(s[3])), pk2(ex2(s[4]), ex2(s[5])), pk2(ex2(s[6]), ex2(s[7]))};
    o = mfmaL((u32x4){v[0].x, v[0].y, v[1].x, v[1].y}, p0, o);
    const u32x4 p1 = {pk2(ex2(s[8]), ex2(s[9])), pk2(ex2(s[10]), ex2(s[11])), pk2(ex2(s[12]), ex2(s[13])), pk2(ex2(s[14]), ex2(s[15]))};
    o = mfmaL((u32x4){v[2].x, v[2].y, v[3].x, v[3].y}, p1, o);
}
DEVI void att_vload(u32x2 (&v)[4], unsigned va) {
#pragma unroll
    for (int e = 0; e < 4; ++e) v[e] = lds_tr(va + e * 384);
}
DEVI float max16(const f32x16& s, float m) {
#pragma unroll
    for (int v = 0; v < 16; v += 2) m = max3f(m, s[v], s[v + 1]);
    return m;
}
DEVI f32x16 splat16(float v) { f32x16 r;
#pragma unroll
    for (int i = 0; i < 16; ++i) r[i] = v;
    return r; }

struct Ctx { int wave, g, p; unsigned tag; };

template <bool ISB>
DEVI void layer_fn(const Params& P, const rsrc_t wsr, const int lane_in, const Ctx& C, const int it, u32x4 (&xf)[18], f32x4 (&res)[3], f32x4 (&acc)[7], float& ssq) {
    const int wave = sfresh(C.wave), g = sfresh(C.g), p = sfresh(C.p), cu = g * 16 + p;
    const unsigned tag = C.tag;
    const int layer = 2 * it + (ISB ? 1 : 0);
    const int tp = (p >> 1) & 3, cg = (p >> 3) * 2 + (p & 1);
    const f32x4 Z4 = {0.f, 0.f, 0.f, 0.f};
    constexpr unsigned csb = L_CS + (ISB ? 448 : 0);
    const bool first = !ISB && it == 0, last = ISB && it == 1;
    u32x4 wof[18];
    float mean, rstd;
    const unsigned wob = WS_WOI + ((ISB ? 36 : 0) + 9 * cg + wave) * TKT;
    const unsigned w1k = wave * 1024;
    const unsigned kbase = ISB ? (it ? ws_k(3) : ws_k(1)) : (it ? ws_k(2) : ws_k(0)), obase = kbase + (ISB ? KVH_BYTES : KVP_BYTES);
    const int rot = first ? 0 : p % 3;
    const unsigned ro0 = rot * 6144, ro1 = rot == 2 ? 0 : ro0 + 6144, ro2 = rot ? ro0 - 6144 : 12288;
    const unsigned char* nimg = P.ws + (ISB ? WS_IMA : WS_IMB) + p * IMG_BYTES + w1k;
    {
        const int lane = fresh(lane_in), gq = lane >> 4, tk = lane & 15;
        const unsigned l16 = lane * 16;
        const unsigned lb[3] = {l16 + ro0, l16 + ro1, l16 + ro2};
        if (!first) {
#pragma unroll
            for (int t = 0; t < 5; ++t) acc[t] = Z4;
            const unsigned xprev = (ISB ? (it ? ws_o(2) : ws_o(0)) : ws_o(1)) + ACT_BYTES;
            const unsigned xb = xprev + (g * 8 + wave) * TKT, sb = xprev + ACT_BYTES + (g * 8 + wave) * 2304;
            unsigned st[9];
            auto xload = [&](int s0, int s1) {
#pragma unroll
                for (int s = s0; s < s1; ++s) xf[s] = ld128c(wsr, l16 + (s % 6) * 1024, xb + (s < 6 ? ro0 : s < 12 ? ro1 : ro2));
            };
            auto xmax = [&](int s0, int s1) { unsigned m = 0u;
#pragma unroll
                for (int s = s0; s < s1; ++s) m = umax4(m, xf[s]);
                return m; };
            auto sload = [&]() {
#pragma unroll
                for (int j = 0; j < 9; ++j) st[j] = __builtin_amdgcn_raw_buffer_load_b32(wsr, lane * 4 + j * 256, sb, 16);
            };
            asm volatile("s_waitcnt vmcnt(2)" ::: "memory");
            BARRIER();
            if (ISB && it == 0) { if (wave < 5) colsum_tile(wave, csb, lane); }
            else __builtin_amdgcn_s_sleep(XSLEEP);
            sload(); xload(0, 3);
            RETRY(layer * 8 + 3, { sload(); xload(0, 3); }, umax3(umax3(umax3(umax3(umax3(xmax(0, 3), st[0], st[1]), st[2], st[3]), st[4], st[5]), st[6], st[7]), st[8], 0u));
            STAMP(layer * 12 + 2);
            xload(3, 9);
            ssq = ((f_of(st[0]) + f_of(st[1])) + (f_of(st[2]) + f_of(st[3]))) + ((f_of(st[4]) + f_of(st[5])) + (f_of(st[6]) + f_of(st[7]))) + f_of(st[8]);
            if (ISB && it == 0) BARRIER();
            const unsigned char* cimg = P.ws + (ISB ? WS_IMB : WS_IMA) + p * IMG_BYTES + w1k;
            f32x4 dummy;
            gemm_img<0, 5, 0, 18, false, 2>(acc, xf, lb, dummy, [&](int s) {
                if (s < 3) xload(9 + 2 * s, 11 + 2 * s);
                if (s == 3) RETRY(layer * 8 + 1, xload(3, 9), xmax(3, 9));
                if (s >= 3 && s < 6) xload(12 + s, 13 + s);
                if (s == 9) RETRY(layer * 8 + 2, xload(9, 18), xmax(9, 18));
                if (s >= 9 && s < 14 && (s < 13 || wave < 4)) dma1k(cimg + (90 + 8 * (s - 9)) * 1024, l16, w1k + (90 + 8 * (s - 9)) * 1024);
                if (!ISB && s >= 9) wof[s] = ld128c(wsr, l16 + s * 1024, wob);
                if (ISB && it == 0 && s >= 14 && s < 17) arm_unit(wsr, l16, 63 + (s - 14) * 8 + wave, cu);
                if (!ISB && s >= 14 && (s < 17 || wave < 4)) arm_unit(wsr, l16, 87 + (s - 14) * 8 + wave, cu);
            });
        }
        f32x4 cs[5];
#pragma unroll
        for (int t = 0; t < 5; ++t) cs[t] = lds_rf4(csb + gq * 112 + 16 * t);
        acc[5] = Z4; acc[6] = Z4;
        STAMP(layer * 12 + 3);
        const f32x2 r1 = sw16(ssq, ssq); const float h1 = r1.x + r1.y; const f32x2 r2 = sw32(h1, h1);
        asm volatile("s_nop 7" : "+v"(acc[4]));
        mean = acc[4][2] * (1.f / DM);
        rstd = __builtin_amdgcn_rsqf(__builtin_fmaxf((r2.x + r2.y) * (1.f / DM) - mean * mean, 0.f) + 1e-5f);
        const int hx = gq >> 1, od = gq & 1;
        f32x2 y[9];
        {
            const f32x2 nm = sp2(-mean);
#pragma unroll
            for (int t = 0; t < 5; ++t) {
                y[2 * t] = fma2(nm, (f32x2){cs[t][0], cs[t][1]}, (f32x2){acc[t][0], acc[t][1]});
                if (t < 4) y[2 * t + 1] = fma2(nm, (f32x2){cs[t][2], cs[t][3]}, (f32x2){acc[t][2], acc[t][3]});
            }
        }
        const u32x4 vm = unit_pair(pk2(y[4].y * rstd, y[5].x * rstd), pk2(y[5].y * rstd, y[6].x * rstd), pk2(y[6].y * rstd, y[7].x * rstd), pk2(y[7].y * rstd, y[8].x * rstd));
        const float v8 = y[8].y * rstd;
        const unsigned vl = pkv(sw16(v8, v8));
        if (ISB) {
            const f32x2 r2 = sp2(rstd);
            const u32x4 km = unit_pair(pkv(y[0] * r2), pkv(y[1] * r2), pkv(y[2] * r2), pkv(y[3] * r2));
            const float k8 = y[4].x * rstd;
            const u32x4 lr = {ok1(pkv(sw16(k8, k8))), 0u, ok1(vl), 0x3F80u};
            const unsigned tb = (wave >> 1) * 5120 + (wave & 1) * 256, tc = (wave >> 1) * 5120 + (wave & 1) * 768 + 1024;
            const unsigned oc = (tk >> 3) * 384 + (tk & 7) * 16;
            const unsigned a0 = od * 2560 + hx * 512 + tk * 16, a1 = od * 2560 + hx * 128 + oc, a2 = hx * 2560 + 256 + (oc ^ 64);
            const unsigned hb = kbase + cu * 20480;
            st128c(wsr, a0, hb + tb, ok4(km)); st128c(wsr, a1, hb + tc, ok4(vm));
            lds_w128(L_STG + tb + a0, km); lds_w128(L_STG + tc + a1, vm);
            if (od == 0) { st128c(wsr, a2, hb + tc, lr); lds_w128(L_STG + tc + a2, lr); }
            asm volatile("s_waitcnt vmcnt(3)" ::: "memory");
            BARRIER();
            if (it == 0 && wave == 7 && lane == 0) set_flag(P.ws, FL_W4, cu, tag);
        } else {
            const float kz[9] = {y[0].x, y[0].y, y[1].x, y[1].y, y[2].x, y[2].y, y[3].x, y[3].y, y[4].x};
            u32x4 fa, fb; unsigned fe;
            dual_softmax(kz, rstd * LOG2E, gq < 2, fa, fb, fe);
            const unsigned pk = L_PK + wave * 1280 + tk * 80, vv = L_VV + wave * 768 + tk * 48;
            lds_w128(pk + gq * 16, fa); lds_w128(pk + gq * 16 + 10240, fb); lds_w32(pk + hx * 10240 + 64 + od * 4, fe);
            lds_w128(vv + od * 6144 + hx * 16, vm);
            lds_w64(vv + hx * 6144 + 32, (u32x2){vl, 0x3F80u});
            STAMP(53 + it);
            if (first && wave == 3) wait_all256(flag_at(P.ws, FL_W1, 0), lane, tag);
            DRAIN();
            BARRIER();
            if (wave == 7 && lane == 0) set_flag(P.ws, it ? FL_W5 : FL_W2, cu, tag);
            STAMP(55 + it);
            if (wave < 6) {
                const int h = wave >= 3, ft = wave - 3 * h;
                const unsigned trow = (unsigned)(lane >> 2), tcol = (unsigned)(lane & 3) * 8u;
                const unsigned pa = L_PK + h * 10240 + ft * 32 + trow * 80 + tcol, pb = L_VV + h * 6144 + trow * 48 + tcol;
                u32x2 ta[8], tb[8], td[8];
#pragma unroll
                for (int s = 0; s < 8; ++s) { ta[s] = lds_tr(pa + (16 * s) * 80); tb[s] = lds_tr(pb + (16 * s) * 48); td[s] = lds_tr(pb + (16 * s) * 48 + 32); }
                SB();
                f32x4 k0 = Z4, k1 = Z4;
#pragma unroll
                for (int s = 0; s < 4; ++s) {
                    const u32x4 af = {ta[2 * s].x, ta[2 * s].y, ta[2 * s + 1].x, ta[2 * s + 1].y};
                    k0 = mfma32(af, (u32x4){tb[2 * s].x, tb[2 * s].y, tb[2 * s + 1].x, tb[2 * s + 1].y}, k0);
                    k1 = mfma32(af, (u32x4){td[2 * s].x, td[2 * s].y, td[2 * s + 1].x, td[2 * s + 1].y}, k1);
                }
                const u32x2 s0 = ok2((u32x2){pk2(k0[0], k0[1]), pk2(k0[2], k0[3])}), s1 = ok2((u32x2){pk2(k1[0], k1[1]), pk2(k1[2], k1[3])});
                const unsigned kb = kbase + (p * 16 + g) * 3072 + h * 1536 + ft * 32;
                const unsigned so = gq * 8 + tk * 80;
                if (ft < 2 || gq < 2) {
                    st64c(wsr, so, kb, s0);
                    if (tk < 3) st64c(wsr, so, kb + 1280, s1);
                }
            }
        }
        STAMP(layer * 12 + 0 + (layer == 0 ? 52 : 0));
        if (it == 0 && wave >= 6) colsum_tile(wave - 1, csb, lane);
        STAMP(layer * 12 + 4);
    }
    {
        const int lane = fresh(lane_in), gq = lane >> 4, tk = lane & 15;
        const unsigned l16 = lane * 16;
        const unsigned lb[3] = {l16 + ro0, l16 + ro1, l16 + ro2};
        { f32x4 dummy; gemm_img<5, 2, 0, 18, false, 3>(acc, xf, lb, dummy, [&](int s) {
              if (!ISB && (s & 1) == 0) wof[s >> 1] = ld128c(wsr, l16 + (s >> 1) * 1024, wob);
              if ((s & 1) && first) wof[9 + (s >> 1)] = ld128c(wsr, l16 + (9 + (s >> 1)) * 1024, wob); }); }
        if (it == 0 || !ISB) BARRIER();
        float q[9];
        {
            const unsigned ca = csb + gq * 112;
            const float c19 = f_of(lds_r32(ca + 76));
            const f32x4 c5 = lds_rf4(ca + 80), c6 = lds_rf4(ca + 96);
            q[0] = acc[4][3] - mean * c19;
#pragma unroll
            for (int i = 0; i < 4; ++i) { q[1 + i] = acc[5][i] - mean * c5[i]; q[5 + i] = acc[6][i] - mean * c6[i]; }
        }
        STAMP(layer * 12 + 5);
        if (ISB) {
            u32x4 hv[5];
            const int src = g - 2 + (wave >> 2);
            auto hload = [&]() {
                const unsigned hb = kbase + (src * 16 + p) * 20480 + (wave & 3) * 5120;
#pragma unroll
                for (int i = 0; i < 5; ++i) hv[i] = ld128c(wsr, l16 + i * 1024, hb);
            };
            if (src >= 0) {
                hload();
            } else {
#pragma unroll
                for (int i = 0; i < 5; ++i) hv[i] = (u32x4){0u, (i == 0 || (i == 2 && lane >= 32) || (i == 3 && lane < 32)) ? 0u : 0xF149u, 0u, 0u};
            }
            {
                const float qs = rstd * (0.23570226039551584f * LOG2E);
                const int hx = gq >> 1, od = gq & 1;
                const unsigned qb = L_QS + (wave >> 1) * 2304;
                lds_w128(qb + (wave & 1) * 256 + od * 1152 + hx * 512 + tk * 16, unit_pair(pk2(q[0] * qs, q[1] * qs), pk2(q[2] * qs, q[3] * qs), pk2(q[4] * qs, q[5] * qs), pk2(q[6] * qs, q[7] * qs)));
                const float q8 = q[8] * qs;
                lds_w32(qb + (wave & 1) * 64 + 1024 + hx * 1152 + tk * 4, pkv(sw16(q8, q8)));
            }
            const int h = wave & 1, jq = wave < 4 ? (wave >> 1) : 3 - ((wave - 4) >> 1);
            const int r = lane & 31, hh = lane >> 5, dl = r - 4 * hh;
            f32x16 msk;
#pragma unroll
            for (int v = 0; v < 16; ++v) msk[v] = ((v & 3) + 8 * (v >> 2) > dl) ? NEGBIG : 0.f;
            const unsigned kb = L_KT + h * 2560 + jq * 5120;
            const unsigned ka = kb + l16, la = kb + 1280 + (r >> 3) * 384 + ((r & 7) ^ 4) * 16;
            const unsigned k8 = 4 * hh + ((lane >> 2) & 3);
            const unsigned va = kb + 1024 + ((lane & 16) ? 256 + (k8 ^ 4) * 16 + 8 : (lane & 2) * 64 + k8 * 16 + (lane & 1) * 8);
            BARRIER();
            const u32x4 q0 = lds_r128(L_QS + (jq * 2 + h) * 1152 + l16);
            const unsigned q1w = lds_r32(L_QS + (jq * 2 + h) * 1152 + 1024 + r * 4);
            u32x4 km = lds_r128(ka + 8 * 5120), kl = lds_r128(la + 8 * 5120);
            u32x2 vf[2][4];
            att_vload(vf[0], va + 8 * 5120);
            STAMP(layer * 12 + 1);
            const u32x4 q1 = {hh ? 0u : q1w, hh ? 0u : 0x3F80u, 0u, 0u};
            f32x16 sc[2], negm, o16 = splat16(0.f);
            sc[0] = att_scores(km, kl, q0, q1, msk);
            {
                const float m = max16(sc[0], NEGBIG);
                const float nm = -vmax(sw32(m, m));
                negm = splat16(nm);
                sc[0] += negm;
                msk = (splat16(NEGBIG) - msk) + negm;
            }
#pragma unroll
            for (int i = 8; i >= 0; --i) {
                if (i >= 5 && i == 8 - jq) {
                    RETRY(layer * 8 + 4, hload(), umax4(umax4(umax4(umax4(umax4(0u, hv[0]), hv[1]), hv[2]), hv[3]), hv[4]));
#pragma unroll
                    for (int j = 0; j < 5; ++j) lds_w128(L_KT + wave * 5120 + j * 1024 + l16, hv[j]);
                    BARRIER();
                    STAMP(layer * 12 + 6);
                }
                SB();
                if (i > 0) { km = lds_r128(ka + (i - 1) * 5120); kl = lds_r128(la + (i - 1) * 5120); att_vload(vf[(i - 1) & 1], va + (i - 1) * 5120); }
                wof[16 - 2 * i] = ld128c(wsr, l16 + (16 - 2 * i) * 1024, wob); wof[17 - 2 * i] = ld128c(wsr, l16 + (17 - 2 * i) * 1024, wob);
                SB();
                const u32x4 p0 = att_p(sc[i & 1], 0);
                SB();
                if (i > 0) sc[(i - 1) & 1] = att_scores(km, kl, q0, q1, i == 1 ? msk : negm);
                o16 = mfmaL((u32x4){vf[i & 1][0].x, vf[i & 1][0].y, vf[i & 1][1].x, vf[i & 1][1].y}, p0, o16);
                SB();
                const u32x4 p1 = att_p(sc[i & 1], 8);
                if (i < 7 && !last) {
                    if (i > 0 || wave < 6) dma1k(nimg + 8 * (6 - i) * 1024, l16, w1k + 8 * (6 - i) * 1024);
                }
                SB();
                o16 = mfmaL((u32x4){vf[i & 1][2].x, vf[i & 1][2].y, vf[i & 1][3].x, vf[i & 1][3].y}, p1, o16);
            }
            asm volatile("s_nop 7\n\ts_nop 7" : "+v"(o16));
            {
                float t = 0.f;
#pragma unroll
                for (int v = 0; v < 11; ++v) t += __builtin_fabsf(o16[v]);
                if (__any(!(t < 3.0e38f))) {
                    float m = NEGBIG;
#pragma unroll 1
                    for (int pass = 0; pass < 2; ++pass) {
                        o16 = splat16(0.f);
#pragma unroll 1
                        for (int i = 0; i <= 8; ++i) {
                            u32x2 v4[4]; att_vload(v4, va + i * 5120);
                            f32x16 s = att_scores(lds_r128(ka + i * 5120), lds_r128(la + i * 5120), q0, q1, splat16(pass ? -m : 0.f));
                            const int lim = i == 8 ? dl : 99, lim2 = i == 0 ? dl : -99;
#pragma unroll
                            for (int v = 0; v < 16; ++v) { const int cv = (v & 3) + 8 * (v >> 2); if (cv > lim || cv <= lim2) s[v] = NEGBIG; }
                            if (pass) att_accum(s, v4, o16); else m = max16(s, m);
                        }
                        if (!pass) m = vmax(sw32(m, m));
                    }
                    asm volatile("s_nop 7\n\ts_nop 7" : "+v"(o16));
                }
            }
            STAMP(layer * 12 + 7);
            const float inv = rcp(o16[10]);
            const unsigned ob = obase + (g * 8 + 2 * jq) * TKT;
            const unsigned vo = (r >> 4) * TKT + (r & 15) * 16;
            st128c(wsr, vo + hh * 256, ob + p * 1024 + h * 512, ok4((u32x4){pk2(o16[0] * inv, o16[1] * inv), pk2(o16[2] * inv, o16[3] * inv), pk2(o16[4] * inv, o16[5] * inv), pk2(o16[6] * inv, o16[7] * inv)}));
            if (hh == 0) st32c(wsr, vo, ob + (16 + (p >> 3)) * 1024 + ((p >> 1) & 3) * 256 + (p & 1) * 8 + h * 4, ok1(pk2(o16[8] * inv, o16[9] * inv)));
        } else {
            const unsigned c16 = (lane & 31) * 16;
            u32x4 pv[8];
            auto pload = [&]() {
                const unsigned kb = kbase + p * 16 * 3072 + (wave < 6 ? wave : 0) * 512;
#pragma unroll
                for (int i = 0; i < 8; ++i) pv[i] = ld128c(wsr, c16 + (lane >> 5) * (8 * 3072), kb + i * 3072);
            };
            if (wave < 6) pload();
            u32x4 fq[2]; unsigned fe;
            dual_softmax(q, rstd * LOG2E, gq < 2, fq[0], fq[1], fe);
            const unsigned r1 = tk == 1 ? 16 : tk == 2 ? 17 : 18;
            const unsigned ra = L_KVS + tk * 80, rb = L_KVS + r1 * 80, ex = 64u + (unsigned)(gq & 1) * 4u;
            if (wave < 6) {
                STAMP(layer * 12 + 6);
                const unsigned keep = (c16 == 496 && (wave == 2 || wave == 5)) ? 0u : ~0u;
                RETRY(layer * 8 + 4, pload(), keep & umax4(umax4(umax4(umax4(umax4(umax4(umax4(umax4(0u, pv[0]), pv[1]), pv[2]), pv[3]), pv[4]), pv[5]), pv[6]), pv[7]));
                float sm[8];
                const unsigned clo = (unsigned)sfresh(0x3F80), chi = (unsigned)sfresh(0x3F800000);
#pragma unroll
                for (int e = 0; e < 8; ++e) sm[e] = 0.f;
#pragma unroll
                for (int i = 0; i < 8; ++i) {
#pragma unroll
                    for (int w = 0; w < 4; ++w) { sm[2 * w] = dot2(clo, pv[i][w], sm[2 * w]); sm[2 * w + 1] = dot2(chi, pv[i][w], sm[2 * w + 1]); }
                }
                const f32x2 a0 = sw32(sm[0], sm[4]), a1 = sw32(sm[1], sm[5]), a2 = sw32(sm[2], sm[6]), a3 = sw32(sm[3], sm[7]);
                lds_w64(L_KVS + wave * 512 + c16 + (lane >> 5) * 8, (u32x2){pk2(a0.x + a0.y, a1.x + a1.y), pk2(a2.x + a2.y, a3.x + a3.y)});
            }
            else if (wave == 7) wait_all256(flag_at(P.ws, it ? FL_W5 : FL_W2, 0), lane, tag);
            STAMP(57 + it);
            BARRIER();
            f32x4 oa[2][2];
            {
                u32x4 am[2][2]; unsigned ae[2][2];
#pragma unroll
                for (int h = 0; h < 2; ++h) {
                    am[h][0] = lds_r128(ra + h * 1536 + gq * 16); am[h][1] = lds_r128(rb + h * 1536 + gq * 16);
                    ae[h][0] = lds_r32(ra + h * 1536 + ex); ae[h][1] = lds_r32(rb + h * 1536 + ex);
                }
                SB();
#pragma unroll
                for (int h = 0; h < 2; ++h) { oa[h][0] = mfma32(am[h][0], fq[h], Z4); oa[h][1] = mfma32(am[h][1], fq[h], Z4); }
#pragma unroll
                for (int h = 0; h < 2; ++h) {
                    const u32x4 fh = w1((gq >> 1) == h ? fe : 0u);
                    oa[h][1] = mfma32(w1(ae[h][1]), fh, oa[h][1]); oa[h][0] = mfma32(w1(ae[h][0]), fh, oa[h][0]);
                }
            }
            STAMP(layer * 12 + 7);
            const float ia = rcp(oa[0][1][0] + 1e-6f), ib = rcp(oa[1][1][0] + 1e-6f);
            const unsigned ob = obase + (g * 8 + wave) * TKT;
            st128c(wsr, l16, ob + p * 1024, ok4((u32x4){pk2(oa[0][0][0] * ia, oa[0][0][1] * ia), pk2(oa[0][0][2] * ia, oa[0][0][3] * ia),
                                                        pk2(oa[1][0][0] * ib, oa[1][0][1] * ib), pk2(oa[1][0][2] * ib, oa[1][0][3] * ib)}));
            if (gq == 0) st64c(wsr, l16, ob + (16 + (p >> 3)) * 1024 + ((p >> 1) & 3) * 256 + (p & 1) * 8,
                               ok2((u32x2){pk2(oa[0][1][1] * ia, oa[0][1][2] * ia), pk2(oa[1][1][1] * ib, oa[1][1][2] * ib)}));
        }
    }
    {
        const int lane = fresh(lane_in), gq = lane >> 4, tk = lane & 15;
        const unsigned l16 = lane * 16;
        BARRIER();
        STAMP(59 + layer);
        u32x4 wox[18];
        if (wave >= 6) {
            const unsigned wb = WS_WOI + ((ISB ? 36 : 0) + 9 * cg + 8) * TKT;
#pragma unroll
            for (int s = 0; s < 18; ++s) wox[s] = ld128c(wsr, l16 + s * 1024, wb);
            if (ISB && it == 0 && wave == 7) wait_all256(flag_at(P.ws, FL_W4, 0), lane, tag);
        } else {
            const unsigned ob = obase + (g * 8 + 2 * tp) * TKT + w1k;
            u32x4 ov[6];
            auto oload = [&]() {
#pragma unroll
                for (int i = 0; i < 6; ++i) ov[i] = ld128c(wsr, l16 + i * 6144, ob);
            };
            oload();
            RETRY(layer * 8 + 5, oload(), umax4(umax4(umax4(umax4(umax4(umax4(0u, ov[0]), ov[1]), ov[2]), ov[3]), ov[4]), ov[5]));
            STAMP(layer * 12 + 8);
#pragma unroll
            for (int i = 0; i < 6; ++i) lds_w128(L_OST + w1k + i * 6144 + l16, ov[i]);
        }
        if (first && blockIdx.x == 0 && wave == 0 && lane == 0) __hip_atomic_store((GAS unsigned*)P.ws, tag, RLX_AGENT);
        BARRIER();
        STAMP(layer * 12 + 9);
        {
            f32x4 c0 = res[0], c1 = res[1], c2 = res[2];
            const unsigned lo = L_OST + l16;
            if (wave >= 6) {
                u32x4 ox[18];
#pragma unroll
                for (int s = 0; s < 18; ++s) ox[s] = lds_r128(lo + (wave & 1) * TKT + s * 1024);
                SB();
#pragma unroll
                for (int s = 0; s < 18; ++s) c2 = mfma32(wox[s], ox[s], c2);
            }
            SB();
            u32x4 of[4][2];
#pragma unroll
            for (int s = 0; s < 3; ++s) { of[s][0] = lds_r128(lo + s * 1024); of[s][1] = lds_r128(lo + TKT + s * 1024); }
#pragma unroll
            for (int s = 0; s < 18; ++s) {
                SB();
                c0 = mfma32(wof[s], of[s & 3][0], c0);
                SB();
                if (s + 3 < 18) of[(s + 3) & 3][0] = lds_r128(lo + (s + 3) * 1024);
                SB();
                c1 = mfma32(wof[s], of[s & 3][1], c1);
                SB();
                if (s + 3 < 18) of[(s + 3) & 3][1] = lds_r128(lo + TKT + (s + 3) * 1024);
                if (ISB) { if ((s & 1) == 0 && s < 10 && !last && (s < 8 || wave < 4)) dma1k(nimg + (54 + 4 * s) * 1024, l16, w1k + (54 + 4 * s) * 1024); }
                else if (s < 12 && (s < 11 || wave < 2)) dma1k(nimg + 8 * s * 1024, l16, w1k + 8 * s * 1024);
            }
            res[0] = c0; res[1] = c1; res[2] = c2;
            STAMP(layer * 12 + 10);
            const u32x4 v = unit_pair(pk2(c0[0], c0[1]), pk2(c0[2], c0[3]), pk2(c1[0], c1[1]), pk2(c1[2], c1[3]));
            const u32x2 v2 = {pk2(c2[0], c2[1]), pk2(c2[2], c2[3])};
            const int ct = 9 * cg + wave, cx = 9 * cg + 8;
            const int tx = 2 * tp + (wave & 1);
            if (!last) {
                const unsigned xb = obase + ACT_BYTES + g * 8 * TKT, sb = obase + 2 * ACT_BYTES + g * 8 * 2304;
                const unsigned vo = (gq >> 1) * 256 + tk * 16;
                st128c(wsr, (gq & 1) * TKT + vo, xb + 2 * tp * TKT + ct * 512, ok4(v));
                const f32x2 a0 = {c0[0], c0[1]}, a1 = {c0[2], c0[3]}, b0 = {c1[0], c1[1]}, b1 = {c1[2], c1[3]};
                const f32x2 qa = fma2(a1, a1, a0 * a0), qb = fma2(b1, b1, b0 * b0);
                const f32x2 u = sw16(qa.x + qa.y, qb.x + qb.y); const float u1 = u.x + u.y; const f32x2 u2 = sw32(u1, u1);
                if (gq < 2) st32c(wsr, gq * 2304 + tk * 4, sb + 2 * tp * 2304 + ct * 64, ok1(u_of(u2.x + u2.y)));
                if (wave >= 6) {
                    st64c(wsr, vo + (gq & 1) * 8, xb + tx * TKT + cx * 512, ok2(v2));
                    const f32x2 d0 = {c2[0], c2[1]}, d1 = {c2[2], c2[3]}, qd = fma2(d1, d1, d0 * d0);
                    const float e0 = qd.x + qd.y; const f32x2 e1 = sw16(e0, e0); const float e2 = e1.x + e1.y; const f32x2 e3 = sw32(e2, e2);
                    if (gq == 0) st32c(wsr, tk * 4, sb + tx * 2304 + cx * 64, ok1(u_of(e3.x + e3.y)));
                }

            } else {
                const int tt = 2 * tp + (gq & 1);
                *(u32x4*)(P.out + (size_t)(g * 128 + tt * 16 + tk) * DM + 16 * ct + 8 * (gq >> 1)) = v;
                if (wave >= 6) *(u32x2*)(P.out + (size_t)(g * 128 + tx * 16 + tk) * DM + 16 * cx + 4 * gq) = v2;
            }
        }
        STAMP(layer * 12 + 11);
    }
    if (ISB) {
#pragma unroll
        for (int s = 0; s < 18; ++s) asm volatile("" : "=v"(xf[s]));
#pragma unroll
        for (int t = 0; t < 5; ++t) asm volatile("" : "=v"(acc[t]));
        asm volatile("" : "=v"(ssq));
    }
}

__global__ void __launch_bounds__(512, 2) fused_kernel(Params P) {
    const int tid = threadIdx.x, lane = tid & 63, wave = __builtin_amdgcn_readfirstlane(tid >> 6);
    const int b = blockIdx.x, g = ((b >> 1) & 3) * 4 + (b >> 6), p = (b & 1) * 8 + ((b >> 3) & 7);
    const rsrc_t wsr = mk_rsrc(P.ws, WS_END);
    STAMP(0);
    if (lane < 3) {
        unsigned long long pc; unsigned keep;
        asm volatile("s_getpc_b64 %0" : "=s"(pc));
        const unsigned o = (((b >> 3) * 8 + wave) * 3 + lane) * 64;
        asm volatile("s_mov_b32 %0, m0\n\ts_mov_b32 m0, %3\n\ts_nop 0\n\tglobal_load_lds_dword %1, %2\n\ts_mov_b32 m0, %0" : "=&s"(keep) : "v"(o < CODE_BYTES ? o : 0u), "s"(pc), "s"(L_CS + 64 + wave * 16) : "memory");
    }
    u32x4 xf[18];
    f32x4 res[3];
    f32x4 acc[7], sq;
    float ssq;
    Ctx C; C.wave = wave; C.g = g; C.p = p;
    {
        const int gq = lane >> 4, tk = lane & 15;
        const f32x4 Z4 = {0.f, 0.f, 0.f, 0.f};
        const unsigned epv = __hip_atomic_load((const GAS unsigned*)P.ws, RLX_AGENT);
        const u16* xs = P.x + (size_t)(g * 128) * DM;
        u32x4 raw[8];
        unsigned ph0, ph1;
        const unsigned c0 = (unsigned)(gq == 1 ? 8 : gq == 2 ? 10 : 0);
        const unsigned wv = ((unsigned)tk * NQKV + c0) * 2u, wo = p * 36 + wave * 576;
        if (wave >= 4) {
            const unsigned xo = ((unsigned)(lane >> 3) * DM + 8u * ((lane & 7) ^ (lane >> 3))) * 2u;
            __builtin_amdgcn_s_sleep(DSLEEP);
#pragma unroll 1
            for (int j = 0; j < 4; ++j) {
                const unsigned oc = (wave - 4 + 4 * j + 2 * p) & 15;
                const u16* src = xs + oc * 8 * DM;
                unsigned dst = (oc >> 1) * TKT + (oc & 1) * 1024, keep, vo = xo;
                asm volatile("s_mov_b32 %0, m0\n\t"
                             ".rept 9\n\ts_mov_b32 m0, %1\n\ts_add_u32 %1, %1, 0x800\n\tglobal_load_lds_dwordx4 %2, %3\n\tv_add_u32 %2, 0x80, %2\n\t.endr\n\t"
                             "s_mov_b32 m0, %0" : "=&s"(keep), "+s"(dst), "+v"(vo) : "s"(src) : "memory", "scc");
            }
            STAMP(49);
        }
        SB();
        u32x4 im, ix, lraw[8];
        unsigned wdst = 0, km[2], kx;
        auto unit_tile = [&](const u32x4* raw, const u32x4 (&bm)[2], const u32x4 (&bl)[2], unsigned pb) {
            u32x2 lf[4];
#pragma unroll
            for (int i = 0; i < 4; ++i) {
                const f32x4 cm = mfma32(raw[i], bm[i >> 1], Z4), cl = mfma32(raw[i], bl[i >> 1], Z4);
                lds_w64(km[i >> 1] + pb + (i & 1) * 512, (u32x2){pk2(cm[0], cm[1]), pk2(cm[2], cm[3])});
                lf[i] = (u32x2){pk2(cl[0], cl[1]), pk2(cl[2], cl[3])};
            }
            if (tk < 2) {
#pragma unroll
                for (int i = 0; i < 4; ++i) lds_w64(kx + pb + (i >> 1) * 128 + (i & 1) * 512, lf[i]);
            }
        };
        auto meet = [&](unsigned word, unsigned val) {
            asm volatile("s_waitcnt lgkmcnt(0)" ::: "memory");
            if (lane == 0) lds_w32(word + wave * 4, val);
#pragma unroll 1
            for (unsigned sp = 0; sp < SPIN_CAP; ++sp) { asm volatile("" ::: "memory"); if (__all(lds_r32(word + (lane < 3 ? lane : 0) * 4) == val)) break; }
        };
        if (wave < 3) {
#pragma unroll
            for (int i = 0; i < 4; ++i) ld128_raw(raw[i], P.wqkvA + wo + g * (32 * NQKV) + (i & 1) * (16 * NQKV) + (i >> 1) * 18, wv);
#pragma unroll
            for (int i = 0; i < 4; ++i) ld128_raw(raw[4 + i], P.wqkvA + wo + (16 + (g & 1)) * (32 * NQKV) + (i & 1) * (16 * NQKV) + (i >> 1) * 18, g < 2 ? wv : 0u);
            asm volatile("global_load_dword %0, %2, %4\n\tglobal_load_dword %1, %3, %4" : "=&v"(ph0), "=&v"(ph1) : "v"(tid * 4u), "v"((tid < 132 ? tid + 192 : 0) * 4u), "s"(P.wphi + p * 648) : "memory");
            SB();
            { const int l2 = fresh(lane); im = onehot8(((l2 >> 3) & 1) == (l2 >> 4) ? (l2 & 7) : -2); ix = onehot8((l2 >> 4) == 2 ? 6 + (l2 & 1) : -2); }
            constexpr unsigned L_PHI = L_PST + 7168;
            unsigned pa[2];
#pragma unroll
            for (int i = 0; i < 2; ++i) {
                const int z = tid + 192 * i, row = z / 7, j = z - 7 * row;
                if (z < 252) lds_w32(L_PHI + row * 64 + (j < 3 ? 32 + 4 * j : 36 + 4 * j), 0u);
                const int e = 2 * z, hh = e >= 324, r = e - 324 * hh, d = r / 18, f = r - 18 * d;
                pa[i] = L_PHI + (unsigned)(hh * 18 + f) * 64u + (unsigned)(d < 16 ? d : d + 6) * 2u;
            }
            const unsigned sb = wave == 0 ? 19 : wave == 2 ? 9 : 0, sl = sb + 8;
            const unsigned ko = L_PST + (gq >> 1) * 256 + (gq & 1) * 8;
#pragma unroll
            for (int h = 0; h < 2; ++h) { const unsigned sm = sb + 4 * h + (tk & 3); km[h] = ko + (tk >> 2) * 64 + (sm >> 2) * 1024 + (sm & 3) * 16; }
            kx = ko + (tk & 1) * 64 + (sl >> 2) * 1024 + (sl & 3) * 16;
            const unsigned onea = L_PST + 4096 + (tk >> 2) * 256 + (4 * (tk & 3) + 2) * 16;
            const u32x4 one4 = {0x3F803F80u, 0x3F803F80u, 0x3F803F80u, 0x3F803F80u};
            if (wave == 2 && lane < 16) lds_w128(onea, one4);
            asm volatile("s_waitcnt vmcnt(0)" : "+v"(ph0), "+v"(ph1), "+v"(raw[0]), "+v"(raw[1]), "+v"(raw[2]), "+v"(raw[3]), "+v"(raw[4]), "+v"(raw[5]), "+v"(raw[6]), "+v"(raw[7]) :: "memory");
            lds_w16(pa[0], ph0); lds_w16(pa[0] + 64, ph0 >> 16);
            if (tid < 132) { lds_w16(pa[1], ph1); lds_w16(pa[1] + 64, ph1 >> 16); }
            const unsigned tag = __builtin_amdgcn_readfirstlane(epv) * 0x9E3779B1u + 0x7F4A7C15u;
            meet(L_CS + 32, tag);
            u32x4 bm[2] = {im, im}, bl[2] = {ix, ix};
            if (wave < 2) {
#pragma unroll
                for (int h = 0; h < 2; ++h) { bm[h] = lds_r128(L_PHI + (h * 18 + tk) * 64 + gq * 16); bl[h] = lds_r128(L_PHI + (h * 18 + 16 + (tk & 1)) * 64 + gq * 16); }
            }
            STAMP(48);
            unit_tile(raw, bm, bl, 0);
            meet(L_CS, tag);
            if (wave == 2 && lane < 16) lds_w128(onea + 7168, one4);
            if (g < 2) { unit_tile(raw + 4, bm, bl, 7168); meet(L_CS + 48, tag); }
            const unsigned o = WS_IMA + p * IMG_BYTES + wave * TKT, la = L_PST + wave * 1024 + lane * 16;
#pragma unroll
            for (int u = 0; u < 2; ++u) {
                if (u == 0 || g < 2) {
                    const unsigned oo = o + (u ? 16 + g : g) * 1024;
                    st128c(wsr, lane * 16, oo, lds_r128(la + u * 7168)); st128c(wsr, lane * 16, oo + 3 * TKT, lds_r128(la + u * 7168 + 3072));
                    if (wave == 0) st128c(wsr, lane * 16, oo + 6 * TKT, lds_r128(la + u * 7168 + 6144));
                }
            }
            DRAIN();
            meet(L_CS + 16, tag);
            if (tid == 0) set_flag(P.ws, FL_IA, p * 32 + g, tag);
            STAMP(63);
            const rsrc_t rw = mk_rsrc(P.wqkvB, DM * NQKV * 2);
#pragma unroll
            for (int i = 0; i < 8; ++i) lraw[i] = __builtin_amdgcn_raw_buffer_load_b128(rw, (i < 4 || (g >> 1) == 1) ? wv : 0x7fffffffu,
                                                      (wo + (i < 4 ? g : 16 + (g & 1)) * (32 * NQKV) + (i & 1) * (16 * NQKV) + ((i >> 1) & 1) * 18) * 2, 0);
        } else {
            { const int l2 = fresh(lane); im = onehot8(((l2 >> 3) & 1) == (l2 >> 4) ? (l2 & 7) : -2); ix = onehot8(((l2 >> 3) & 1) + 2 == (l2 >> 4) ? (l2 & 7) : -2); }
#pragma unroll
            for (int i = 2; i < 8; ++i) lraw[i] = im;
            const int u = b + 256 * (wave - 3), m = u >= 324, r = u - 324 * m, cp = r / 18, s = r - 18 * cp;
            const unsigned w0 = (unsigned)(wo_krow(m, s, tk >> 2, tk & 3) * DM + 8 * gq) * 2u, w1o = (unsigned)(wo_krow(m, s, tk >> 2, 4 + (tk & 3)) * DM + 8 * gq) * 2u;
            const rsrc_t rw = mk_rsrc(m ? P.woB : P.woA, u < 648 ? DM * DM * 2 : 0);
            const unsigned tag = __builtin_amdgcn_readfirstlane(epv) * 0x9E3779B1u + 0x7F4A7C15u;
            lraw[0] = __builtin_amdgcn_raw_buffer_load_b128(rw, w0, 64 * cp, 0); lraw[1] = __builtin_amdgcn_raw_buffer_load_b128(rw, w1o, 64 * cp, 0);
            wdst = u < 648 ? WS_WOI + (unsigned)((m * 36 + 2 * cp) * 18 + s) * 1024u : 0u;
            asm volatile("s_waitcnt vmcnt(2)" ::: "memory");
            STAMP(48);
            if (wave == 3) wait_flags(flag_at(P.ws, FL_IA, p * 32), tk * 128, tag);
        }
        C.tag = __builtin_amdgcn_readfirstlane(epv) * 0x9E3779B1u + 0x7F4A7C15u;
        BARRIER();
        STAMP(50);
        {
            const unsigned xa = (tk >> 3) * 1024 + (tk & 7) * 128;
#pragma unroll
            for (int s = 0; s < 18; ++s) xf[s] = lds_r128(wave * TKT + (s >> 1) * 2048 + xa + (((4 * (s & 1) + gq) ^ (tk & 7)) * 16));
            const int tp = (p >> 1) & 3, cg = (p >> 3) * 2 + (p & 1);
#pragma unroll
            for (int i = 0; i < 3; ++i) {
                const int tt = 2 * tp + (i < 2 ? i : (wave & 1)), ct = 9 * cg + (i < 2 ? wave : 8);
                const u32x2 r = *(const LAS u32x2*)((LAS unsigned char*)smem + tt * TKT + (ct >> 2) * 2048 + xa + (((2 * (ct & 3) + (gq >> 1)) ^ (tk & 7)) * 16) + (gq & 1) * 8);
                res[i] = (f32x4){f_of(r.x << 16), f_of(r.x & 0xffff0000u), f_of(r.y << 16), f_of(r.y & 0xffff0000u)};
            }
        }
        BARRIER();
        STAMP(51);
        const unsigned char* img = P.ws + WS_IMA + p * IMG_BYTES;
        unsigned fo[4];
#pragma unroll
        for (int i = 0; i < 4; ++i) { const int j = i * 8 + wave, jt = j / 6; fo[i] = (j < 30 ? jt * 12 + j : 60 + j) * 1024; }
        const unsigned st3 = wave < 6 ? 6144 : 2048;
        auto piece = [&](int c, int i) {
            unsigned f = c < 3 ? fo[i] + c * (i < 3 ? 6144 : st3) : (96 + i * 8 + wave) * 1024;
            if (c == 3 && i == 3) f = f < 125 * 1024 ? f : 125 * 1024;
            dma1k(img + f, lane * 16, f);
        };
#pragma unroll
        for (int i = 0; i < 8; ++i) asm volatile("" :: "v"(lraw[i]));
#pragma unroll
        for (int i = 0; i < 8; ++i) piece(i >> 2, i & 3);
        STAMP(1);
        if (wave < 3) {
            const u32x4 bm[2] = {im, im}, bl[2] = {ix, ix};
            unit_tile(lraw, bm, bl, 0);
            if ((g >> 1) == 1) unit_tile(lraw + 4, bm, bl, 7168);
        } else {
            if (wdst) {
                const f32x4 a0 = mfma32(lraw[0], im, Z4), a1 = mfma32(lraw[1], im, Z4), c0 = mfma32(lraw[0], ix, Z4), c1 = mfma32(lraw[1], ix, Z4);
                st128c(wsr, lane * 16, wdst, (u32x4){pk2(a0[0], a0[1]), pk2(a0[2], a0[3]), pk2(a1[0], a1[1]), pk2(a1[2], a1[3])});
                st128c(wsr, lane * 16, wdst + TKT, (u32x4){pk2(c0[0], c0[1]), pk2(c0[2], c0[3]), pk2(c1[0], c1[1]), pk2(c1[2], c1[3])});
            }
            if (wave >= 5) arm_unit(wsr, lane * 16, wave - 5, b);
        }
#pragma unroll
        for (int t = 0; t < 7; ++t) acc[t] = Z4;
        sq = Z4;
        const unsigned lb0[3] = {(unsigned)lane * 16, (unsigned)lane * 16 + 6144, (unsigned)lane * 16 + 12288};
        if (wave < 3) asm volatile("s_waitcnt vmcnt(4)" ::: "memory"); else if (wave == 5) asm volatile("s_waitcnt vmcnt(5)" ::: "memory");
        else if (wave < 5) asm volatile("s_waitcnt vmcnt(6)" ::: "memory"); else asm volatile("s_waitcnt vmcnt(5)" ::: "memory");
        BARRIER();
        STAMP(2);
        gemm_img<0, 5, 0, 6, true, 2>(acc, xf, lb0, sq, [&](int s) {
            if (s < 2 && wave < 7 && (s == 0 || (g >> 1) == 1)) st128c(wsr, lane * 16, WS_IMB + p * IMG_BYTES + wave * TKT + (s ? 14 + g : g) * 1024, lds_r128(L_PST + s * 7168 + wave * 1024 + lane * 16));
            if (s >= 2) piece(2, s - 2); });
        STAMP(25);
        asm volatile("s_waitcnt vmcnt(4)" ::: "memory"); BARRIER();
        if (tid == 0) set_flag(P.ws, FL_W1, g * 16 + p, C.tag);
        STAMP(26);
        gemm_img<0, 5, 6, 12, true, 2>(acc, xf, lb0, sq, [&](int s) { if (s < 10) piece(3, s - 6); if (s >= 10) arm_unit(wsr, lane * 16, 3 + (s - 10) * 8 + wave, b); });
        STAMP(27);
        asm volatile("s_waitcnt vmcnt(6)" ::: "memory"); BARRIER();
        STAMP(28);
        gemm_img<0, 5, 12, 18, true, 2>(acc, xf, lb0, sq, [&](int s) { if (s < 17 || wave < 4) arm_unit(wsr, lane * 16, 19 + (s - 12) * 8 + wave, b); });
        STAMP(29);
        if (wave < 4) colsum_tile(wave, L_CS, lane);
        if (wave == 0) colsum_tile(4, L_CS, lane);
        asm volatile("s_nop 7" : "+v"(sq));
        const float d0 = (tk & 1) ? sq[1] : sq[0], d1 = (tk & 1) ? sq[3] : sq[2], dg = (tk & 2) ? d1 : d0;
        ssq = gq == (tk >> 2) ? dg : 0.f;
        BARRIER();
    }
#pragma unroll 1
    for (int it = 0; it < NIT; ++it) {
        layer_fn<false>(P, wsr, lane, C, it, xf, res, acc, ssq);
        layer_fn<true>(P, wsr, lane, C, it, xf, res, acc, ssq);
    }
}

extern "C" void kernel_launch(void* const* d_in, const int* in_sizes, int n_in, void* d_out, int out_size, void* d_ws, size_t ws_size, hipStream_t stream) {
    Params P;
    P.x = (const u16*)d_in[0]; P.wqkvA = (const u16*)d_in[1]; P.wphi = (const u16*)d_in[2]; P.woA = (const u16*)d_in[3];
    P.wqkvB = (const u16*)d_in[5]; P.woB = (const u16*)d_in[6];
    P.out = (u16*)d_out; P.ws = (unsigned char*)d_ws;
    (void)hipFuncSetAttribute((const void*)fused_kernel, hipFuncAttributeMaxDynamicSharedMemorySize, LDS_BYTES);
    hipLaunchKernelGGL(fused_kernel, dim3(256), dim3(512), LDS_BYTES, stream, P);
}
```

```cpp
#include <hip/hip_runtime.h>
#include <stdint.h>
#define ODRAIN 0
#define DSLEEP 16
#define CSPA 0
#define CSPB 1
#define BAR2 1
#define XSLEEP 4
#define RSLEEP 6
#define NIT 2
#define ONEPOLL 0

typedef __attribute__((ext_vector_type(8))) __bf16 bf16x8;
typedef __attribute__((ext_vector_type(2))) __bf16 bf16x2;
typedef __attribute__((ext_vector_type(4))) short s16x4;
typedef __attribute__((ext_vector_type(4))) float f32x4;
typedef __attribute__((ext_vector_type(2))) float f32x2;
typedef __attribute__((ext_vector_type(16))) float f32x16;
typedef __attribute__((ext_vector_type(4))) unsigned u32x4;
typedef __attribute__((ext_vector_type(2))) unsigned u32x2;
typedef unsigned short u16;
#define LAS __attribute__((address_space(3)))
#define GAS __attribute__((address_space(1)))
#define RLX_AGENT __ATOMIC_RELAXED, __HIP_MEMORY_SCOPE_AGENT
#define DEVI __device__ __forceinline__

constexpr int SEQ = 2048, DM = 576, NQKV = 1728;
constexpr unsigned TKT = 18 * 1024;
constexpr unsigned ACT_BYTES = 128 * TKT;
constexpr unsigned IMG_BYTES = 7 * TKT;
constexpr unsigned WS_FLAGS = 4096;
constexpr unsigned FL_W1 = 0, FL_W2 = 1, FL_W4 = 2, FL_W5 = 3, FL_IA = 4, FL_IB = 6, FL_N = 8;
constexpr unsigned KVP_BYTES = 256 * 3072, KVH_BYTES = 256 * 20480, ST_BYTES = 128 * 2304, UNIT = 256 * 1024;
constexpr unsigned SETA = KVP_BYTES + 2 * ACT_BYTES + ST_BYTES;
constexpr unsigned N0 = 23, N1 = 40, N2 = 52;
constexpr unsigned WS_S0 = 524288, WS_S1 = WS_S0 + N0 * UNIT, WS_S2 = WS_S1 + N1 * UNIT;
static_assert(WS_FLAGS + FL_N * 256 * 128 <= WS_S0 && SETA <= N0 * UNIT && KVH_BYTES + 2 * ACT_BYTES + ST_BYTES <= N1 * UNIT && SETA + KVH_BYTES + ACT_BYTES <= N2 * UNIT, "map");
__host__ __device__ constexpr unsigned ws_k(int layer) { return layer == 0 ? WS_S0 : layer == 1 ? WS_S1 : layer == 2 ? WS_S2 : WS_S2 + SETA; }
__host__ __device__ constexpr unsigned ws_o(int layer) { return ws_k(layer) + ((layer & 1) ? KVH_BYTES : KVP_BYTES); }
constexpr unsigned WS_IMA = WS_S2 + N2 * UNIT;
constexpr unsigned WS_IMB = WS_IMA + 16 * IMG_BYTES;
constexpr unsigned WS_WOI = WS_IMB + 16 * IMG_BYTES;
constexpr unsigned WS_CSP = WS_WOI + 2 * 36 * TKT;
constexpr unsigned WS_END = WS_CSP + 2 * 16 * 18 * 448;
constexpr unsigned WS_DBG = WS_END;
constexpr unsigned L_OST = 5 * TKT;
constexpr unsigned L_STG = IMG_BYTES;
constexpr unsigned L_KT = L_STG - 40960;
constexpr unsigned L_QS = L_STG + 20480;
constexpr unsigned L_PK = L_STG, L_VV = L_STG + 20480;
constexpr unsigned L_KVS = 0;
constexpr unsigned L_CS = L_STG + 32768;
constexpr unsigned L_PST = 147456;
constexpr unsigned LDS_BYTES = 163840;
constexpr unsigned CODE_BYTES = 42 * 1024;
constexpr unsigned SPIN_CAP = 1u << 17;
constexpr float LOG2E = 1.4426950408889634f;
constexpr float NEGBIG = -1e30f;

struct Params {
    const u16* x; const u16* wqkvA; const u16* wphi; const u16* woA; const u16* wqkvB; const u16* woB;
    u16* out; unsigned char* ws;
};

DEVI f32x4 mfma32(u32x4 a, u32x4 b, f32x4 c) { return __builtin_amdgcn_mfma_f32_16x16x32_bf16(__builtin_bit_cast(bf16x8, a), __builtin_bit_cast(bf16x8, b), c, 0, 0, 0); }
DEVI f32x16 mfmaL(u32x4 a, u32x4 b, f32x16 c) { return __builtin_amdgcn_mfma_f32_32x32x16_bf16(__builtin_bit_cast(bf16x8, a), __builtin_bit_cast(bf16x8, b), c, 0, 0, 0); }
DEVI u32x4 w1(unsigned a) { return (u32x4){a, 0u, 0u, 0u}; }
DEVI unsigned pkv(f32x2 f) { return __builtin_bit_cast(unsigned, __builtin_convertvector(f, bf16x2)); }
DEVI unsigned pk2(float lo, float hi) { return pkv((f32x2){lo, hi}); }
DEVI float dot2(unsigned a, unsigned b, float c) { return __builtin_amdgcn_fdot2_f32_bf16(__builtin_bit_cast(bf16x2, a), __builtin_bit_cast(bf16x2, b), c, false); }
DEVI float ex2(float x) { return __builtin_amdgcn_exp2f(x); }
DEVI float rcp(float x) { return __builtin_amdgcn_rcpf(x); }
DEVI float f_of(unsigned u) { return __builtin_bit_cast(float, u); }
DEVI unsigned u_of(float f) { return __builtin_bit_cast(unsigned, f); }
DEVI f32x2 fma2(f32x2 a, f32x2 b, f32x2 c) { return __builtin_elementwise_fma(a, b, c); }
DEVI f32x2 sp2(float v) { return (f32x2){v, v}; }
DEVI f32x2 sw16(float a, float b) { const auto r = __builtin_amdgcn_permlane16_swap(u_of(a), u_of(b), false, false); return (f32x2){f_of(r[0]), f_of(r[1])}; }
DEVI f32x2 sw32(float a, float b) { const auto r = __builtin_amdgcn_permlane32_swap(u_of(a), u_of(b), false, false); return (f32x2){f_of(r[0]), f_of(r[1])}; }
DEVI float vmax(f32x2 v) { float r; asm("v_max_f32 %0, %1, %2" : "=v"(r) : "v"(v.x), "v"(v.y)); return r; }
DEVI float vmaxn(f32x2 v) { float r; asm("v_max_f32_e64 %0, -%1, -%2" : "=v"(r) : "v"(v.x), "v"(v.y)); return r; }
DEVI float max3f(float a, float b, float c) { return __builtin_fmaxf(__builtin_fmaxf(a, b), c); }
DEVI float min3f(float a, float b, float c) { return __builtin_fminf(__builtin_fminf(a, b), c); }
DEVI u32x4 unit_pair(unsigned a0, unsigned a1, unsigned b0, unsigned b1) {
    const auto s = __builtin_amdgcn_permlane16_swap(a0, b0, false, false);
    const auto t = __builtin_amdgcn_permlane16_swap(a1, b1, false, false);
    return (u32x4){s[0], t[0], s[1], t[1]};
}

#define BARRIER() asm volatile("s_waitcnt lgkmcnt(0)\n\ts_barrier" ::: "memory")
#define DRAIN() asm volatile("s_waitcnt vmcnt(0)" ::: "memory")
#define SB() __builtin_amdgcn_sched_barrier(0)

extern __shared__ __attribute__((aligned(16))) unsigned char smem[];
DEVI u32x4 lds_r128(unsigned off) { return *(const LAS u32x4*)((LAS unsigned char*)smem + off); }
DEVI f32x4 lds_rf4(unsigned off) { return *(const LAS f32x4*)((LAS unsigned char*)smem + off); }
DEVI unsigned lds_r32(unsigned off) { return *(const LAS unsigned*)((LAS unsigned char*)smem + off); }
DEVI void lds_w128(unsigned off, u32x4 v) { *(LAS u32x4*)((LAS unsigned char*)smem + off) = v; }
DEVI void lds_w64(unsigned off, u32x2 v) { *(LAS u32x2*)((LAS unsigned char*)smem + off) = v; }
DEVI void lds_w32(unsigned off, unsigned v) { *(LAS unsigned*)((LAS unsigned char*)smem + off) = v; }
DEVI void lds_w16(unsigned off, unsigned v) { *(LAS u16*)((LAS unsigned char*)smem + off) = (u16)v; }
DEVI u32x2 lds_tr(unsigned off) { return __builtin_bit_cast(u32x2, __builtin_amdgcn_ds_read_tr16_b64_v4i16((LAS s16x4*)((LAS unsigned char*)smem + off))); }

typedef __amdgpu_buffer_rsrc_t rsrc_t;
DEVI rsrc_t mk_rsrc(const void* p, unsigned bytes) { return __builtin_amdgcn_make_buffer_rsrc((void*)p, 0, bytes, 0x00020000); }
DEVI u32x4 ld128c(rsrc_t r, unsigned voff, unsigned soff) { return __builtin_amdgcn_raw_buffer_load_b128(r, voff, soff, 16); }
DEVI void st128c(rsrc_t r, unsigned voff, unsigned soff, u32x4 v) {
    __builtin_amdgcn_raw_buffer_store_b128(v, r, voff, soff, 16);
    asm volatile("s_nop 1" :: "v"(v) : "memory");
}
DEVI void st64c(rsrc_t r, unsigned voff, unsigned soff, u32x2 v) { __builtin_amdgcn_raw_buffer_store_b64(v, r, voff, soff, 16); }
DEVI void st32c(rsrc_t r, unsigned voff, unsigned soff, unsigned v) { __builtin_amdgcn_raw_buffer_store_b32(v, r, voff, soff, 16); }
DEVI void dma1k(const void* sbase, unsigned voff, unsigned lds_dst) {
    unsigned keep;
    asm volatile("s_mov_b32 %0, m0\n\ts_mov_b32 m0, %3\n\ts_nop 0\n\tglobal_load_lds_dwordx4 %1, %2 sc1\n\ts_mov_b32 m0, %0" : "=&s"(keep) : "v"(voff), "s"(sbase), "s"(lds_dst) : "memory");
}
DEVI void dma1k_in(const void* sbase, unsigned voff, unsigned lds_dst) {
    unsigned keep;
    asm volatile("s_mov_b32 %0, m0\n\ts_mov_b32 m0, %3\n\ts_nop 0\n\tglobal_load_lds_dwordx4 %1, %2\n\ts_mov_b32 m0, %0" : "=&s"(keep) : "v"(voff), "s"(sbase), "s"(lds_dst) : "memory");
}
DEVI void ld128_raw(u32x4& d, const void* base, unsigned voff) { asm volatile("global_load_dwordx4 %0, %1, %2" : "=v"(d) : "v"(voff), "s"(base) : "memory"); }
DEVI void ld64_raw(u32x2& d, const void* base, unsigned voff) { asm volatile("global_load_dwordx2 %0, %1, %2" : "=v"(d) : "v"(voff), "s"(base) : "memory"); }
DEVI void wait_flags(const unsigned char* base, unsigned voff, unsigned tag) {
    const GAS unsigned* f = (const GAS unsigned*)(base + voff);
#pragma unroll 1
    for (unsigned sp = 0; sp < SPIN_CAP; ++sp) {
        const unsigned v = __hip_atomic_load(f, RLX_AGENT);
        if (__all(v == tag)) break;
        __builtin_amdgcn_s_sleep(1);
    }
    asm volatile("" ::: "memory");
}
DEVI unsigned umax3(unsigned a, unsigned b, unsigned c) { return __builtin_elementwise_max(__builtin_elementwise_max(a, b), c); }
DEVI unsigned umax4(unsigned m, u32x4 v) { return umax3(umax3(m, v.x, v.y), v.z, v.w); }
DEVI unsigned ok1(unsigned v) { return __builtin_elementwise_min(v, 0xFFFFFFFEu); }
DEVI u32x2 ok2(u32x2 v) { return (u32x2){ok1(v.x), ok1(v.y)}; }
DEVI u32x4 ok4(u32x4 v) { return (u32x4){ok1(v.x), ok1(v.y), ok1(v.z), ok1(v.w)}; }
DEVI void arm_unit(rsrc_t r, unsigned l16, unsigned unit, unsigned cu) { st128c(r, l16, WS_S0 + (unit * 256 + cu) * 1024, (u32x4){~0u, ~0u, ~0u, ~0u}); }
DEVI void wait_all256(const unsigned char* base, int lane, unsigned tag) {
    const GAS unsigned* f = (const GAS unsigned*)(base + lane * 128);
#pragma unroll 1
    for (unsigned sp = 0; sp < SPIN_CAP; ++sp) {
        const unsigned v0 = __hip_atomic_load(f, RLX_AGENT), v1 = __hip_atomic_load(f + 2048, RLX_AGENT), v2 = __hip_atomic_load(f + 4096, RLX_AGENT), v3 = __hip_atomic_load(f + 6144, RLX_AGENT);
        if (__all(v0 == tag && v1 == tag && v2 == tag && v3 == tag)) break;
        __builtin_amdgcn_s_sleep(1);
    }
    asm volatile("" ::: "memory");
}
#define RCOUNT(site) do { } while (0)
#define RETRY(site, LOADS, MAXEXPR) do { _Pragma("unroll 1") for (unsigned sp_ = 0; sp_ < SPIN_CAP; ++sp_) { if (!__any((MAXEXPR) == 0xFFFFFFFFu)) break; \
    RCOUNT(site); __builtin_amdgcn_s_sleep(RSLEEP); asm volatile("" ::: "memory"); LOADS; } } while (0)
DEVI const unsigned char* flag_at(const unsigned char* ws, unsigned kind, unsigned idx) { return ws + WS_FLAGS + (kind * 256 + idx) * 128; }
DEVI void set_flag(unsigned char* ws, unsigned kind, unsigned idx, unsigned tag) {
    __hip_atomic_store((GAS unsigned*)(ws + WS_FLAGS + (kind * 256 + idx) * 128), tag, RLX_AGENT);
}

#define STAMP(i) do { } while (0)

DEVI u32x4 onehot8(int j) {
    const unsigned v = (j & 1) ? 0x3F800000u : 0x3F80u; const int w = j >> 1;
    return (u32x4){w == 0 ? v : 0u, w == 1 ? v : 0u, w == 2 ? v : 0u, w == 3 ? v : 0u};
}
DEVI int wo_krow(int mat, int s, int gq, int j) {
    const int a = 36 * s + (mat ? 18 * (gq >> 1) + 8 * (j >> 2) + 4 * (gq & 1) : 18 * (j >> 2) + 4 * gq) + (j & 3);
    const int c = 36 * (8 * (s - 16) + 2 * gq + (j >> 2)) + 18 * ((j >> 1) & 1) + 16 + (j & 1);
    return s < 16 ? a : c;
}
DEVI int fresh(int v) { asm volatile("" : "+v"(v)); return v; }
DEVI int sfresh(int v) { asm volatile("" : "+s"(v)); return v; }

template <int T0, int NT, int S0, int S1, bool SSQ, int D, class F>
DEVI void gemm_img(f32x4 (&acc)[7], u32x4 (&xf)[18], const unsigned (&lb)[3], f32x4& sq, F side) {
    u32x4 a[D][NT];
#pragma unroll
    for (int s = S0; s < S0 + D; ++s)
#pragma unroll
        for (int t = 0; t < NT; ++t) a[s % D][t] = lds_r128(lb[s / 6] + (T0 + t) * TKT + (s % 6) * 1024);
    SB();
#pragma unroll
    for (int s = S0; s < S1; ++s) {
        side(s);
        SB();
        if (SSQ) { sq = mfma32(xf[s], xf[s], sq); SB(); }
#pragma unroll
        for (int t = 0; t < NT; ++t) {
            acc[T0 + t] = mfma32(a[s % D][t], xf[s], acc[T0 + t]);
            SB();
            if (s + D < S1) { a[s % D][t] = lds_r128(lb[(s + D) / 6] + (T0 + t) * TKT + ((s + D) % 6) * 1024); SB(); }
        }
    }
}
template <bool SUM>
DEVI void put_frag(rsrc_t wsr, unsigned dst, unsigned ks, int t, u32x4 f, int lane) {
    st128c(wsr, lane * 16, dst, f);
    if (!SUM) return;
    f32x4 c = mfma32((u32x4){0x3F803F80u, 0x3F803F80u, 0x3F803F80u, 0x3F803F80u}, f, (f32x4){0.f, 0.f, 0.f, 0.f});
    asm volatile("s_nop 7" : "+v"(c));
    if (lane < 16) st32c(wsr, lane * 4, WS_CSP + ks * 448 + t * 64, u_of(c[0]));
}
DEVI void colsum_get(rsrc_t wsr, unsigned img_p, unsigned csb, int lane) {
    u32x2 v[18];
#pragma unroll
    for (int s = 0; s < 18; ++s) v[s] = __builtin_amdgcn_raw_buffer_load_b64(wsr, lane < 56 ? lane * 8 : 0, WS_CSP + (img_p * 18 + s) * 448, 16);
    SB();
    f32x2 c = __builtin_bit_cast(f32x2, v[0]);
#pragma unroll
    for (int s = 1; s < 18; ++s) c += __builtin_bit_cast(f32x2, v[s]);
    if (lane < 56) lds_w64(csb + ((lane >> 1) & 3) * 112 + (lane >> 3) * 16 + (lane & 1) * 8, __builtin_bit_cast(u32x2, c));
}
DEVI void colsum_tile(int t, unsigned csb, int lane) {
    f32x4 c = {0.f, 0.f, 0.f, 0.f}; const u32x4 ones = {0x3F803F80u, 0x3F803F80u, 0x3F803F80u, 0x3F803F80u};
    u32x4 a[18];
#pragma unroll
    for (int s = 0; s < 18; ++s) a[s] = lds_r128(t * TKT + s * 1024 + lane * 16);
    SB();
#pragma unroll
    for (int s = 0; s < 18; ++s) c = mfma32(a[s], ones, c);
    asm volatile("s_nop 7" : "+v"(c));
    if ((lane & 15) == 0) lds_w128(csb + ((lane >> 4) * 28 + 4 * t) * 4, __builtin_bit_cast(u32x4, c));
}
DEVI void dual_softmax(const float (&t)[9], float ks, bool lo, u32x4& fa, u32x4& fb, unsigned& fe) {
    const float zA = lo ? t[8] : t[0], zB = lo ? t[4] : t[8];
    const float mA = max3f(max3f(t[0], t[1], t[2]), t[3], zA), nA = min3f(min3f(t[0], t[1], t[2]), t[3], zA);
    const float mB = max3f(max3f(t[4], t[5], t[6]), t[7], zB), nB = min3f(min3f(t[4], t[5], t[6]), t[7], zB);
    const float T = -ks * vmax(sw32(vmax(sw16(mA, mB)), vmaxn(sw16(nA, nB))));
    const f32x2 u = sw16(T, T);
    const f32x2 sa = sw32(u.x, u.x), sb = sw32(u.y, u.y);
    const f32x2 s8 = lo ? sa : sb, k2 = {ks, -ks};
    f32x2 e[9];
#pragma unroll
    for (int i = 0; i < 9; ++i) { const f32x2 z = fma2(sp2(t[i]), k2, i < 4 ? sa : i < 8 ? sb : s8); e[i] = (f32x2){ex2(z.x), ex2(z.y)}; }
    const float wl = lo ? 1.f : 0.f;
    const f32x2 sA = fma2(e[8], sp2(wl), (e[0] + e[1]) + (e[2] + e[3])), sB = fma2(e[8], sp2(1.f - wl), (e[4] + e[5]) + (e[6] + e[7]));
    const f32x2 p = sw16(sA.x, sB.x), q = sw16(sA.y, sB.y), w = sw32(p.x + p.y, q.x + q.y);
    const float R = rcp(w.x + w.y);
    const f32x2 v = sw16(R, R);
    const f32x2 ra = sw32(v.x, v.x), rb = sw32(v.y, v.y), r8 = lo ? ra : rb;
    fa = (u32x4){pkv(e[0] * ra), pkv(e[1] * ra), pkv(e[2] * ra), pkv(e[3] * ra)};
    fb = (u32x4){pkv(e[4] * rb), pkv(e[5] * rb), pkv(e[6] * rb), pkv(e[7] * rb)};
    fe = pkv(e[8] * r8);
}

DEVI f32x16 att_scores(u32x4 km, u32x4 kl, u32x4 q0, u32x4 q1, f32x16 c) { return mfmaL(kl, q1, mfmaL(km, q0, c)); }
DEVI u32x4 att_p(const f32x16& s, int b) { return (u32x4){pk2(ex2(s[b]), ex2(s[b + 1])), pk2(ex2(s[b + 2]), ex2(s[b + 3])), pk2(ex2(s[b + 4]), ex2(s[b + 5])), pk2(ex2(s[b + 6]), ex2(s[b + 7]))}; }
DEVI void att_accum(const f32x16& s, const u32x2 (&v)[4], f32x16& o) {
    const u32x4 p0 = {pk2(ex2(s[0]), ex2(s[1])), pk2(ex2(s[2]), ex2(s[3])), pk2(ex2(s[4]), ex2(s[5])), pk2(ex2(s[6]), ex2(s[7]))};
    o = mfmaL((u32x4){v[0].x, v[0].y, v[1].x, v[1].y}, p0, o);
    const u32x4 p1 = {pk2(ex2(s[8]), ex2(s[9])), pk2(ex2(s[10]), ex2(s[11])), pk2(ex2(s[12]), ex2(s[13])), pk2(ex2(s[14]), ex2(s[15]))};
    o = mfmaL((u32x4){v[2].x, v[2].y, v[3].x, v[3].y}, p1, o);
}
DEVI void att_vload(u32x2 (&v)[4], unsigned va) {
#pragma unroll
    for (int e = 0; e < 4; ++e) v[e] = lds_tr(va + e * 384);
}
DEVI float max16(const f32x16& s, float m) {
#pragma unroll
    for (int v = 0; v < 16; v += 2) m = max3f(m, s[v], s[v + 1]);
    return m;
}
DEVI f32x16 splat16(float v) { f32x16 r;
#pragma unroll
    for (int i = 0; i < 16; ++i) r[i] = v;
    return r; }

struct Ctx { int wave, g, p; unsigned tag; };

template <bool ISB>
DEVI void layer_fn(const Params& P, const rsrc_t wsr, const int lane_in, const Ctx& C, const int it, u32x4 (&xf)[18], f32x4 (&res)[3], f32x4 (&acc)[7], float& ssq) {
    const int wave = sfresh(C.wave), g = sfresh(C.g), p = sfresh(C.p), cu = g * 16 + p;
    const unsigned tag = C.tag;
    const int layer = 2 * it + (ISB ? 1 : 0);
    const int tp = (p >> 1) & 3, cg = (p >> 3) * 2 + (p & 1);
    const f32x4 Z4 = {0.f, 0.f, 0.f, 0.f};
    constexpr unsigned csb = L_CS + (ISB ? 448 : 0);
    const bool first = !ISB && it == 0, last = ISB && it == 1;
    u32x4 wof[18];
    float mean, rstd;
    const unsigned wob = WS_WOI + ((ISB ? 36 : 0) + 9 * cg + wave) * TKT;
    const unsigned w1k = wave * 1024;
    const unsigned kbase = ISB ? (it ? ws_k(3) : ws_k(1)) : (it ? ws_k(2) : ws_k(0)), obase = kbase + (ISB ? KVH_BYTES : KVP_BYTES);
    const int rot = first ? 0 : p % 3;
    const unsigned ro0 = rot * 6144, ro1 = rot == 2 ? 0 : ro0 + 6144, ro2 = rot ? ro0 - 6144 : 12288;
    const unsigned char* nimg = P.ws + (ISB ? WS_IMA : WS_IMB) + p * IMG_BYTES + w1k;
    {
        const int lane = fresh(lane_in), gq = lane >> 4, tk = lane & 15;
        const unsigned l16 = lane * 16;
        const unsigned lb[3] = {l16 + ro0, l16 + ro1, l16 + ro2};
        if (!first) {
#pragma unroll
            for (int t = 0; t < 5; ++t) acc[t] = Z4;
            const unsigned xprev = (ISB ? (it ? ws_o(2) : ws_o(0)) : ws_o(1)) + ACT_BYTES;
            const unsigned xb = xprev + (g * 8 + wave) * TKT, sb = xprev + ACT_BYTES + (g * 8 + wave) * 2304;
            unsigned st[9];
            auto xload = [&](int s0, int s1) {
#pragma unroll
                for (int s = s0; s < s1; ++s) xf[s] = ld128c(wsr, l16 + (s % 6) * 1024, xb + (s < 6 ? ro0 : s < 12 ? ro1 : ro2));
            };
            auto xmax = [&](int s0, int s1) { unsigned m = 0u;
#pragma unroll
                for (int s = s0; s < s1; ++s) m = umax4(m, xf[s]);
                return m; };
            auto sload = [&]() {
#pragma unroll
                for (int j = 0; j < 9; ++j) st[j] = __builtin_amdgcn_raw_buffer_load_b32(wsr, lane * 4 + j * 256, sb, 16);
            };
            asm volatile("s_waitcnt vmcnt(2)" ::: "memory");
            BARRIER();
            if (!CSPB && ISB && it == 0) { if (wave < 5) colsum_tile(wave, csb, lane); } else
            __builtin_amdgcn_s_sleep(XSLEEP);
            sload(); xload(0, 3);
            RETRY(layer * 8 + 3, { sload(); xload(0, 3); }, umax3(umax3(umax3(umax3(umax3(xmax(0, 3), st[0], st[1]), st[2], st[3]), st[4], st[5]), st[6], st[7]), st[8], 0u));
            STAMP(layer * 12 + 2);
            xload(3, 9);
            ssq = ((f_of(st[0]) + f_of(st[1])) + (f_of(st[2]) + f_of(st[3]))) + ((f_of(st[4]) + f_of(st[5])) + (f_of(st[6]) + f_of(st[7]))) + f_of(st[8]);
            if (!CSPB && ISB && it == 0) BARRIER();
            const unsigned char* cimg = P.ws + (ISB ? WS_IMB : WS_IMA) + p * IMG_BYTES + w1k;
            f32x4 dummy;
            gemm_img<0, 5, 0, 18, false, 2>(acc, xf, lb, dummy, [&](int s) {
                if (s < 3) xload(9 + 2 * s, 11 + 2 * s);
                if (s == 3) RETRY(layer * 8 + 1, xload(3, 9), xmax(3, 9));
                if (s >= 3 && s < 6) xload(12 + s, 13 + s);
                if (s == 9) RETRY(layer * 8 + 2, xload(9, 18), xmax(9, 18));
                if (s >= 9 && s < 14 && (s < 13 || wave < 4)) dma1k(cimg + (90 + 8 * (s - 9)) * 1024, l16, w1k + (90 + 8 * (s - 9)) * 1024);
                if (!ISB && s >= 9) wof[s] = ld128c(wsr, l16 + s * 1024, wob);
                if (ISB && it == 0 && s >= 14 && s < 17) arm_unit(wsr, l16, 63 + (s - 14) * 8 + wave, cu);
                if (!ISB && s >= 14 && (s < 17 || wave < 4)) arm_unit(wsr, l16, 87 + (s - 14) * 8 + wave, cu);
            });
        }
        f32x4 cs[5];
#pragma unroll
        for (int t = 0; t < 5; ++t) cs[t] = lds_rf4(csb + gq * 112 + 16 * t);
        acc[5] = Z4; acc[6] = Z4;
        STAMP(layer * 12 + 3);
        const f32x2 r1 = sw16(ssq, ssq); const float h1 = r1.x + r1.y; const f32x2 r2 = sw32(h1, h1);
        asm volatile("s_nop 7" : "+v"(acc[4]));
        mean = acc[4][2] * (1.f / DM);
        rstd = __builtin_amdgcn_rsqf(__builtin_fmaxf((r2.x + r2.y) * (1.f / DM) - mean * mean, 0.f) + 1e-5f);
        const int hx = gq >> 1, od = gq & 1;
        f32x2 y[9];
        {
            const f32x2 nm = sp2(-mean);
#pragma unroll
            for (int t = 0; t < 5; ++t) {
                y[2 * t] = fma2(nm, (f32x2){cs[t][0], cs[t][1]}, (f32x2){acc[t][0], acc[t][1]});
                if (t < 4) y[2 * t + 1] = fma2(nm, (f32x2){cs[t][2], cs[t][3]}, (f32x2){acc[t][2], acc[t][3]});
            }
        }
        const u32x4 vm = unit_pair(pk2(y[4].y * rstd, y[5].x * rstd), pk2(y[5].y * rstd, y[6].x * rstd), pk2(y[6].y * rstd, y[7].x * rstd), pk2(y[7].y * rstd, y[8].x * rstd));
        const float v8 = y[8].y * rstd;
        const unsigned vl = pkv(sw16(v8, v8));
        if (ISB) {
            const f32x2 r2 = sp2(rstd);
            const u32x4 km = unit_pair(pkv(y[0] * r2), pkv(y[1] * r2), pkv(y[2] * r2), pkv(y[3] * r2));
            const float k8 = y[4].x * rstd;
            const u32x4 lr = {ok1(pkv(sw16(k8, k8))), 0u, ok1(vl), 0x3F80u};
            const unsigned tb = (wave >> 1) * 5120 + (wave & 1) * 256, tc = (wave >> 1) * 5120 + (wave & 1) * 768 + 1024;
            const unsigned oc = (tk >> 3) * 384 + (tk & 7) * 16;
            const unsigned a0 = od * 2560 + hx * 512 + tk * 16, a1 = od * 2560 + hx * 128 + oc, a2 = hx * 2560 + 256 + (oc ^ 64);
            const unsigned hb = kbase + cu * 20480;
            st128c(wsr, a0, hb + tb, ok4(km)); st128c(wsr, a1, hb + tc, ok4(vm));
            lds_w128(L_STG + tb + a0, km); lds_w128(L_STG + tc + a1, vm);
            if (od == 0) { st128c(wsr, a2, hb + tc, lr); lds_w128(L_STG + tc + a2, lr); }
            asm volatile("s_waitcnt vmcnt(3)" ::: "memory");
            BARRIER();
            if (it == 0 && wave == 7 && lane == 0) set_flag(P.ws, FL_W4, cu, tag);
        } else {
            const float kz[9] = {y[0].x, y[0].y, y[1].x, y[1].y, y[2].x, y[2].y, y[3].x, y[3].y, y[4].x};
            u32x4 fa, fb; unsigned fe;
            dual_softmax(kz, rstd * LOG2E, gq < 2, fa, fb, fe);
            const unsigned pk = L_PK + wave * 1280 + tk * 80, vv = L_VV + wave * 768 + tk * 48;
            lds_w128(pk + gq * 16, fa); lds_w128(pk + gq * 16 + 10240, fb); lds_w32(pk + hx * 10240 + 64 + od * 4, fe);
            lds_w128(vv + od * 6144 + hx * 16, vm);
            lds_w64(vv + hx * 6144 + 32, (u32x2){vl, 0x3F80u});
            STAMP(53 + it);
            if (first && wave == 3) wait_all256(flag_at(P.ws, FL_W1, 0), lane, tag);
            DRAIN();
            BARRIER();
            if (wave == 7 && lane == 0) set_flag(P.ws, it ? FL_W5 : FL_W2, cu, tag);
            STAMP(55 + it);
            if (wave < 6) {
                const int h = wave >= 3, ft = wave - 3 * h;
                const unsigned trow = (unsigned)(lane >> 2), tcol = (unsigned)(lane & 3) * 8u;
                const unsigned pa = L_PK + h * 10240 + ft * 32 + trow * 80 + tcol, pb = L_VV + h * 6144 + trow * 48 + tcol;
                u32x2 ta[8], tb[8], td[8];
#pragma unroll
                for (int s = 0; s < 8; ++s) { ta[s] = lds_tr(pa + (16 * s) * 80); tb[s] = lds_tr(pb + (16 * s) * 48); td[s] = lds_tr(pb + (16 * s) * 48 + 32); }
                SB();
                f32x4 k0 = Z4, k1 = Z4;
#pragma unroll
                for (int s = 0; s < 4; ++s) {
                    const u32x4 af = {ta[2 * s].x, ta[2 * s].y, ta[2 * s + 1].x, ta[2 * s + 1].y};
                    k0 = mfma32((u32x4){tb[2 * s].x, tb[2 * s].y, tb[2 * s + 1].x, tb[2 * s + 1].y}, af, k0);
                    k1 = mfma32((u32x4){td[2 * s].x, td[2 * s].y, td[2 * s + 1].x, td[2 * s + 1].y}, af, k1);
                }
                const u32x2 s0 = ok2((u32x2){pk2(k0[0], k0[1]), pk2(k0[2], k0[3])}), s1 = ok2((u32x2){pk2(k1[0], k1[1]), pk2(k1[2], k1[3])});
                const unsigned kb = kbase + (p * 16 + g) * 3072 + h * 1536;
                if (ft < 2) {
                    st64c(wsr, lane * 8, kb + ft * 512, s0);
                    if (gq == 0) st64c(wsr, lane * 8, kb + 1024 + ft * 128, s1);
                } else if (tk < 4) {
                    st64c(wsr, gq * 32 + tk * 8, kb + 1280, s0);
                    if (gq == 0) st64c(wsr, tk * 8, kb + 1408, s1);
                }
            }
        }
        STAMP(layer * 12 + 0 + (layer == 0 ? 52 : 0));
        if (!(ISB ? CSPB : CSPA) && it == 0 && wave >= 6) colsum_tile(wave - 1, csb, lane);
        STAMP(layer * 12 + 4);
    }
    {
        const int lane = fresh(lane_in), gq = lane >> 4, tk = lane & 15;
        const unsigned l16 = lane * 16;
        const unsigned lb[3] = {l16 + ro0, l16 + ro1, l16 + ro2};
        f32x16 msk;
        const int dl = (lane & 31) - 4 * (lane >> 5);
        { f32x4 dummy; gemm_img<5, 2, 0, 18, false, 3>(acc, xf, lb, dummy, [&](int s) {
              if (!ISB && (s & 1) == 0) wof[s >> 1] = ld128c(wsr, l16 + (s >> 1) * 1024, wob);
              if (ISB && s < 16) msk[s] = ((s & 3) + 8 * (s >> 2) > dl) ? NEGBIG : 0.f;
              if ((s & 1) && first) wof[9 + (s >> 1)] = ld128c(wsr, l16 + (9 + (s >> 1)) * 1024, wob); }); }
        if ((!(ISB ? CSPB : CSPA) && it == 0) || (BAR2 && !ISB)) BARRIER();
        float q[9];
        {
            const unsigned ca = csb + gq * 112;
            const float c19 = f_of(lds_r32(ca + 76));
            const f32x4 c5 = lds_rf4(ca + 80), c6 = lds_rf4(ca + 96);
            q[0] = acc[4][3] - mean * c19;
#pragma unroll
            for (int i = 0; i < 4; ++i) { q[1 + i] = acc[5][i] - mean * c5[i]; q[5 + i] = acc[6][i] - mean * c6[i]; }
        }
        STAMP(layer * 12 + 5);
        if (ISB) {
            u32x4 hv[5];
            const int src = g - 2 + (wave >> 2);
            auto hload = [&]() {
                const unsigned hb = kbase + (src * 16 + p) * 20480 + (wave & 3) * 5120;
#pragma unroll
                for (int i = 0; i < 5; ++i) hv[i] = ld128c(wsr, l16 + i * 1024, hb);
            };
            if (src >= 0) {
                hload();
            } else {
#pragma unroll
                for (int i = 0; i < 5; ++i) hv[i] = (u32x4){0u, (i == 0 || (i == 2 && lane >= 32) || (i == 3 && lane < 32)) ? 0u : 0xF149u, 0u, 0u};
            }
            {
                const float qs = rstd * (0.23570226039551584f * LOG2E);
                const int hx = gq >> 1, od = gq & 1;
                const unsigned qb = L_QS + (wave >> 1) * 2304;
                lds_w128(qb + (wave & 1) * 256 + od * 1152 + hx * 512 + tk * 16, unit_pair(pk2(q[0] * qs, q[1] * qs), pk2(q[2] * qs, q[3] * qs), pk2(q[4] * qs, q[5] * qs), pk2(q[6] * qs, q[7] * qs)));
                const float q8 = q[8] * qs;
                lds_w32(qb + (wave & 1) * 64 + 1024 + hx * 1152 + tk * 4, pkv(sw16(q8, q8)));
            }
            const int h = wave & 1, jq = wave < 4 ? (wave >> 1) : 3 - ((wave - 4) >> 1);
            const int r = lane & 31, hh = lane >> 5;
            const unsigned kb = L_KT + h * 2560 + jq * 5120;
            const unsigned ka = kb + l16, la = kb + 1280 + (r >> 3) * 384 + ((r & 7) ^ 4) * 16;
            const unsigned k8 = 4 * hh + ((lane >> 2) & 3);
            const unsigned va = kb + 1024 + ((lane & 16) ? 256 + (k8 ^ 4) * 16 + 8 : (lane & 2) * 64 + k8 * 16 + (lane & 1) * 8);
            BARRIER();
            const u32x4 q0 = lds_r128(L_QS + (jq * 2 + h) * 1152 + l16);
            const unsigned q1w = lds_r32(L_QS + (jq * 2 + h) * 1152 + 1024 + r * 4);
            u32x4 km = lds_r128(ka + 8 * 5120), kl = lds_r128(la + 8 * 5120);
            u32x2 vf[2][4];
            att_vload(vf[0], va + 8 * 5120);
            STAMP(layer * 12 + 1);
            const u32x4 q1 = {hh ? 0u : q1w, hh ? 0u : 0x3F80u, 0u, 0u};
            f32x16 sc[2], negm, o16 = splat16(0.f);
            sc[0] = att_scores(km, kl, q0, q1, msk);
            {
                const float m = max16(sc[0], NEGBIG);
                const float nm = -vmax(sw32(m, m));
                negm = splat16(nm);
                sc[0] += negm;
                msk = (splat16(NEGBIG) - msk) + negm;
            }
#pragma unroll
            for (int i = 8; i >= 0; --i) {
                if (i >= 5 && i == 8 - jq) {
                    RETRY(layer * 8 + 4, hload(), umax4(umax4(umax4(umax4(umax4(0u, hv[0]), hv[1]), hv[2]), hv[3]), hv[4]));
#pragma unroll
                    for (int j = 0; j < 5; ++j) lds_w128(L_KT + wave * 5120 + j * 1024 + l16, hv[j]);
                    BARRIER();
                    STAMP(layer * 12 + 6);
                }
                SB();
                if (i > 0) { km = lds_r128(ka + (i - 1) * 5120); kl = lds_r128(la + (i - 1) * 5120); att_vload(vf[(i - 1) & 1], va + (i - 1) * 5120); }
                wof[16 - 2 * i] = ld128c(wsr, l16 + (16 - 2 * i) * 1024, wob); wof[17 - 2 * i] = ld128c(wsr, l16 + (17 - 2 * i) * 1024, wob);
                SB();
                const u32x4 p0 = att_p(sc[i & 1], 0);
                SB();
                if (i > 0) sc[(i - 1) & 1] = att_scores(km, kl, q0, q1, i == 1 ? msk : negm);
                o16 = mfmaL((u32x4){vf[i & 1][0].x, vf[i & 1][0].y, vf[i & 1][1].x, vf[i & 1][1].y}, p0, o16);
                SB();
                const u32x4 p1 = att_p(sc[i & 1], 8);
                if (i < 7 && !last) {
                    if (i > 0 || wave < 6) dma1k(nimg + 8 * (6 - i) * 1024, l16, w1k + 8 * (6 - i) * 1024);
                }
                SB();
                o16 = mfmaL((u32x4){vf[i & 1][2].x, vf[i & 1][2].y, vf[i & 1][3].x, vf[i & 1][3].y}, p1, o16);
            }
            asm volatile("s_nop 7\n\ts_nop 7" : "+v"(o16));
            {
                float t = 0.f;
#pragma unroll
                for (int v = 0; v < 11; ++v) t += __builtin_fabsf(o16[v]);
                if (__any(!(t < 3.0e38f))) {
                    float m = NEGBIG;
#pragma unroll 1
                    for (int pass = 0; pass < 2; ++pass) {
                        o16 = splat16(0.f);
#pragma unroll 1
                        for (int i = 0; i <= 8; ++i) {
                            u32x2 v4[4]; att_vload(v4, va + i * 5120);
                            f32x16 s = att_scores(lds_r128(ka + i * 5120), lds_r128(la + i * 5120), q0, q1, splat16(pass ? -m : 0.f));
                            const int lim = i == 8 ? dl : 99, lim2 = i == 0 ? dl : -99;
#pragma unroll
                            for (int v = 0; v < 16; ++v) { const int cv = (v & 3) + 8 * (v >> 2); if (cv > lim || cv <= lim2) s[v] = NEGBIG; }
                            if (pass) att_accum(s, v4, o16); else m = max16(s, m);
                        }
                        if (!pass) m = vmax(sw32(m, m));
                    }
                    asm volatile("s_nop 7\n\ts_nop 7" : "+v"(o16));
                }
            }
            STAMP(layer * 12 + 7);
            const float inv = rcp(o16[10]);
            const unsigned ob = obase + (g * 8 + 2 * jq) * TKT;
            const unsigned vo = (r >> 4) * TKT + (r & 15) * 16;
            st128c(wsr, vo + hh * 256, ob + p * 1024 + h * 512, ok4((u32x4){pk2(o16[0] * inv, o16[1] * inv), pk2(o16[2] * inv, o16[3] * inv), pk2(o16[4] * inv, o16[5] * inv), pk2(o16[6] * inv, o16[7] * inv)}));
            if (hh == 0) st32c(wsr, (r >> 4) * TKT + (r & 15) * 4, ob + 16384 + p * 128 + h * 64, ok1(pk2(o16[8] * inv, o16[9] * inv)));
        } else {
            const unsigned c16 = (lane & 31) * 16;
            u32x4 pv[8];
            auto pload = [&]() {
                const unsigned kb = kbase + p * 16 * 3072 + (wave < 6 ? wave : 0) * 512;
#pragma unroll
                for (int i = 0; i < 8; ++i) pv[i] = ld128c(wsr, c16 + (lane >> 5) * (8 * 3072), kb + i * 3072);
            };
            if (wave < 6) pload();
            u32x4 fq[2]; unsigned fe;
            dual_softmax(q, rstd * LOG2E, gq < 2, fq[0], fq[1], fe);
            const unsigned q4 = (lane >> 2) & 3, c4 = lane & 3, fm = (gq & 1) * 8 + q4, fx = (gq & 1) * 2 + (q4 & 1);
            const unsigned ra = L_KVS + (gq >> 1) * 512 + c4 * 128 + fm * 8, rb = L_KVS + 1024 + (gq >> 1) * 128 + fm * 8;
            const unsigned rc = L_KVS + 1280 + c4 * 32 + fx * 8, rd = L_KVS + 1408 + fx * 8;
            if (wave < 6) {
                STAMP(layer * 12 + 6);
                const unsigned keep = (c16 >= 416 && (wave == 2 || wave == 5)) ? 0u : ~0u;
                RETRY(layer * 8 + 4, pload(), keep & umax4(umax4(umax4(umax4(umax4(umax4(umax4(umax4(0u, pv[0]), pv[1]), pv[2]), pv[3]), pv[4]), pv[5]), pv[6]), pv[7]));
                float sm[8];
                const unsigned clo = (unsigned)sfresh(0x3F80), chi = (unsigned)sfresh(0x3F800000);
#pragma unroll
                for (int e = 0; e < 8; ++e) sm[e] = 0.f;
#pragma unroll
                for (int i = 0; i < 8; ++i) {
#pragma unroll
                    for (int w = 0; w < 4; ++w) { sm[2 * w] = dot2(clo, pv[i][w], sm[2 * w]); sm[2 * w + 1] = dot2(chi, pv[i][w], sm[2 * w + 1]); }
                }
                const f32x2 a0 = sw32(sm[0], sm[4]), a1 = sw32(sm[1], sm[5]), a2 = sw32(sm[2], sm[6]), a3 = sw32(sm[3], sm[7]);
                lds_w64(L_KVS + wave * 512 + c16 + (lane >> 5) * 8, (u32x2){pk2(a0.x + a0.y, a1.x + a1.y), pk2(a2.x + a2.y, a3.x + a3.y)});
            }
            else if (wave == 7) wait_all256(flag_at(P.ws, it ? FL_W5 : FL_W2, 0), lane, tag);
            else if (CSPB && first) colsum_get(wsr, 16 + p, L_CS + 448, lane);
            STAMP(57 + it);
            BARRIER();
            f32x4 oa[2][2];
            {
                u32x2 am[2][4], ae[2][2];
#pragma unroll
                for (int h = 0; h < 2; ++h) {
                    am[h][0] = lds_tr(ra + h * 1536); am[h][1] = lds_tr(ra + h * 1536 + 32); am[h][2] = lds_tr(rb + h * 1536); am[h][3] = lds_tr(rb + h * 1536 + 32);
                    ae[h][0] = lds_tr(rc + h * 1536); ae[h][1] = lds_tr(rd + h * 1536);
                }
                SB();
#pragma unroll
                for (int h = 0; h < 2; ++h) {
                    oa[h][0] = mfma32((u32x4){am[h][0].x, am[h][0].y, am[h][1].x, am[h][1].y}, fq[h], Z4);
                    oa[h][1] = mfma32((u32x4){am[h][2].x, am[h][2].y, am[h][3].x, am[h][3].y}, fq[h], Z4);
                }
#pragma unroll
                for (int h = 0; h < 2; ++h) {
                    const u32x4 fh = w1((gq >> 1) == h ? fe : 0u);
                    oa[h][1] = mfma32(w1(ae[h][1].x), fh, oa[h][1]); oa[h][0] = mfma32(w1(ae[h][0].x), fh, oa[h][0]);
                }
            }
            STAMP(layer * 12 + 7);
            const float ia = rcp(oa[0][1][2] + 1e-6f), ib = rcp(oa[1][1][2] + 1e-6f);
            const unsigned ob = obase + (g * 8 + wave) * TKT;
            st128c(wsr, l16, ob + p * 1024, ok4((u32x4){pk2(oa[0][0][0] * ia, oa[0][0][1] * ia), pk2(oa[0][0][2] * ia, oa[0][0][3] * ia),
                                                        pk2(oa[1][0][0] * ib, oa[1][0][1] * ib), pk2(oa[1][0][2] * ib, oa[1][0][3] * ib)}));
            const unsigned lv = (gq & 1) ? pk2(oa[1][1][0] * ib, oa[1][1][1] * ib) : pk2(oa[0][1][0] * ia, oa[0][1][1] * ia);
            if (gq < 2) st32c(wsr, lane * 4, ob + 16384 + p * 128, ok1(lv));
        }
    }
    {
        const int lane = fresh(lane_in), gq = lane >> 4, tk = lane & 15;
        const unsigned l16 = lane * 16;
        BARRIER();
        STAMP(59 + layer);
        u32x4 wox[18];
        if (wave >= 6) {
            const unsigned wb = WS_WOI + ((ISB ? 36 : 0) + 9 * cg + 8) * TKT;
#pragma unroll
            for (int s = 0; s < 18; ++s) wox[s] = ld128c(wsr, l16 + s * 1024, wb);
            if (ISB && it == 0 && wave == 7) wait_all256(flag_at(P.ws, FL_W4, 0), lane, tag);
        } else {
            const unsigned ob = obase + (g * 8 + 2 * tp) * TKT + w1k;
            u32x4 ov[6];
            auto oload = [&]() {
#pragma unroll
                for (int i = 0; i < 6; ++i) ov[i] = ld128c(wsr, l16 + i * 6144, ob);
            };
            oload();
            RETRY(layer * 8 + 5, oload(), umax4(umax4(umax4(umax4(umax4(umax4(0u, ov[0]), ov[1]), ov[2]), ov[3]), ov[4]), ov[5]));
            STAMP(layer * 12 + 8);
#pragma unroll
            for (int i = 0; i < 6; ++i) {
                if ((i == 2 || i == 5) && wave >= 4) {
                    const unsigned d = L_OST + w1k + i * 6144 + (lane >> 4) * 256 + (lane & 3) * 64 + (lane & 12);
                    lds_w32(d, ov[i].x); lds_w32(d + 16, ov[i].y); lds_w32(d + 32, ov[i].z); lds_w32(d + 48, ov[i].w);
                } else lds_w128(L_OST + w1k + i * 6144 + l16, ov[i]);
            }
        }
        if (first && blockIdx.x == 0 && wave == 0 && lane == 0) __hip_atomic_store((GAS unsigned*)P.ws, tag, RLX_AGENT);
        BARRIER();
        STAMP(layer * 12 + 9);
        {
            f32x4 c0 = res[0], c1 = res[1], c2 = res[2];
            const unsigned lo = L_OST + l16;
            if (wave >= 6) {
                u32x4 ox[18];
#pragma unroll
                for (int s = 0; s < 18; ++s) ox[s] = lds_r128(lo + (wave & 1) * TKT + s * 1024);
                SB();
#pragma unroll
                for (int s = 0; s < 18; ++s) c2 = mfma32(wox[s], ox[s], c2);
            }
            SB();
            u32x4 of[4][2];
#pragma unroll
            for (int s = 0; s < 3; ++s) { of[s][0] = lds_r128(lo + s * 1024); of[s][1] = lds_r128(lo + TKT + s * 1024); }
#pragma unroll
            for (int s = 0; s < 18; ++s) {
                SB();
                c0 = mfma32(wof[s], of[s & 3][0], c0);
                SB();
                if (s + 3 < 18) of[(s + 3) & 3][0] = lds_r128(lo + (s + 3) * 1024);
                SB();
                c1 = mfma32(wof[s], of[s & 3][1], c1);
                SB();
                if (s + 3 < 18) of[(s + 3) & 3][1] = lds_r128(lo + TKT + (s + 3) * 1024);
                if (ISB) { if ((s & 1) == 0 && s < 10 && !last && (s < 8 || wave < 4)) dma1k(nimg + (54 + 4 * s) * 1024, l16, w1k + (54 + 4 * s) * 1024); }
                else if (s < 12 && (s < 11 || wave < 2)) dma1k(nimg + 8 * s * 1024, l16, w1k + 8 * s * 1024);
            }
            res[0] = c0; res[1] = c1; res[2] = c2;
            STAMP(layer * 12 + 10);
            const u32x4 v = unit_pair(pk2(c0[0], c0[1]), pk2(c0[2], c0[3]), pk2(c1[0], c1[1]), pk2(c1[2], c1[3]));
            const u32x2 v2 = {pk2(c2[0], c2[1]), pk2(c2[2], c2[3])};
            const int ct = 9 * cg + wave, cx = 9 * cg + 8;
            const int tx = 2 * tp + (wave & 1);
            if (!last) {
                const unsigned xb = obase + ACT_BYTES + g * 8 * TKT, sb = obase + 2 * ACT_BYTES + g * 8 * 2304;
                const unsigned vo = (gq >> 1) * 256 + tk * 16;
                st128c(wsr, (gq & 1) * TKT + vo, xb + 2 * tp * TKT + ct * 512, ok4(v));
                const f32x2 a0 = {c0[0], c0[1]}, a1 = {c0[2], c0[3]}, b0 = {c1[0], c1[1]}, b1 = {c1[2], c1[3]};
                const f32x2 qa = fma2(a1, a1, a0 * a0), qb = fma2(b1, b1, b0 * b0);
                const f32x2 u = sw16(qa.x + qa.y, qb.x + qb.y); const float u1 = u.x + u.y; const f32x2 u2 = sw32(u1, u1);
                if (gq < 2) st32c(wsr, gq * 2304 + tk * 4, sb + 2 * tp * 2304 + ct * 64, ok1(u_of(u2.x + u2.y)));
                if (wave >= 6) {
                    st64c(wsr, vo + (gq & 1) * 8, xb + tx * TKT + cx * 512, ok2(v2));
                    const f32x2 d0 = {c2[0], c2[1]}, d1 = {c2[2], c2[3]}, qd = fma2(d1, d1, d0 * d0);
                    const float e0 = qd.x + qd.y; const f32x2 e1 = sw16(e0, e0); const float e2 = e1.x + e1.y; const f32x2 e3 = sw32(e2, e2);
                    if (gq == 0) st32c(wsr, tk * 4, sb + tx * 2304 + cx * 64, ok1(u_of(e3.x + e3.y)));
                }

            } else {
                const int tt = 2 * tp + (gq & 1);
                *(u32x4*)(P.out + (size_t)(g * 128 + tt * 16 + tk) * DM + 16 * ct + 8 * (gq >> 1)) = v;
                if (wave >= 6) *(u32x2*)(P.out + (size_t)(g * 128 + tx * 16 + tk) * DM + 16 * cx + 4 * gq) = v2;
            }
        }
        STAMP(layer * 12 + 11);
    }
    if (ISB) {
#pragma unroll
        for (int s = 0; s < 18; ++s) asm volatile("" : "=v"(xf[s]));
#pragma unroll
        for (int t = 0; t < 5; ++t) asm volatile("" : "=v"(acc[t]));
        asm volatile("" : "=v"(ssq));
    }
}

__global__ void __launch_bounds__(512, 2) fused_kernel(Params P) {
    const int tid = threadIdx.x, lane = tid & 63, wave = __builtin_amdgcn_readfirstlane(tid >> 6);
    const int b = blockIdx.x, g = ((b >> 1) & 3) * 4 + (b >> 6), p = (b & 1) * 8 + ((b >> 3) & 7);
    const rsrc_t wsr = mk_rsrc(P.ws, WS_END);
    STAMP(0);
    if (lane < 3) {
        unsigned long long pc; unsigned keep;
        asm volatile("s_getpc_b64 %0" : "=s"(pc));
        const unsigned o = (((b >> 3) * 8 + wave) * 3 + lane) * 64;
        asm volatile("s_mov_b32 %0, m0\n\ts_mov_b32 m0, %3\n\ts_nop 0\n\tglobal_load_lds_dword %1, %2\n\ts_mov_b32 m0, %0" : "=&s"(keep) : "v"(o < CODE_BYTES ? o : 0u), "s"(pc), "s"(L_CS + 64 + wave * 16) : "memory");
    }
    u32x4 xf[18];
    f32x4 res[3];
    f32x4 acc[7], sq;
    float ssq;
    Ctx C; C.wave = wave; C.g = g; C.p = p;
    {
        const int gq = lane >> 4, tk = lane & 15;
        const f32x4 Z4 = {0.f, 0.f, 0.f, 0.f};
        const unsigned epv = __hip_atomic_load((const GAS unsigned*)P.ws, RLX_AGENT);
        const u16* xs = P.x + (size_t)(g * 128) * DM;
        u32x4 raw[8];
        unsigned ph0, ph1;
        const unsigned c0 = (unsigned)(gq == 1 ? 8 : gq == 2 ? 10 : 0);
        const unsigned wv = ((unsigned)tk * NQKV + c0) * 2u, wo = p * 36 + wave * 576;
        if (wave >= 4) {
            const unsigned xo = ((unsigned)(lane >> 3) * DM + 8u * ((lane & 7) ^ (lane >> 3))) * 2u;
            __builtin_amdgcn_s_sleep(DSLEEP);
#pragma unroll 1
            for (int j = 0; j < 4; ++j) {
                const unsigned oc = (wave - 4 + 4 * j + 2 * p) & 15;
                const u16* src = xs + oc * 8 * DM;
                unsigned dst = (oc >> 1) * TKT + (oc & 1) * 1024, keep, vo = xo;
                asm volatile("s_mov_b32 %0, m0\n\t"
                             ".rept 9\n\ts_mov_b32 m0, %1\n\ts_add_u32 %1, %1, 0x800\n\tglobal_load_lds_dwordx4 %2, %3\n\tv_add_u32 %2, 0x80, %2\n\t.endr\n\t"
                             "s_mov_b32 m0, %0" : "=&s"(keep), "+s"(dst), "+v"(vo) : "s"(src) : "memory", "scc");
            }
            STAMP(49);
        }
        SB();
        u32x4 im, ix, lraw[8];
        unsigned wdst = 0, km[2], kx;
        auto unit_tile = [&](const u32x4* raw, const u32x4 (&bm)[2], const u32x4 (&bl)[2], unsigned pb) {
            u32x2 lf[4];
#pragma unroll
            for (int i = 0; i < 4; ++i) {
                const f32x4 cm = mfma32(raw[i], bm[i >> 1], Z4), cl = mfma32(raw[i], bl[i >> 1], Z4);
                lds_w64(km[i >> 1] + pb + (i & 1) * 512, (u32x2){pk2(cm[0], cm[1]), pk2(cm[2], cm[3])});
                lf[i] = (u32x2){pk2(cl[0], cl[1]), pk2(cl[2], cl[3])};
            }
            if (tk < 2) {
#pragma unroll
                for (int i = 0; i < 4; ++i) lds_w64(kx + pb + (i >> 1) * 128 + (i & 1) * 512, lf[i]);
            }
        };
        auto meet = [&](unsigned word, unsigned val) {
            asm volatile("s_waitcnt lgkmcnt(0)" ::: "memory");
            if (lane == 0) lds_w32(word + wave * 4, val);
#pragma unroll 1
            for (unsigned sp = 0; sp < SPIN_CAP; ++sp) { asm volatile("" ::: "memory"); if (__all(lds_r32(word + (lane < 3 ? lane : 0) * 4) == val)) break; }
        };
        if (wave < 3) {
#pragma unroll
            for (int i = 0; i < 4; ++i) ld128_raw(raw[i], P.wqkvA + wo + g * (32 * NQKV) + (i & 1) * (16 * NQKV) + (i >> 1) * 18, wv);
#pragma unroll
            for (int i = 0; i < 4; ++i) ld128_raw(raw[4 + i], P.wqkvA + wo + (16 + (g & 1)) * (32 * NQKV) + (i & 1) * (16 * NQKV) + (i >> 1) * 18, g < 2 ? wv : 0u);
            asm volatile("global_load_dword %0, %2, %4\n\tglobal_load_dword %1, %3, %4" : "=&v"(ph0), "=&v"(ph1) : "v"(tid * 4u), "v"((tid < 132 ? tid + 192 : 0) * 4u), "s"(P.wphi + p * 648) : "memory");
            SB();
            { const int l2 = fresh(lane); im = onehot8(((l2 >> 3) & 1) == (l2 >> 4) ? (l2 & 7) : -2); ix = onehot8((l2 >> 4) == 2 ? 6 + (l2 & 1) : -2); }
            constexpr unsigned L_PHI = L_PST + 7168;
            unsigned pa[2];
#pragma unroll
            for (int i = 0; i < 2; ++i) {
                const int z = tid + 192 * i, row = z / 7, j = z - 7 * row;
                if (z < 252) lds_w32(L_PHI + row * 64 + (j < 3 ? 32 + 4 * j : 36 + 4 * j), 0u);
                const int e = 2 * z, hh = e >= 324, r = e - 324 * hh, d = r / 18, f = r - 18 * d;
                pa[i] = L_PHI + (unsigned)(hh * 18 + f) * 64u + (unsigned)(d < 16 ? d : d + 6) * 2u;
            }
            const unsigned sb = wave == 0 ? 19 : wave == 2 ? 9 : 0, sl = sb + 8;
            const unsigned ko = L_PST + (gq >> 1) * 256 + (gq & 1) * 8;
#pragma unroll
            for (int h = 0; h < 2; ++h) { const unsigned sm = sb + 4 * h + (tk & 3); km[h] = ko + (tk >> 2) * 64 + (sm >> 2) * 1024 + (sm & 3) * 16; }
            kx = ko + (tk & 1) * 64 + (sl >> 2) * 1024 + (sl & 3) * 16;
            const unsigned onea = L_PST + 4096 + (tk >> 2) * 256 + (4 * (tk & 3) + 2) * 16;
            const u32x4 one4 = {0x3F803F80u, 0x3F803F80u, 0x3F803F80u, 0x3F803F80u};
            if (wave == 2 && lane < 16) lds_w128(onea, one4);
            asm volatile("s_waitcnt vmcnt(0)" : "+v"(ph0), "+v"(ph1), "+v"(raw[0]), "+v"(raw[1]), "+v"(raw[2]), "+v"(raw[3]), "+v"(raw[4]), "+v"(raw[5]), "+v"(raw[6]), "+v"(raw[7]) :: "memory");
            lds_w16(pa[0], ph0); lds_w16(pa[0] + 64, ph0 >> 16);
            if (tid < 132) { lds_w16(pa[1], ph1); lds_w16(pa[1] + 64, ph1 >> 16); }
            const unsigned tag = __builtin_amdgcn_readfirstlane(epv) * 0x9E3779B1u + 0x7F4A7C15u;
            meet(L_CS + 32, tag);
            u32x4 bm[2] = {im, im}, bl[2] = {ix, ix};
            if (wave < 2) {
#pragma unroll
                for (int h = 0; h < 2; ++h) { bm[h] = lds_r128(L_PHI + (h * 18 + tk) * 64 + gq * 16); bl[h] = lds_r128(L_PHI + (h * 18 + 16 + (tk & 1)) * 64 + gq * 16); }
            }
            STAMP(48);
            unit_tile(raw, bm, bl, 0);
            meet(L_CS, tag);
            if (wave == 2 && lane < 16) lds_w128(onea + 7168, one4);
            if (g < 2) { unit_tile(raw + 4, bm, bl, 7168); meet(L_CS + 48, tag); }
            const unsigned o = WS_IMA + p * IMG_BYTES + wave * TKT, la = L_PST + wave * 1024 + lane * 16;
#pragma unroll
            for (int u = 0; u < 2; ++u) {
                if (u == 0 || g < 2) {
                    const unsigned oo = o + (u ? 16 + g : g) * 1024;
                    const unsigned ks = p * 18 + (u ? 16 + g : g);
                    put_frag<CSPA>(wsr, oo, ks, wave, lds_r128(la + u * 7168), lane); put_frag<CSPA>(wsr, oo + 3 * TKT, ks, wave + 3, lds_r128(la + u * 7168 + 3072), lane);
                    if (wave == 0) put_frag<CSPA>(wsr, oo + 6 * TKT, ks, 6, lds_r128(la + u * 7168 + 6144), lane);
                }
            }
            DRAIN();
            meet(L_CS + 16, tag);
            if (tid == 0) set_flag(P.ws, FL_IA, p * 32 + g, tag);
            STAMP(63);
            const rsrc_t rw = mk_rsrc(P.wqkvB, DM * NQKV * 2);
#pragma unroll
            for (int i = 0; i < 8; ++i) lraw[i] = __builtin_amdgcn_raw_buffer_load_b128(rw, (i < 4 || (g >> 1) == 1) ? wv : 0x7fffffffu,
                                                      (wo + (i < 4 ? g : 16 + (g & 1)) * (32 * NQKV) + (i & 1) * (16 * NQKV) + ((i >> 1) & 1) * 18) * 2, 0);
        } else {
            { const int l2 = fresh(lane); im = onehot8(((l2 >> 3) & 1) == (l2 >> 4) ? (l2 & 7) : -2); ix = onehot8(((l2 >> 3) & 1) + 2 == (l2 >> 4) ? (l2 & 7) : -2); }
#pragma unroll
            for (int i = 2; i < 8; ++i) lraw[i] = im;
            const int u = b + 256 * (wave - 3), m = u >= 324, r = u - 324 * m, cp = r / 18, s = r - 18 * cp;
            const unsigned w0 = (unsigned)(wo_krow(m, s, tk >> 2, tk & 3) * DM + 8 * gq) * 2u, w1o = (unsigned)(wo_krow(m, s, tk >> 2, 4 + (tk & 3)) * DM + 8 * gq) * 2u;
            const rsrc_t rw = mk_rsrc(m ? P.woB : P.woA, u < 648 ? DM * DM * 2 : 0);
            const unsigned tag = __builtin_amdgcn_readfirstlane(epv) * 0x9E3779B1u + 0x7F4A7C15u;
            lraw[0] = __builtin_amdgcn_raw_buffer_load_b128(rw, w0, 64 * cp, 0); lraw[1] = __builtin_amdgcn_raw_buffer_load_b128(rw, w1o, 64 * cp, 0);
            wdst = u < 648 ? WS_WOI + (unsigned)((m * 36 + 2 * cp) * 18 + s) * 1024u : 0u;
            asm volatile("s_waitcnt vmcnt(2)" ::: "memory");
            STAMP(48);
            if (wave == 3) wait_flags(flag_at(P.ws, FL_IA, p * 32), tk * 128, tag);
        }
        C.tag = __builtin_amdgcn_readfirstlane(epv) * 0x9E3779B1u + 0x7F4A7C15u;
        BARRIER();
        STAMP(50);
        {
            const unsigned xa = (tk >> 3) * 1024 + (tk & 7) * 128;
#pragma unroll
            for (int s = 0; s < 18; ++s) xf[s] = lds_r128(wave * TKT + (s >> 1) * 2048 + xa + (((4 * (s & 1) + gq) ^ (tk & 7)) * 16));
            const int tp = (p >> 1) & 3, cg = (p >> 3) * 2 + (p & 1);
#pragma unroll
            for (int i = 0; i < 3; ++i) {
                const int tt = 2 * tp + (i < 2 ? i : (wave & 1)), ct = 9 * cg + (i < 2 ? wave : 8);
                const u32x2 r = *(const LAS u32x2*)((LAS unsigned char*)smem + tt * TKT + (ct >> 2) * 2048 + xa + (((2 * (ct & 3) + (gq >> 1)) ^ (tk & 7)) * 16) + (gq & 1) * 8);
                res[i] = (f32x4){f_of(r.x << 16), f_of(r.x & 0xffff0000u), f_of(r.y << 16), f_of(r.y & 0xffff0000u)};
            }
        }
        BARRIER();
        STAMP(51);
        const unsigned char* img = P.ws + WS_IMA + p * IMG_BYTES;
        unsigned fo[4];
#pragma unroll
        for (int i = 0; i < 4; ++i) { const int j = i * 8 + wave, jt = j / 6; fo[i] = (j < 30 ? jt * 12 + j : 60 + j) * 1024; }
        const unsigned st3 = wave < 6 ? 6144 : 2048;
        auto piece = [&](int c, int i) {
            unsigned f = c < 3 ? fo[i] + c * (i < 3 ? 6144 : st3) : (96 + i * 8 + wave) * 1024;
            if (c == 3 && i == 3) f = f < 125 * 1024 ? f : 125 * 1024;
            dma1k(img + f, lane * 16, f);
        };
#pragma unroll
        for (int i = 0; i < 8; ++i) asm volatile("" :: "v"(lraw[i]));
#pragma unroll
        for (int i = 0; i < 8; ++i) piece(i >> 2, i & 3);
        STAMP(1);
        if (wave < 3) {
            const u32x4 bm[2] = {im, im}, bl[2] = {ix, ix};
            unit_tile(lraw, bm, bl, 0);
            if ((g >> 1) == 1) unit_tile(lraw + 4, bm, bl, 7168);
        } else {
            wdst = __builtin_amdgcn_readfirstlane(wdst);
            if (wdst) {
                const f32x4 a0 = mfma32(lraw[0], im, Z4), a1 = mfma32(lraw[1], im, Z4), c0 = mfma32(lraw[0], ix, Z4), c1 = mfma32(lraw[1], ix, Z4);
                st128c(wsr, lane * 16, wdst, (u32x4){pk2(a0[0], a0[1]), pk2(a0[2], a0[3]), pk2(a1[0], a1[1]), pk2(a1[2], a1[3])});
                st128c(wsr, lane * 16, wdst + TKT, (u32x4){pk2(c0[0], c0[1]), pk2(c0[2], c0[3]), pk2(c1[0], c1[1]), pk2(c1[2], c1[3])});
            }
            if (wave >= 5) arm_unit(wsr, lane * 16, wave - 5, b);
            if (CSPA && wave == 7) colsum_get(wsr, p, L_CS, lane);
        }
#pragma unroll
        for (int t = 0; t < 7; ++t) acc[t] = Z4;
        sq = Z4;
        const unsigned lb0[3] = {(unsigned)lane * 16, (unsigned)lane * 16 + 6144, (unsigned)lane * 16 + 12288};
        if (wave < 3) asm volatile("s_waitcnt vmcnt(4)" ::: "memory"); else if (wave == 5) asm volatile("s_waitcnt vmcnt(5)" ::: "memory");
        else if (wave < 5) asm volatile("s_waitcnt vmcnt(6)" ::: "memory"); else asm volatile("s_waitcnt vmcnt(5)" ::: "memory");
        BARRIER();
        STAMP(2);
        gemm_img<0, 5, 0, 6, true, 2>(acc, xf, lb0, sq, [&](int s) {
            if (s < 2 && wave < 7 && (s == 0 || (g >> 1) == 1)) put_frag<CSPB>(wsr, WS_IMB + p * IMG_BYTES + wave * TKT + (s ? 14 + g : g) * 1024, (16 + p) * 18 + (s ? 14 + g : g), wave, lds_r128(L_PST + s * 7168 + wave * 1024 + lane * 16), lane);
            if (s >= 2) piece(2, s - 2); });
        STAMP(25);
        asm volatile("s_waitcnt vmcnt(4)" ::: "memory"); BARRIER();
        if (tid == 0) set_flag(P.ws, FL_W1, g * 16 + p, C.tag);
        STAMP(26);
        gemm_img<0, 5, 6, 12, true, 2>(acc, xf, lb0, sq, [&](int s) { if (s < 10) piece(3, s - 6); if (s >= 10) arm_unit(wsr, lane * 16, 3 + (s - 10) * 8 + wave, b); });
        STAMP(27);
        asm volatile("s_waitcnt vmcnt(6)" ::: "memory"); BARRIER();
        STAMP(28);
        gemm_img<0, 5, 12, 18, true, 2>(acc, xf, lb0, sq, [&](int s) { if (s < 17 || wave < 4) arm_unit(wsr, lane * 16, 19 + (s - 12) * 8 + wave, b); });
        STAMP(29);
        asm volatile("s_nop 7" : "+v"(sq));
        const float d0 = (tk & 1) ? sq[1] : sq[0], d1 = (tk & 1) ? sq[3] : sq[2], dg = (tk & 2) ? d1 : d0;
        ssq = gq == (tk >> 2) ? dg : 0.f;
        if (!CSPA) { if (wave < 4) colsum_tile(wave, L_CS, lane); if (wave == 0) colsum_tile(4, L_CS, lane); BARRIER(); }
    }
#pragma unroll 1
    for (int it = 0; it < NIT; ++it) {
        layer_fn<false>(P, wsr, lane, C, it, xf, res, acc, ssq);
        layer_fn<true>(P, wsr, lane, C, it, xf, res, acc, ssq);
    }
}

extern "C" void kernel_launch(void* const* d_in, const int* in_sizes, int n_in, void* d_out, int out_size, void* d_ws, size_t ws_size, hipStream_t stream) {
    Params P;
    P.x = (const u16*)d_in[0]; P.wqkvA = (const u16*)d_in[1]; P.wphi = (const u16*)d_in[2]; P.woA = (const u16*)d_in[3];
    P.wqkvB = (const u16*)d_in[5]; P.woB = (const u16*)d_in[6];
    P.out = (u16*)d_out; P.ws = (unsigned char*)d_ws;
    (void)hipFuncSetAttribute((const void*)fused_kernel, hipFuncAttributeMaxDynamicSharedMemorySize, LDS_BYTES);
    hipLaunchKernelGGL(fused_kernel, dim3(256), dim3(512), LDS_BYTES, stream, P);
}
```

```cpp
#include <hip/hip_runtime.h>
#include <stdint.h>
#define ODRAIN 0
#define DSLEEP 16
#define CSPA 0
#define CSPB 1
#define BAR2 1
#define WOBAL 1
#define OSLEEP 10
#define PSLEEP 0
#define XSLEEP 10
#define RSLEEP 6
#define NIT 2
#define ONEPOLL 0

typedef __attribute__((ext_vector_type(8))) __bf16 bf16x8;
typedef __attribute__((ext_vector_type(2))) __bf16 bf16x2;
typedef __attribute__((ext_vector_type(4))) short s16x4;
typedef __attribute__((ext_vector_type(4))) float f32x4;
typedef __attribute__((ext_vector_type(2))) float f32x2;
typedef __attribute__((ext_vector_type(16))) float f32x16;
typedef __attribute__((ext_vector_type(4))) unsigned u32x4;
typedef __attribute__((ext_vector_type(2))) unsigned u32x2;
typedef unsigned short u16;
#define LAS __attribute__((address_space(3)))
#define GAS __attribute__((address_space(1)))
#define RLX_AGENT __ATOMIC_RELAXED, __HIP_MEMORY_SCOPE_AGENT
#define DEVI __device__ __forceinline__

constexpr int SEQ = 2048, DM = 576, NQKV = 1728;
constexpr unsigned TKT = 18 * 1024;
constexpr unsigned ACT_BYTES = 128 * TKT;
constexpr unsigned IMG_BYTES = 7 * TKT;
constexpr unsigned WS_FLAGS = 4096;
constexpr unsigned FL_W1 = 0, FL_W2 = 1, FL_W4 = 2, FL_W5 = 3, FL_IA = 4, FL_N = 8;
constexpr unsigned KVP_BYTES = 256 * 3072, KVH_BYTES = 256 * 20480, ST_BYTES = 128 * 2304, UNIT = 256 * 1024;
constexpr unsigned SETA = KVP_BYTES + 2 * ACT_BYTES + ST_BYTES;
constexpr unsigned N0 = 23, N1 = 40, N2 = 52;
constexpr unsigned WS_S0 = 524288, WS_S1 = WS_S0 + N0 * UNIT, WS_S2 = WS_S1 + N1 * UNIT;
static_assert(WS_FLAGS + FL_N * 256 * 128 <= WS_S0 && SETA <= N0 * UNIT && KVH_BYTES + 2 * ACT_BYTES + ST_BYTES <= N1 * UNIT && SETA + KVH_BYTES + ACT_BYTES <= N2 * UNIT, "map");
__host__ __device__ constexpr unsigned ws_k(int layer) { return layer == 0 ? WS_S0 : layer == 1 ? WS_S1 : layer == 2 ? WS_S2 : WS_S2 + SETA; }
__host__ __device__ constexpr unsigned ws_o(int layer) { return ws_k(layer) + ((layer & 1) ? KVH_BYTES : KVP_BYTES); }
constexpr unsigned WS_IMA = WS_S2 + N2 * UNIT;
constexpr unsigned WS_IMB = WS_IMA + 16 * IMG_BYTES;
constexpr unsigned WS_WOI = WS_IMB + 16 * IMG_BYTES;
constexpr unsigned WS_CSP = WS_WOI + 2 * 36 * TKT;
constexpr unsigned WS_END = WS_CSP + 2 * 16 * 18 * 448;
constexpr unsigned WS_DBG = WS_END;
constexpr unsigned L_OST = 5 * TKT;
constexpr unsigned L_STG = IMG_BYTES;
constexpr unsigned L_KT = L_STG - 40960;
constexpr unsigned L_QS = L_STG + 20480;
constexpr unsigned L_PK = L_STG, L_VV = L_STG + 20480;
constexpr unsigned L_KVS = 0;
constexpr unsigned L_CS = L_STG + 32768;
constexpr unsigned L_PST = 147456;
constexpr unsigned LDS_BYTES = 163840;
constexpr unsigned CODE_BYTES = 42 * 1024;
constexpr unsigned SPIN_CAP = 1u << 17;
constexpr float LOG2E = 1.4426950408889634f;
constexpr float NEGBIG = -1e30f;

struct Params {
    const u16* x; const u16* wqkvA; const u16* wphi; const u16* woA; const u16* wqkvB; const u16* woB;
    u16* out; unsigned char* ws;
};

DEVI f32x4 mfma32(u32x4 a, u32x4 b, f32x4 c) { return __builtin_amdgcn_mfma_f32_16x16x32_bf16(__builtin_bit_cast(bf16x8, a), __builtin_bit_cast(bf16x8, b), c, 0, 0, 0); }
DEVI f32x16 mfmaL(u32x4 a, u32x4 b, f32x16 c) { return __builtin_amdgcn_mfma_f32_32x32x16_bf16(__builtin_bit_cast(bf16x8, a), __builtin_bit_cast(bf16x8, b), c, 0, 0, 0); }
DEVI u32x4 w1(unsigned a) { return (u32x4){a, 0u, 0u, 0u}; }
DEVI unsigned pkv(f32x2 f) { return __builtin_bit_cast(unsigned, __builtin_convertvector(f, bf16x2)); }
DEVI unsigned pk2(float lo, float hi) { return pkv((f32x2){lo, hi}); }
DEVI float dot2(unsigned a, unsigned b, float c) { return __builtin_amdgcn_fdot2_f32_bf16(__builtin_bit_cast(bf16x2, a), __builtin_bit_cast(bf16x2, b), c, false); }
DEVI float ex2(float x) { return __builtin_amdgcn_exp2f(x); }
DEVI float rcp(float x) { return __builtin_amdgcn_rcpf(x); }
DEVI float f_of(unsigned u) { return __builtin_bit_cast(float, u); }
DEVI unsigned u_of(float f) { return __builtin_bit_cast(unsigned, f); }
DEVI f32x2 fma2(f32x2 a, f32x2 b, f32x2 c) { return __builtin_elementwise_fma(a, b, c); }
DEVI f32x2 sp2(float v) { return (f32x2){v, v}; }
DEVI f32x2 sw16(float a, float b) { const auto r = __builtin_amdgcn_permlane16_swap(u_of(a), u_of(b), false, false); return (f32x2){f_of(r[0]), f_of(r[1])}; }
DEVI f32x2 sw32(float a, float b) { const auto r = __builtin_amdgcn_permlane32_swap(u_of(a), u_of(b), false, false); return (f32x2){f_of(r[0]), f_of(r[1])}; }
DEVI float vmax(f32x2 v) { float r; asm("v_max_f32 %0, %1, %2" : "=v"(r) : "v"(v.x), "v"(v.y)); return r; }
DEVI float vmaxn(f32x2 v) { float r; asm("v_max_f32_e64 %0, -%1, -%2" : "=v"(r) : "v"(v.x), "v"(v.y)); return r; }
DEVI float max3f(float a, float b, float c) { return __builtin_fmaxf(__builtin_fmaxf(a, b), c); }
DEVI float min3f(float a, float b, float c) { return __builtin_fminf(__builtin_fminf(a, b), c); }
DEVI u32x4 unit_pair(unsigned a0, unsigned a1, unsigned b0, unsigned b1) {
    const auto s = __builtin_amdgcn_permlane16_swap(a0, b0, false, false);
    const auto t = __builtin_amdgcn_permlane16_swap(a1, b1, false, false);
    return (u32x4){s[0], t[0], s[1], t[1]};
}

#define BARRIER() asm volatile("s_waitcnt lgkmcnt(0)\n\ts_barrier" ::: "memory")
#define DRAIN() asm volatile("s_waitcnt vmcnt(0)" ::: "memory")
#define SB() __builtin_amdgcn_sched_barrier(0)

extern __shared__ __attribute__((aligned(16))) unsigned char smem[];
DEVI u32x4 lds_r128(unsigned off) { return *(const LAS u32x4*)((LAS unsigned char*)smem + off); }
DEVI f32x4 lds_rf4(unsigned off) { return *(const LAS f32x4*)((LAS unsigned char*)smem + off); }
DEVI unsigned lds_r32(unsigned off) { return *(const LAS unsigned*)((LAS unsigned char*)smem + off); }
DEVI void lds_w128(unsigned off, u32x4 v) { *(LAS u32x4*)((LAS unsigned char*)smem + off) = v; }
DEVI void lds_w64(unsigned off, u32x2 v) { *(LAS u32x2*)((LAS unsigned char*)smem + off) = v; }
DEVI void lds_w32(unsigned off, unsigned v) { *(LAS unsigned*)((LAS unsigned char*)smem + off) = v; }
DEVI void lds_w16(unsigned off, unsigned v) { *(LAS u16*)((LAS unsigned char*)smem + off) = (u16)v; }
DEVI u32x2 lds_tr(unsigned off) { return __builtin_bit_cast(u32x2, __builtin_amdgcn_ds_read_tr16_b64_v4i16((LAS s16x4*)((LAS unsigned char*)smem + off))); }

typedef __amdgpu_buffer_rsrc_t rsrc_t;
DEVI rsrc_t mk_rsrc(const void* p, unsigned bytes) { return __builtin_amdgcn_make_buffer_rsrc((void*)p, 0, bytes, 0x00020000); }
DEVI u32x4 ld128c(rsrc_t r, unsigned voff, unsigned soff) { return __builtin_amdgcn_raw_buffer_load_b128(r, voff, soff, 16); }
DEVI void st128c(rsrc_t r, unsigned voff, unsigned soff, u32x4 v) {
    __builtin_amdgcn_raw_buffer_store_b128(v, r, voff, soff, 16);
    asm volatile("s_nop 1" :: "v"(v) : "memory");
}
DEVI void st64c(rsrc_t r, unsigned voff, unsigned soff, u32x2 v) { __builtin_amdgcn_raw_buffer_store_b64(v, r, voff, soff, 16); }
DEVI void st32c(rsrc_t r, unsigned voff, unsigned soff, unsigned v) { __builtin_amdgcn_raw_buffer_store_b32(v, r, voff, soff, 16); }
DEVI void dma1k(const void* sbase, unsigned voff, unsigned lds_dst) {
    unsigned keep;
    asm volatile("s_mov_b32 %0, m0\n\ts_mov_b32 m0, %3\n\ts_nop 0\n\tglobal_load_lds_dwordx4 %1, %2 sc1\n\ts_mov_b32 m0, %0" : "=&s"(keep) : "v"(voff), "s"(sbase), "s"(lds_dst) : "memory");
}
DEVI void dma1k_in(const void* sbase, unsigned voff, unsigned lds_dst) {
    unsigned keep;
    asm volatile("s_mov_b32 %0, m0\n\ts_mov_b32 m0, %3\n\ts_nop 0\n\tglobal_load_lds_dwordx4 %1, %2\n\ts_mov_b32 m0, %0" : "=&s"(keep) : "v"(voff), "s"(sbase), "s"(lds_dst) : "memory");
}
DEVI void ld128_raw(u32x4& d, const void* base, unsigned voff) { asm volatile("global_load_dwordx4 %0, %1, %2" : "=v"(d) : "v"(voff), "s"(base) : "memory"); }
DEVI void ld64_raw(u32x2& d, const void* base, unsigned voff) { asm volatile("global_load_dwordx2 %0, %1, %2" : "=v"(d) : "v"(voff), "s"(base) : "memory"); }
DEVI void wait_flags(const unsigned char* base, unsigned voff, unsigned tag) {
    const GAS unsigned* f = (const GAS unsigned*)(base + voff);
#pragma unroll 1
    for (unsigned sp = 0; sp < SPIN_CAP; ++sp) {
        const unsigned v = __hip_atomic_load(f, RLX_AGENT);
        if (__all(v == tag)) break;
        __builtin_amdgcn_s_sleep(1);
    }
    asm volatile("" ::: "memory");
}
DEVI unsigned umax3(unsigned a, unsigned b, unsigned c) { return __builtin_elementwise_max(__builtin_elementwise_max(a, b), c); }
DEVI unsigned umax4(unsigned m, u32x4 v) { return umax3(umax3(m, v.x, v.y), v.z, v.w); }
DEVI unsigned ok1(unsigned v) { return __builtin_elementwise_min(v, 0xFFFFFFFEu); }
DEVI u32x2 ok2(u32x2 v) { return (u32x2){ok1(v.x), ok1(v.y)}; }
DEVI u32x4 ok4(u32x4 v) { return (u32x4){ok1(v.x), ok1(v.y), ok1(v.z), ok1(v.w)}; }
DEVI void arm_unit(rsrc_t r, unsigned l16, unsigned unit, unsigned cu) { st128c(r, l16, WS_S0 + (unit * 256 + cu) * 1024, (u32x4){~0u, ~0u, ~0u, ~0u}); }
DEVI void wait_all256(const unsigned char* base, int lane, unsigned tag) {
    const GAS unsigned* f = (const GAS unsigned*)(base + lane * 128);
#pragma unroll 1
    for (unsigned sp = 0; sp < SPIN_CAP; ++sp) {
        const unsigned v0 = __hip_atomic_load(f, RLX_AGENT), v1 = __hip_atomic_load(f + 2048, RLX_AGENT), v2 = __hip_atomic_load(f + 4096, RLX_AGENT), v3 = __hip_atomic_load(f + 6144, RLX_AGENT);
        if (__all(v0 == tag && v1 == tag && v2 == tag && v3 == tag)) break;
        __builtin_amdgcn_s_sleep(1);
    }
    asm volatile("" ::: "memory");
}
DEVI u32x4 peek256(const unsigned char* base, int lane) {
    const GAS unsigned* f = (const GAS unsigned*)(base + lane * 128);
    return (u32x4){__hip_atomic_load(f, RLX_AGENT), __hip_atomic_load(f + 2048, RLX_AGENT), __hip_atomic_load(f + 4096, RLX_AGENT), __hip_atomic_load(f + 6144, RLX_AGENT)};
}
DEVI void judge256(u32x4 v, const unsigned char* base, int lane, unsigned tag) {
    if (!__all(v.x == tag && v.y == tag && v.z == tag && v.w == tag)) wait_all256(base, lane, tag);
    asm volatile("" ::: "memory");
}
#define RCOUNT(site) do { } while (0)
#define RETRY(site, LOADS, MAXEXPR) do { _Pragma("unroll 1") for (unsigned sp_ = 0; sp_ < SPIN_CAP; ++sp_) { if (!__any((MAXEXPR) == 0xFFFFFFFFu)) break; \
    RCOUNT(site); if (sp_ < 4) __builtin_amdgcn_s_sleep(RSLEEP); else __builtin_amdgcn_s_sleep(48); asm volatile("" ::: "memory"); LOADS; } } while (0)
DEVI const unsigned char* flag_at(const unsigned char* ws, unsigned kind, unsigned idx) { return ws + WS_FLAGS + (kind * 256 + idx) * 128; }
DEVI void set_flag(unsigned char* ws, unsigned kind, unsigned idx, unsigned tag) {
    __hip_atomic_store((GAS unsigned*)(ws + WS_FLAGS + (kind * 256 + idx) * 128), tag, RLX_AGENT);
}

#define STAMP(i) do { } while (0)

DEVI u32x4 onehot8(int j) {
    const unsigned v = (j & 1) ? 0x3F800000u : 0x3F80u; const int w = j >> 1;
    return (u32x4){w == 0 ? v : 0u, w == 1 ? v : 0u, w == 2 ? v : 0u, w == 3 ? v : 0u};
}
DEVI int wo_krow(int mat, int s, int gq, int j) {
    const int a = 36 * s + (mat ? 18 * (gq >> 1) + 8 * (j >> 2) + 4 * (gq & 1) : 18 * (j >> 2) + 4 * gq) + (j & 3);
    const int c = 36 * (8 * (s - 16) + 2 * gq + (j >> 2)) + 18 * ((j >> 1) & 1) + 16 + (j & 1);
    return s < 16 ? a : c;
}
DEVI int fresh(int v) { asm volatile("" : "+v"(v)); return v; }
DEVI int sfresh(int v) { asm volatile("" : "+s"(v)); return v; }

template <int T0, int NT, int S0, int S1, bool SSQ, int D, class F>
DEVI void gemm_img(f32x4 (&acc)[7], u32x4 (&xf)[18], const unsigned (&lb)[3], f32x4& sq, F side) {
    u32x4 a[D][NT];
#pragma unroll
    for (int s = S0; s < S0 + D; ++s)
#pragma unroll
        for (int t = 0; t < NT; ++t) a[s % D][t] = lds_r128(lb[s / 6] + (T0 + t) * TKT + (s % 6) * 1024);
    SB();
#pragma unroll
    for (int s = S0; s < S1; ++s) {
        side(s);
        SB();
        if (SSQ) { sq = mfma32(xf[s], xf[s], sq); SB(); }
#pragma unroll
        for (int t = 0; t < NT; ++t) {
            acc[T0 + t] = mfma32(a[s % D][t], xf[s], acc[T0 + t]);
            SB();
            if (s + D < S1) { a[s % D][t] = lds_r128(lb[(s + D) / 6] + (T0 + t) * TKT + ((s + D) % 6) * 1024); SB(); }
        }
    }
}
template <bool SUM>
DEVI void put_frag(rsrc_t wsr, unsigned dst, unsigned ks, int t, u32x4 f, int lane) {
    st128c(wsr, lane * 16, dst, f);
    if (!SUM) return;
    f32x4 c = mfma32((u32x4){0x3F803F80u, 0x3F803F80u, 0x3F803F80u, 0x3F803F80u}, f, (f32x4){0.f, 0.f, 0.f, 0.f});
    asm volatile("s_nop 7" : "+v"(c));
    if (lane < 16) st32c(wsr, lane * 4, WS_CSP + ks * 448 + t * 64, u_of(c[0]));
}
DEVI void colsum_get(rsrc_t wsr, unsigned img_p, unsigned csb, int lane) {
    u32x2 v[18];
#pragma unroll
    for (int s = 0; s < 18; ++s) v[s] = __builtin_amdgcn_raw_buffer_load_b64(wsr, lane < 56 ? lane * 8 : 0, WS_CSP + (img_p * 18 + s) * 448, 16);
    SB();
    f32x2 c = __builtin_bit_cast(f32x2, v[0]);
#pragma unroll
    for (int s = 1; s < 18; ++s) c += __builtin_bit_cast(f32x2, v[s]);
    if (lane < 56) lds_w64(csb + ((lane >> 1) & 3) * 112 + (lane >> 3) * 16 + (lane & 1) * 8, __builtin_bit_cast(u32x2, c));
}
DEVI void colsum_tile(int t, unsigned csb, int lane) {
    f32x4 c = {0.f, 0.f, 0.f, 0.f}; const u32x4 ones = {0x3F803F80u, 0x3F803F80u, 0x3F803F80u, 0x3F803F80u};
    u32x4 a[18];
#pragma unroll
    for (int s = 0; s < 18; ++s) a[s] = lds_r128(t * TKT + s * 1024 + lane * 16);
    SB();
#pragma unroll
    for (int s = 0; s < 18; ++s) c = mfma32(a[s], ones, c);
    asm volatile("s_nop 7" : "+v"(c));
    if ((lane & 15) == 0) lds_w128(csb + ((lane >> 4) * 28 + 4 * t) * 4, __builtin_bit_cast(u32x4, c));
}
DEVI void dual_softmax(const float (&t)[9], float ks, bool lo, u32x4& fa, u32x4& fb, unsigned& fe) {
    const float zA = lo ? t[8] : t[0], zB = lo ? t[4] : t[8];
    const float mA = max3f(max3f(t[0], t[1], t[2]), t[3], zA), nA = min3f(min3f(t[0], t[1], t[2]), t[3], zA);
    const float mB = max3f(max3f(t[4], t[5], t[6]), t[7], zB), nB = min3f(min3f(t[4], t[5], t[6]), t[7], zB);
    const float T = -ks * vmax(sw32(vmax(sw16(mA, mB)), vmaxn(sw16(nA, nB))));
    const f32x2 u = sw16(T, T);
    const f32x2 sa = sw32(u.x, u.x), sb = sw32(u.y, u.y);
    const f32x2 s8 = lo ? sa : sb, k2 = {ks, -ks};
    f32x2 e[9];
#pragma unroll
    for (int i = 0; i < 9; ++i) { const f32x2 z = fma2(sp2(t[i]), k2, i < 4 ? sa : i < 8 ? sb : s8); e[i] = (f32x2){ex2(z.x), ex2(z.y)}; }
    const float wl = lo ? 1.f : 0.f;
    const f32x2 sA = fma2(e[8], sp2(wl), (e[0] + e[1]) + (e[2] + e[3])), sB = fma2(e[8], sp2(1.f - wl), (e[4] + e[5]) + (e[6] + e[7]));
    const f32x2 p = sw16(sA.x, sB.x), q = sw16(sA.y, sB.y), w = sw32(p.x + p.y, q.x + q.y);
    const float R = rcp(w.x + w.y);
    const f32x2 v = sw16(R, R);
    const f32x2 ra = sw32(v.x, v.x), rb = sw32(v.y, v.y), r8 = lo ? ra : rb;
    fa = (u32x4){pkv(e[0] * ra), pkv(e[1] * ra), pkv(e[2] * ra), pkv(e[3] * ra)};
    fb = (u32x4){pkv(e[4] * rb), pkv(e[5] * rb), pkv(e[6] * rb), pkv(e[7] * rb)};
    fe = pkv(e[8] * r8);
}

DEVI f32x16 att_scores(u32x4 km, u32x4 kl, u32x4 q0, u32x4 q1, f32x16 c) { return mfmaL(kl, q1, mfmaL(km, q0, c)); }
DEVI u32x4 att_p(const f32x16& s, int b) { return (u32x4){pk2(ex2(s[b]), ex2(s[b + 1])), pk2(ex2(s[b + 2]), ex2(s[b + 3])), pk2(ex2(s[b + 4]), ex2(s[b + 5])), pk2(ex2(s[b + 6]), ex2(s[b + 7]))}; }
DEVI void att_accum(const f32x16& s, const u32x2 (&v)[4], f32x16& o) {
    const u32x4 p0 = {pk2(ex2(s[0]), ex2(s[1])), pk2(ex2(s[2]), ex2(s[3])), pk2(ex2(s[4]), ex2(s[5])), pk2(ex2(s[6]), ex2(s[7]))};
    o = mfmaL((u32x4){v[0].x, v[0].y, v[1].x, v[1].y}, p0, o);
    const u32x4 p1 = {pk2(ex2(s[8]), ex2(s[9])), pk2(ex2(s[10]), ex2(s[11])), pk2(ex2(s[12]), ex2(s[13])), pk2(ex2(s[14]), ex2(s[15]))};
    o = mfmaL((u32x4){v[2].x, v[2].y, v[3].x, v[3].y}, p1, o);
}
DEVI void att_vload(u32x2 (&v)[4], unsigned va) {
#pragma unroll
    for (int e = 0; e < 4; ++e) v[e] = lds_tr(va + e * 384);
}
DEVI float max16(const f32x16& s, float m) {
#pragma unroll
    for (int v = 0; v < 16; v += 2) m = max3f(m, s[v], s[v + 1]);
    return m;
}
DEVI f32x16 splat16(float v) { f32x16 r;
#pragma unroll
    for (int i = 0; i < 16; ++i) r[i] = v;
    return r; }

struct Ctx { int wave, g, p; unsigned tag; };

template <bool ISB>
DEVI void layer_fn(const Params& P, const rsrc_t wsr, const int lane_in, const Ctx& C, const int it, u32x4 (&xf)[18], f32x4 (&res)[3], f32x4 (&acc)[7], float& ssq) {
    const int wave = sfresh(C.wave), g = sfresh(C.g), p = sfresh(C.p), cu = g * 16 + p;
    const unsigned tag = C.tag;
    const int layer = 2 * it + (ISB ? 1 : 0);
    const int tp = (p >> 1) & 3, cg = (p >> 3) * 2 + (p & 1);
    const f32x4 Z4 = {0.f, 0.f, 0.f, 0.f};
    constexpr unsigned csb = L_CS + (ISB ? 448 : 0);
    const bool first = !ISB && it == 0, last = ISB && it == 1;
    u32x4 wof[18];
    float mean, rstd;
    const unsigned wob = WS_WOI + ((ISB ? 36 : 0) + 9 * cg + wave) * TKT;
    const unsigned w1k = wave * 1024;
    const unsigned kbase = ISB ? (it ? ws_k(3) : ws_k(1)) : (it ? ws_k(2) : ws_k(0)), obase = kbase + (ISB ? KVH_BYTES : KVP_BYTES);
    const int rot = first ? 0 : p % 3;
    const unsigned ro0 = rot * 6144, ro1 = rot == 2 ? 0 : ro0 + 6144, ro2 = rot ? ro0 - 6144 : 12288;
    const unsigned char* nimg = P.ws + (ISB ? WS_IMA : WS_IMB) + p * IMG_BYTES + w1k;
    {
        const int lane = fresh(lane_in), gq = lane >> 4, tk = lane & 15;
        const unsigned l16 = lane * 16;
        const unsigned lb[3] = {l16 + ro0, l16 + ro1, l16 + ro2};
        if (!first) {
#pragma unroll
            for (int t = 0; t < 5; ++t) acc[t] = Z4;
            const unsigned xprev = (ISB ? (it ? ws_o(2) : ws_o(0)) : ws_o(1)) + ACT_BYTES;
            const unsigned xb = xprev + (g * 8 + wave) * TKT, sb = xprev + ACT_BYTES + (g * 8 + wave) * 2304;
            unsigned st[9];
            auto xload = [&](int s0, int s1) {
#pragma unroll
                for (int s = s0; s < s1; ++s) xf[s] = ld128c(wsr, l16 + (s % 6) * 1024, xb + (s < 6 ? ro0 : s < 12 ? ro1 : ro2));
            };
            auto xmax = [&](int s0, int s1) { unsigned m = 0u;
#pragma unroll
                for (int s = s0; s < s1; ++s) m = umax4(m, xf[s]);
                return m; };
            auto sload = [&]() {
#pragma unroll
                for (int j = 0; j < 9; ++j) st[j] = __builtin_amdgcn_raw_buffer_load_b32(wsr, lane * 4 + j * 256, sb, 16);
            };
            asm volatile("s_waitcnt vmcnt(2)" ::: "memory");
            BARRIER();
            if (!CSPB && ISB && it == 0) { if (wave < 5) colsum_tile(wave, csb, lane); } else
            __builtin_amdgcn_s_sleep(XSLEEP);
            sload(); xload(0, 3);
            RETRY(layer * 8 + 3, { sload(); xload(0, 3); }, umax3(umax3(umax3(umax3(umax3(xmax(0, 3), st[0], st[1]), st[2], st[3]), st[4], st[5]), st[6], st[7]), st[8], 0u));
            STAMP(layer * 12 + 2);
            xload(3, 9);
            ssq = ((f_of(st[0]) + f_of(st[1])) + (f_of(st[2]) + f_of(st[3]))) + ((f_of(st[4]) + f_of(st[5])) + (f_of(st[6]) + f_of(st[7]))) + f_of(st[8]);
            if (!CSPB && ISB && it == 0) BARRIER();
            const unsigned char* cimg = P.ws + (ISB ? WS_IMB : WS_IMA) + p * IMG_BYTES + w1k;
            f32x4 dummy;
            gemm_img<0, 5, 0, 18, false, 2>(acc, xf, lb, dummy, [&](int s) {
                if (s < 3) xload(9 + 2 * s, 11 + 2 * s);
                if (s == 3) RETRY(layer * 8 + 1, xload(3, 9), xmax(3, 9));
                if (s >= 3 && s < 6) xload(12 + s, 13 + s);
                if (s == 9) RETRY(layer * 8 + 2, xload(9, 18), xmax(9, 18));
                if (s >= 9 && s < 14 && (s < 13 || wave < 4)) dma1k(cimg + (90 + 8 * (s - 9)) * 1024, l16, w1k + (90 + 8 * (s - 9)) * 1024);
                if (!ISB && s >= 9) wof[s] = ld128c(wsr, l16 + s * 1024, wob);
                if (ISB && it == 0 && s >= 14 && s < 17) arm_unit(wsr, l16, 63 + (s - 14) * 8 + wave, cu);
                if (!ISB && s >= 14 && (s < 17 || wave < 4)) arm_unit(wsr, l16, 87 + (s - 14) * 8 + wave, cu);
            });
        }
        f32x4 cs[5];
#pragma unroll
        for (int t = 0; t < 5; ++t) cs[t] = lds_rf4(csb + gq * 112 + 16 * t);
        acc[5] = Z4; acc[6] = Z4;
        STAMP(layer * 12 + 3);
        const f32x2 r1 = sw16(ssq, ssq); const float h1 = r1.x + r1.y; const f32x2 r2 = sw32(h1, h1);
        asm volatile("s_nop 7" : "+v"(acc[4]));
        mean = acc[4][2] * (1.f / DM);
        rstd = __builtin_amdgcn_rsqf(__builtin_fmaxf((r2.x + r2.y) * (1.f / DM) - mean * mean, 0.f) + 1e-5f);
        const int hx = gq >> 1, od = gq & 1;
        f32x2 y[9];
        {
            const f32x2 nm = sp2(-mean);
#pragma unroll
            for (int t = 0; t < 5; ++t) {
                y[2 * t] = fma2(nm, (f32x2){cs[t][0], cs[t][1]}, (f32x2){acc[t][0], acc[t][1]});
                if (t < 4) y[2 * t + 1] = fma2(nm, (f32x2){cs[t][2], cs[t][3]}, (f32x2){acc[t][2], acc[t][3]});
            }
        }
        const u32x4 vm = unit_pair(pk2(y[4].y * rstd, y[5].x * rstd), pk2(y[5].y * rstd, y[6].x * rstd), pk2(y[6].y * rstd, y[7].x * rstd), pk2(y[7].y * rstd, y[8].x * rstd));
        const float v8 = y[8].y * rstd;
        const unsigned vl = pkv(sw16(v8, v8));
        if (ISB) {
            const f32x2 r2 = sp2(rstd);
            const u32x4 km = unit_pair(pkv(y[0] * r2), pkv(y[1] * r2), pkv(y[2] * r2), pkv(y[3] * r2));
            const float k8 = y[4].x * rstd;
            const u32x4 lr = {ok1(pkv(sw16(k8, k8))), 0u, ok1(vl), 0x3F80u};
            const unsigned tb = (wave >> 1) * 5120 + (wave & 1) * 256, tc = (wave >> 1) * 5120 + (wave & 1) * 768 + 1024;
            const unsigned oc = (tk >> 3) * 384 + (tk & 7) * 16;
            const unsigned a0 = od * 2560 + hx * 512 + tk * 16, a1 = od * 2560 + hx * 128 + oc, a2 = hx * 2560 + 256 + (oc ^ 64);
            const unsigned hb = kbase + cu * 20480;
            st128c(wsr, a0, hb + tb, ok4(km)); st128c(wsr, a1, hb + tc, ok4(vm));
            lds_w128(L_STG + tb + a0, km); lds_w128(L_STG + tc + a1, vm);
            if (od == 0) { st128c(wsr, a2, hb + tc, lr); lds_w128(L_STG + tc + a2, lr); }
            asm volatile("s_waitcnt vmcnt(3)" ::: "memory");
            BARRIER();
            if (it == 0 && wave == 7 && lane == 0) set_flag(P.ws, FL_W4, cu, tag);
        } else {
            const float kz[9] = {y[0].x, y[0].y, y[1].x, y[1].y, y[2].x, y[2].y, y[3].x, y[3].y, y[4].x};
            u32x4 fa, fb; unsigned fe;
            dual_softmax(kz, rstd * LOG2E, gq < 2, fa, fb, fe);
            const unsigned pk = L_PK + wave * 1280 + tk * 80, vv = L_VV + wave * 768 + tk * 48;
            lds_w128(pk + gq * 16, fa); lds_w128(pk + gq * 16 + 10240, fb); lds_w32(pk + hx * 10240 + 64 + od * 4, fe);
            lds_w128(vv + od * 6144 + hx * 16, vm);
            lds_w64(vv + hx * 6144 + 32, (u32x2){vl, 0x3F80u});
            STAMP(53 + it);
            if (first && wave == 3) wait_all256(flag_at(P.ws, FL_W1, 0), lane, tag);
            DRAIN();
            BARRIER();
            if (wave == 7 && lane == 0) set_flag(P.ws, it ? FL_W5 : FL_W2, cu, tag);
            STAMP(55 + it);
            if (wave < 6) {
                const int h = wave >= 3, ft = wave - 3 * h;
                const unsigned trow = (unsigned)(lane >> 2), tcol = (unsigned)(lane & 3) * 8u;
                const unsigned pa = L_PK + h * 10240 + ft * 32 + trow * 80 + tcol, pb = L_VV + h * 6144 + trow * 48 + tcol;
                u32x2 ta[8], tb[8], td[8];
#pragma unroll
                for (int s = 0; s < 8; ++s) { ta[s] = lds_tr(pa + (16 * s) * 80); tb[s] = lds_tr(pb + (16 * s) * 48); td[s] = lds_tr(pb + (16 * s) * 48 + 32); }
                SB();
                f32x4 k0 = Z4, k1 = Z4;
#pragma unroll
                for (int s = 0; s < 4; ++s) {
                    const u32x4 af = {ta[2 * s].x, ta[2 * s].y, ta[2 * s + 1].x, ta[2 * s + 1].y};
                    k0 = mfma32((u32x4){tb[2 * s].x, tb[2 * s].y, tb[2 * s + 1].x, tb[2 * s + 1].y}, af, k0);
                    k1 = mfma32((u32x4){td[2 * s].x, td[2 * s].y, td[2 * s + 1].x, td[2 * s + 1].y}, af, k1);
                }
                const u32x2 s0 = ok2((u32x2){pk2(k0[0], k0[1]), pk2(k0[2], k0[3])}), s1 = ok2((u32x2){pk2(k1[0], k1[1]), pk2(k1[2], k1[3])});
                const unsigned kb = kbase + (p * 16 + g) * 3072 + h * 1536;
                if (ft < 2) {
                    st64c(wsr, lane * 8, kb + ft * 512, s0);
                    if (gq == 0) st64c(wsr, lane * 8, kb + 1024 + ft * 128, s1);
                } else if (tk < 4) {
                    st64c(wsr, gq * 32 + tk * 8, kb + 1280, s0);
                    if (gq == 0) st64c(wsr, tk * 8, kb + 1408, s1);
                }
            }
        }
        STAMP(layer * 12 + 0 + (layer == 0 ? 52 : 0));
        if (!(ISB ? CSPB : CSPA) && it == 0 && wave >= 6) colsum_tile(wave - 1, csb, lane);
        STAMP(layer * 12 + 4);
    }
    {
        const int lane = fresh(lane_in), gq = lane >> 4, tk = lane & 15;
        const unsigned l16 = lane * 16;
        const unsigned lb[3] = {l16 + ro0, l16 + ro1, l16 + ro2};
        f32x16 msk;
        const int dl = (lane & 31) - 4 * (lane >> 5);
        { f32x4 dummy; gemm_img<5, 2, 0, 18, false, 3>(acc, xf, lb, dummy, [&](int s) {
              if (!ISB && (s & 1) == 0) wof[s >> 1] = ld128c(wsr, l16 + (s >> 1) * 1024, wob);
              if (ISB && s < 16) msk[s] = ((s & 3) + 8 * (s >> 2) > dl) ? NEGBIG : 0.f;
              if ((s & 1) && first) wof[9 + (s >> 1)] = ld128c(wsr, l16 + (9 + (s >> 1)) * 1024, wob); }); }
        if ((!(ISB ? CSPB : CSPA) && it == 0) || (BAR2 && !ISB)) BARRIER();
        float q[9];
        {
            const unsigned ca = csb + gq * 112;
            const float c19 = f_of(lds_r32(ca + 76));
            const f32x4 c5 = lds_rf4(ca + 80), c6 = lds_rf4(ca + 96);
            q[0] = acc[4][3] - mean * c19;
#pragma unroll
            for (int i = 0; i < 4; ++i) { q[1 + i] = acc[5][i] - mean * c5[i]; q[5 + i] = acc[6][i] - mean * c6[i]; }
        }
        STAMP(layer * 12 + 5);
        if (ISB) {
            u32x4 hv[5];
            const int src = g - 2 + (wave >> 2);
            auto hload = [&]() {
                const unsigned hb = kbase + (src * 16 + p) * 20480 + (wave & 3) * 5120;
#pragma unroll
                for (int i = 0; i < 5; ++i) hv[i] = ld128c(wsr, l16 + i * 1024, hb);
            };
            if (src >= 0) {
                hload();
            } else {
#pragma unroll
                for (int i = 0; i < 5; ++i) hv[i] = (u32x4){0u, (i == 0 || (i == 2 && lane >= 32) || (i == 3 && lane < 32)) ? 0u : 0xF149u, 0u, 0u};
            }
            {
                const float qs = rstd * (0.23570226039551584f * LOG2E);
                const int hx = gq >> 1, od = gq & 1;
                const unsigned qb = L_QS + (wave >> 1) * 2304;
                lds_w128(qb + (wave & 1) * 256 + od * 1152 + hx * 512 + tk * 16, unit_pair(pk2(q[0] * qs, q[1] * qs), pk2(q[2] * qs, q[3] * qs), pk2(q[4] * qs, q[5] * qs), pk2(q[6] * qs, q[7] * qs)));
                const float q8 = q[8] * qs;
                lds_w32(qb + (wave & 1) * 64 + 1024 + hx * 1152 + tk * 4, pkv(sw16(q8, q8)));
            }
            const int h = wave & 1, jq = wave < 4 ? (wave >> 1) : 3 - ((wave - 4) >> 1);
            const int r = lane & 31, hh = lane >> 5;
            const unsigned kb = L_KT + h * 2560 + jq * 5120;
            const unsigned ka = kb + l16, la = kb + 1280 + (r >> 3) * 384 + ((r & 7) ^ 4) * 16;
            const unsigned k8 = 4 * hh + ((lane >> 2) & 3);
            const unsigned va = kb + 1024 + ((lane & 16) ? 256 + (k8 ^ 4) * 16 + 8 : (lane & 2) * 64 + k8 * 16 + (lane & 1) * 8);
            BARRIER();
            const u32x4 q0 = lds_r128(L_QS + (jq * 2 + h) * 1152 + l16);
            const unsigned q1w = lds_r32(L_QS + (jq * 2 + h) * 1152 + 1024 + r * 4);
            u32x4 km = lds_r128(ka + 8 * 5120), kl = lds_r128(la + 8 * 5120);
            u32x2 vf[2][4];
            att_vload(vf[0], va + 8 * 5120);
            STAMP(layer * 12 + 1);
            const u32x4 q1 = {hh ? 0u : q1w, hh ? 0u : 0x3F80u, 0u, 0u};
            f32x16 sc[2], negm, o16 = splat16(0.f);
            sc[0] = att_scores(km, kl, q0, q1, msk);
            {
                const float m = max16(sc[0], NEGBIG);
                const float nm = -vmax(sw32(m, m));
                negm = splat16(nm);
                sc[0] += negm;
                msk = (splat16(NEGBIG) - msk) + negm;
            }
#pragma unroll
            for (int i = 8; i >= 0; --i) {
                if (i >= 5 && i == 8 - jq) {
                    RETRY(layer * 8 + 4, hload(), umax4(umax4(umax4(umax4(umax4(0u, hv[0]), hv[1]), hv[2]), hv[3]), hv[4]));
#pragma unroll
                    for (int j = 0; j < 5; ++j) lds_w128(L_KT + wave * 5120 + j * 1024 + l16, hv[j]);
                    BARRIER();
                    STAMP(layer * 12 + 6);
                }
                SB();
                if (i > 0) { km = lds_r128(ka + (i - 1) * 5120); kl = lds_r128(la + (i - 1) * 5120); att_vload(vf[(i - 1) & 1], va + (i - 1) * 5120); }
                wof[16 - 2 * i] = ld128c(wsr, l16 + (16 - 2 * i) * 1024, wob); wof[17 - 2 * i] = ld128c(wsr, l16 + (17 - 2 * i) * 1024, wob);
                SB();
                const u32x4 p0 = att_p(sc[i & 1], 0);
                SB();
                if (i > 0) sc[(i - 1) & 1] = att_scores(km, kl, q0, q1, i == 1 ? msk : negm);
                o16 = mfmaL((u32x4){vf[i & 1][0].x, vf[i & 1][0].y, vf[i & 1][1].x, vf[i & 1][1].y}, p0, o16);
                SB();
                const u32x4 p1 = att_p(sc[i & 1], 8);
                if (i < 7 && !last) {
                    if (i > 0 || wave < 6) dma1k(nimg + 8 * (6 - i) * 1024, l16, w1k + 8 * (6 - i) * 1024);
                }
                SB();
                o16 = mfmaL((u32x4){vf[i & 1][2].x, vf[i & 1][2].y, vf[i & 1][3].x, vf[i & 1][3].y}, p1, o16);
            }
            asm volatile("s_nop 7\n\ts_nop 7" : "+v"(o16));
            {
                float t = 0.f;
#pragma unroll
                for (int v = 0; v < 11; ++v) t += __builtin_fabsf(o16[v]);
                if (__any(!(t < 3.0e38f))) {
                    float m = NEGBIG;
#pragma unroll 1
                    for (int pass = 0; pass < 2; ++pass) {
                        o16 = splat16(0.f);
#pragma unroll 1
                        for (int i = 0; i <= 8; ++i) {
                            u32x2 v4[4]; att_vload(v4, va + i * 5120);
                            f32x16 s = att_scores(lds_r128(ka + i * 5120), lds_r128(la + i * 5120), q0, q1, splat16(pass ? -m : 0.f));
                            const int lim = i == 8 ? dl : 99, lim2 = i == 0 ? dl : -99;
#pragma unroll
                            for (int v = 0; v < 16; ++v) { const int cv = (v & 3) + 8 * (v >> 2); if (cv > lim || cv <= lim2) s[v] = NEGBIG; }
                            if (pass) att_accum(s, v4, o16); else m = max16(s, m);
                        }
                        if (!pass) m = vmax(sw32(m, m));
                    }
                    asm volatile("s_nop 7\n\ts_nop 7" : "+v"(o16));
                }
            }
            STAMP(layer * 12 + 7);
            const float inv = rcp(o16[10]);
            const unsigned ob = obase + (g * 8 + 2 * jq) * TKT;
            const unsigned vo = (r >> 4) * TKT + (r & 15) * 16;
            st128c(wsr, vo + hh * 256, ob + p * 1024 + h * 512, ok4((u32x4){pk2(o16[0] * inv, o16[1] * inv), pk2(o16[2] * inv, o16[3] * inv), pk2(o16[4] * inv, o16[5] * inv), pk2(o16[6] * inv, o16[7] * inv)}));
            if (hh == 0) st32c(wsr, (r >> 4) * TKT + (r & 15) * 4, ob + 16384 + p * 128 + h * 64, ok1(pk2(o16[8] * inv, o16[9] * inv)));
        } else {
            u32x4 pv[8];
            auto pload = [&]() {
                const unsigned kb = kbase + p * 16 * 3072 + wave * 256;
#pragma unroll
                for (int i = 0; i < 8; ++i) if (i < 4 || wave < 4) pv[i] = ld128c(wsr, tk * 16 + gq * 3072, kb + (i & 3) * (4 * 3072) + (i >> 2) * 2048);
            };
#pragma unroll
            for (int i = 4; i < 8; ++i) pv[i] = (u32x4){0u, 0u, 0u, 0u};
            if (PSLEEP) __builtin_amdgcn_s_sleep(PSLEEP);
            pload();
            u32x4 pk = {tag, tag, tag, tag};
            if (wave == 7) pk = peek256(flag_at(P.ws, it ? FL_W5 : FL_W2, 0), lane);
            u32x4 fq[2]; unsigned fe;
            dual_softmax(q, rstd * LOG2E, gq < 2, fq[0], fq[1], fe);
            const unsigned q4 = (lane >> 2) & 3, c4 = lane & 3, fm = (gq & 1) * 8 + q4, fx = (gq & 1) * 2 + (q4 & 1);
            const unsigned ra = L_KVS + (gq >> 1) * 512 + c4 * 128 + fm * 8, rb = L_KVS + 1024 + (gq >> 1) * 128 + fm * 8;
            const unsigned rc = L_KVS + 1280 + c4 * 32 + fx * 8, rd = L_KVS + 1408 + fx * 8;
            {
                STAMP(layer * 12 + 6);
                const unsigned k0 = (wave == 5 && tk >= 10) ? 0u : ~0u, k1 = (wave == 3 && tk >= 10) ? 0u : ~0u;
                RETRY(layer * 8 + 4, pload(), __builtin_elementwise_max(k0 & umax4(umax4(umax4(umax4(0u, pv[0]), pv[1]), pv[2]), pv[3]), k1 & umax4(umax4(umax4(umax4(0u, pv[4]), pv[5]), pv[6]), pv[7])));
                const u32x4 sel = onehot8(tk < 8 ? tk : -2);
                f32x4 s0 = Z4, s1 = Z4;
#pragma unroll
                for (int i = 0; i < 4; ++i) { s0 = mfma32(sel, pv[i], s0); s1 = mfma32(sel, pv[4 + i], s1); }
                const u32x2 w0 = {pk2(s0[0], s0[1]), pk2(s0[2], s0[3])}, w1v = {pk2(s1[0], s1[1]), pk2(s1[2], s1[3])};
                if (gq < 2) {
                    lds_w64(L_KVS + wave * 256 + tk * 16 + gq * 8, w0);
                    if (wave < 4) lds_w64(L_KVS + 2048 + wave * 256 + tk * 16 + gq * 8, w1v);
                }
            }
            judge256(pk, flag_at(P.ws, it ? FL_W5 : FL_W2, 0), lane, tag);
            if (CSPB && first && wave == 6) colsum_get(wsr, 16 + p, L_CS + 448, lane);
            STAMP(57 + it);
            BARRIER();
            f32x4 oa[2][2];
            {
                u32x2 am[2][4], ae[2][2];
#pragma unroll
                for (int h = 0; h < 2; ++h) {
                    am[h][0] = lds_tr(ra + h * 1536); am[h][1] = lds_tr(ra + h * 1536 + 32); am[h][2] = lds_tr(rb + h * 1536); am[h][3] = lds_tr(rb + h * 1536 + 32);
                    ae[h][0] = lds_tr(rc + h * 1536); ae[h][1] = lds_tr(rd + h * 1536);
                }
                SB();
#pragma unroll
                for (int h = 0; h < 2; ++h) {
                    oa[h][0] = mfma32((u32x4){am[h][0].x, am[h][0].y, am[h][1].x, am[h][1].y}, fq[h], Z4);
                    oa[h][1] = mfma32((u32x4){am[h][2].x, am[h][2].y, am[h][3].x, am[h][3].y}, fq[h], Z4);
                }
#pragma unroll
                for (int h = 0; h < 2; ++h) {
                    const u32x4 fh = w1((gq >> 1) == h ? fe : 0u);
                    oa[h][1] = mfma32(w1(ae[h][1].x), fh, oa[h][1]); oa[h][0] = mfma32(w1(ae[h][0].x), fh, oa[h][0]);
                }
            }
            STAMP(layer * 12 + 7);
            const float ia = rcp(oa[0][1][2] + 1e-6f), ib = rcp(oa[1][1][2] + 1e-6f);
            const unsigned ob = obase + (g * 8 +
        u32x4 wox[18];
        if (wave >= 6) {
            const unsigned wb = WS_WOI + ((ISB ? 36 : 0) + 9 * cg + 8) * TKT;
#pragma unroll
            for (int s = 0; s < 18; ++s) wox[s] = ld128c(wsr, l16 + s * 1024, wb);
            if (ISB && it == 0 && wave == 7) wait_all256(flag_at(P.ws, FL_W4, 0), lane, tag);
        } else {
            const unsigned ob = obase + (g * 8 + 2 * tp) * TKT + w1k;
            u32x4 ov[6];
            auto oload = [&]() {
#pragma unroll
                for (int i = 0; i < 6; ++i) ov[i] = ld128c(wsr, l16 + i * 6144, ob);
            };
            if (OSLEEP) __builtin_amdgcn_s_sleep(OSLEEP);
            oload();
            RETRY(layer * 8 + 5, oload(), umax4(umax4(umax4(umax4(umax4(umax4(0u, ov[0]), ov[1]), ov[2]), ov[3]), ov[4]), ov[5]));
            STAMP(layer * 12 + 8);
#pragma unroll
            for (int i = 0; i < 6; ++i) {
                if ((i == 2 || i == 5) && wave >= 4) {
                    const unsigned d = L_OST + w1k + i * 6144 + (lane >> 4) * 256 + (lane & 3) * 64 + (lane & 12);
                    lds_w32(d, ov[i].x); lds_w32(d + 16, ov[i].y); lds_w32(d + 32, ov[i].z); lds_w32(d + 48, ov[i].w);
                } else lds_w128(L_OST + w1k + i * 6144 + l16, ov[i]);
            }
        }
        if (first && blockIdx.x == 0 && wave == 0 && lane == 0) __hip_atomic_store((GAS unsigned*)P.ws, tag, RLX_AGENT);
        BARRIER();
        STAMP(layer * 12 + 9);
        {
            f32x4 c0 = res[0], c1 = res[1], c2 = res[2];
            const unsigned lo = L_OST + l16;
            if (wave >= 6) {
                u32x4 ox[18];
#pragma unroll
                for (int s = 0; s < 18; ++s) ox[s] = lds_r128(lo + (wave & 1) * TKT + s * 1024);
                SB();
#pragma unroll
                for (int s = 0; s < 18; ++s) c2 = mfma32(wox[s], ox[s], c2);
            }
            SB();
            u32x4 of[4][2];
#pragma unroll
            for (int s = 0; s < 3; ++s) { of[s][0] = lds_r128(lo + s * 1024); of[s][1] = lds_r128(lo + TKT + s * 1024); }
#pragma unroll
            for (int s = 0; s < 18; ++s) {
                SB();
                c0 = mfma32(wof[s], of[s & 3][0], c0);
                SB();
                if (s + 3 < 18) of[(s + 3) & 3][0] = lds_r128(lo + (s + 3) * 1024);
                SB();
                c1 = mfma32(wof[s], of[s & 3][1], c1);
                SB();
                if (s + 3 < 18) of[(s + 3) & 3][1] = lds_r128(lo + TKT + (s + 3) * 1024);
                if (ISB) { if ((s & 1) == 0 && s < 10 && !last && (s < 8 || wave < 4)) dma1k(nimg + (54 + 4 * s) * 1024, l16, w1k + (54 + 4 * s) * 1024); }
                else if (s < 12 && (s < 11 || wave < 2)) dma1k(nimg + 8 * s * 1024, l16, w1k + 8 * s * 1024);
            }
            res[0] = c0; res[1] = c1; res[2] = c2;
            STAMP(layer * 12 + 10);
            const u32x4 v = unit_pair(pk2(c0[0], c0[1]), pk2(c0[2], c0[3]), pk2(c1[0], c1[1]), pk2(c1[2], c1[3]));
            const u32x2 v2 = {pk2(c2[0], c2[1]), pk2(c2[2], c2[3])};
            const int ct = 9 * cg + wave, cx = 9 * cg + 8;
            const int tx = 2 * tp + (wave & 1);
            if (!last) {
                const unsigned xb = obase + ACT_BYTES + g * 8 * TKT, sb = obase + 2 * ACT_BYTES + g * 8 * 2304;
                const unsigned vo = (gq >> 1) * 256 + tk * 16;
                st128c(wsr, (gq & 1) * TKT + vo, xb + 2 * tp * TKT + ct * 512, ok4(v));
                const f32x2 a0 = {c0[0], c0[1]}, a1 = {c0[2], c0[3]}, b0 = {c1[0], c1[1]}, b1 = {c1[2], c1[3]};
                const f32x2 qa = fma2(a1, a1, a0 * a0), qb = fma2(b1, b1, b0 * b0);
                const f32x2 u = sw16(qa.x + qa.y, qb.x + qb.y); const float u1 = u.x + u.y; const f32x2 u2 = sw32(u1, u1);
                if (gq < 2) st32c(wsr, gq * 2304 + tk * 4, sb + 2 * tp * 2304 + ct * 64, ok1(u_of(u2.x + u2.y)));
                if (wave >= 6) {
                    st64c(wsr, vo + (gq & 1) * 8, xb + tx * TKT + cx * 512, ok2(v2));
                    const f32x2 d0 = {c2[0], c2[1]}, d1 = {c2[2], c2[3]}, qd = fma2(d1, d1, d0 * d0);
                    const float e0 = qd.x + qd.y; const f32x2 e1 = sw16(e0, e0); const float e2 = e1.x + e1.y; const f32x2 e3 = sw32(e2, e2);
                    if (gq == 0) st32c(wsr, tk * 4, sb + tx * 2304 + cx * 64, ok1(u_of(e3.x + e3.y)));
                }

            } else {
                const int tt = 2 * tp + (gq & 1);
                *(u32x4*)(P.out + (size_t)(g * 128 + tt * 16 + tk) * DM + 16 * ct + 8 * (gq >> 1)) = v;
                if (wave >= 6) *(u32x2*)(P.out + (size_t)(g * 128 + tx * 16 + tk) * DM + 16 * cx + 4 * gq) = v2;
            }
        }
if (!last) {
                const unsigned xb = obase + ACT_BYTES + g * 8 * TKT, sb = obase + 2 * ACT_BYTES + g * 8 * 2304;
                const unsigned vo = (gq >> 1) * 256 + tk * 16;
                st128c(wsr, (gq & 1) * TKT + vo, xb + 2 * tp * TKT + ct * 512, ok4(v));
                const f32x2 a0 = {c0[0], c0[1]}, a1 = {c0[2], c0[3]}, b0 = {c1[0], c1[1]}, b1 = {c1[2], c1[3]};
                const f32x2 qa = fma2(a1, a1, a0 * a0), qb = fma2(b1, b1, b0 * b0);
                const f32x2 u = sw16(qa.x + qa.y, qb.x + qb.y); const float u1 = u.x + u.y; const f32x2 u2 = sw32(u1, u1);
                if (gq < 2) st32c(wsr, gq * 2304 + tk * 4, sb + 2 * tp * 2304 + ct * 64, ok1(u_of(u2.x + u2.y)));
                if (wave < 2) {
                    st64c(wsr, vo + (gq & 1) * 8, xb + tx * TKT + cx * 512, ok2(v2));
                    const f32x2 d0 = {c2[0], c2[1]}, d1 = {c2[2], c2[3]}, qd = fma2(d1, d1, d0 * d0);
                    const float e0 = qd.x + qd.y; const f32x2 e1 = sw16(e0, e0); const float e2 = e1.x + e1.y; const f32x2 e3 = sw32(e2, e2);
                    if (gq == 0) st32c(wsr, tk * 4, sb + tx * 2304 + cx * 64, ok1(u_of(e3.x + e3.y)));
                }

            } else {
                const int tt = 2 * tp + (gq & 1);
                *(u32x4*)(P.out + (size_t)(g * 128 + tt * 16 + tk) * DM + 16 * ct + 8 * (gq >> 1)) = v;
                if (wave < 2) *(u32x2*)(P.out + (size_t)(g * 128 + tx * 16 + tk) * DM + 16 * cx + 4 * gq) = v2;
            }
        }
        STAMP(layer * 12 + 11);
    }
    if (ISB) {
#pragma unroll
        for (int s = 0; s < 18; ++s) asm volatile("" : "=v"(xf[s]));
#pragma unroll
        for (int t = 0; t < 5; ++t) asm volatile("" : "=v"(acc[t]));
        asm volatile("" : "=v"(ssq));
    }
}

__global__ void __launch_bounds__(512, 2) fused_kernel(Params P) {
    const int tid = threadIdx.x, lane = tid & 63, wave = __builtin_amdgcn_readfirstlane(tid >> 6);
    const int b = blockIdx.x, g = ((b >> 1) & 3) * 4 + (b >> 6), p = (b & 1) * 8 + ((b >> 3) & 7);
    const rsrc_t wsr = mk_rsrc(P.ws, WS_END);
    STAMP(0);
    if (lane < 3) {
        unsigned long long pc; unsigned keep;
        asm volatile("s_getpc_b64 %0" : "=s"(pc));
        const unsigned o = (((b >> 3) * 8 + wave) * 3 + lane) * 64;
        asm volatile("s_mov_b32 %0, m0\n\ts_mov_b32 m0, %3\n\ts_nop 0\n\tglobal_load_lds_dword %1, %2\n\ts_mov_b32 m0, %0" : "=&s"(keep) : "v"(o < CODE_BYTES ? o : 0u), "s"(pc), "s"(L_CS + 64 + wave * 16) : "memory");
    }
    u32x4 xf[18];
    f32x4 res[3];
    f32x4 acc[7], sq;
    float ssq;
    Ctx C; C.wave = wave; C.g = g; C.p = p;
    {
        const int gq = lane >> 4, tk = lane & 15;
        const f32x4 Z4 = {0.f, 0.f, 0.f, 0.f};
        const unsigned epv = __hip_atomic_load((const GAS unsigned*)P.ws, RLX_AGENT);
        const u16* xs = P.x + (size_t)(g * 128) * DM;
        u32x4 raw[8];
        unsigned ph0, ph1;
        const unsigned c0 = (unsigned)(gq == 1 ? 8 : gq == 2 ? 10 : 0);
        const unsigned wv = ((unsigned)tk * NQKV + c0) * 2u, wo = p * 36 + wave * 576;
        if (wave >= 4) {
            const unsigned xo = ((unsigned)(lane >> 3) * DM + 8u * ((lane & 7) ^ (lane >> 3))) * 2u;
            __builtin_amdgcn_s_sleep(DSLEEP);
#pragma unroll 1
            for (int j = 0; j < 4; ++j) {
                const unsigned oc = (wave - 4 + 4 * j + 2 * p) & 15;
                const u16* src = xs + oc * 8 * DM;
                unsigned dst = (oc >> 1) * TKT + (oc & 1) * 1024, keep, vo = xo;
                asm volatile("s_mov_b32 %0, m0\n\t"
                             ".rept 9\n\ts_mov_b32 m0, %1\n\ts_add_u32 %1, %1, 0x800\n\tglobal_load_lds_dwordx4 %2, %3\n\tv_add_u32 %2, 0x80, %2\n\t.endr\n\t"
                             "s_mov_b32 m0, %0" : "=&s"(keep), "+s"(dst), "+v"(vo) : "s"(src) : "memory", "scc");
            }
            STAMP(49);
        }
        SB();
        u32x4 im, ix, lraw[8];
        unsigned wdst = 0, km[2], kx;
        auto unit_tile = [&](const u32x4* raw, const u32x4 (&bm)[2], const u32x4 (&bl)[2], unsigned pb) {
            u32x2 lf[4];
#pragma unroll
            for (int i = 0; i < 4; ++i) {
                const f32x4 cm = mfma32(raw[i], bm[i >> 1], Z4), cl = mfma32(raw[i], bl[i >> 1], Z4);
                lds_w64(km[i >> 1] + pb + (i & 1) * 512, (u32x2){pk2(cm[0], cm[1]), pk2(cm[2], cm[3])});
                lf[i] = (u32x2){pk2(cl[0], cl[1]), pk2(cl[2], cl[3])};
            }
            if (tk < 2) {
#pragma unroll
                for (int i = 0; i < 4; ++i) lds_w64(kx + pb + (i >> 1) * 128 + (i & 1) * 512, lf[i]);
            }
        };
        auto meet = [&](unsigned word, unsigned val) {
            asm volatile("s_waitcnt lgkmcnt(0)" ::: "memory");
            if (lane == 0) lds_w32(word + wave * 4, val);
#pragma unroll 1
            for (unsigned sp = 0; sp < SPIN_CAP; ++sp) { asm volatile("" ::: "memory"); if (__all(lds_r32(word + (lane < 3 ? lane : 0) * 4) == val)) break; }
        };
        if (wave < 3) {
#pragma unroll
            for (int i = 0; i < 4; ++i) ld128_raw(raw[i], P.wqkvA + wo + g * (32 * NQKV) + (i & 1) * (16 * NQKV) + (i >> 1) * 18, wv);
#pragma unroll
            for (int i = 0; i < 4; ++i) ld128_raw(raw[4 + i], P.wqkvA + wo + (16 + (g & 1)) * (32 * NQKV) + (i & 1) * (16 * NQKV) + (i >> 1) * 18, g < 2 ? wv : 0u);
            asm volatile("global_load_dword %0, %2, %4\n\tglobal_load_dword %1, %3, %4" : "=&v"(ph0), "=&v"(ph1) : "v"(tid * 4u), "v"((tid < 132 ? tid + 192 : 0) * 4u), "s"(P.wphi + p * 648) : "memory");
            SB();
            { const int l2 = fresh(lane); im = onehot8(((l2 >> 3) & 1) == (l2 >> 4) ? (l2 & 7) : -2); ix = onehot8((l2 >> 4) == 2 ? 6 + (l2 & 1) : -2); }
            constexpr unsigned L_PHI = L_PST + 7168;
            unsigned pa[2];
#pragma unroll
            for (int i = 0; i < 2; ++i) {
                const int z = tid + 192 * i, row = z / 7, j = z - 7 * row;
                if (z < 252) lds_w32(L_PHI + row * 64 + (j < 3 ? 32 + 4 * j : 36 + 4 * j), 0u);
                const int e = 2 * z, hh = e >= 324, r = e - 324 * hh, d = r / 18, f = r - 18 * d;
                pa[i] = L_PHI + (unsigned)(hh * 18 + f) * 64u + (unsigned)(d < 16 ? d : d + 6) * 2u;
            }
            const unsigned sb = wave == 0 ? 19 : wave == 2 ? 9 : 0, sl = sb + 8;
            const unsigned ko = L_PST + (gq >> 1) * 256 + (gq & 1) * 8;
#pragma unroll
            for (int h = 0; h < 2; ++h) { const unsigned sm = sb + 4 * h + (tk & 3); km[h] = ko + (tk >> 2) * 64 + (sm >> 2) * 1024 + (sm & 3) * 16; }
            kx = ko + (tk & 1) * 64 + (sl >> 2) * 1024 + (sl & 3) * 16;
            const unsigned onea = L_PST + 4096 + (tk >> 2) * 256 + (4 * (tk & 3) + 2) * 16;
            const u32x4 one4 = {0x3F803F80u, 0x3F803F80u, 0x3F803F80u, 0x3F803F80u};
            if (wave == 2 && lane < 16) lds_w128(onea, one4);
            asm volatile("s_waitcnt vmcnt(0)" : "+v"(ph0), "+v"(ph1), "+v"(raw[0]), "+v"(raw[1]), "+v"(raw[2]), "+v"(raw[3]), "+v"(raw[4]), "+v"(raw[5]), "+v"(raw[6]), "+v"(raw[7]) :: "memory");
            lds_w16(pa[0], ph0); lds_w16(pa[0] + 64, ph0 >> 16);
            if (tid < 132) { lds_w16(pa[1], ph1); lds_w16(pa[1] + 64, ph1 >> 16); }
            const unsigned tag = __builtin_amdgcn_readfirstlane(epv) * 0x9E3779B1u + 0x7F4A7C15u;
            meet(L_CS + 32, tag);
            u32x4 bm[2] = {im, im}, bl[2] = {ix, ix};
            if (wave < 2) {
#pragma unroll
                for (int h = 0; h < 2; ++h) { bm[h] = lds_r128(L_PHI + (h * 18 + tk) * 64 + gq * 16); bl[h] = lds_r128(L_PHI + (h * 18 + 16 + (tk & 1)) * 64 + gq * 16); }
            }
            STAMP(48);
            unit_tile(raw, bm, bl, 0);
            meet(L_CS, tag);
            if (wave == 2 && lane < 16) lds_w128(onea + 7168, one4);
            if (g < 2) { unit_tile(raw + 4, bm, bl, 7168); meet(L_CS + 48, tag); }
            const unsigned o = WS_IMA + p * IMG_BYTES + wave * TKT, la = L_PST + wave * 1024 + lane * 16;
#pragma unroll
            for (int u = 0; u < 2; ++u) {
                if (u == 0 || g < 2) {
                    const unsigned oo = o + (u ? 16 + g : g) * 1024;
                    const unsigned ks = p * 18 + (u ? 16 + g : g);
                    put_frag<CSPA>(wsr, oo, ks, wave, lds_r128(la + u * 7168), lane); put_frag<CSPA>(wsr, oo + 3 * TKT, ks, wave + 3, lds_r128(la + u * 7168 + 3072), lane);
                    if (wave == 0) put_frag<CSPA>(wsr, oo + 6 * TKT, ks, 6, lds_r128(la + u * 7168 + 6144), lane);
                }
            }
            DRAIN();
            if (lane == 0) set_flag(P.ws, FL_IA, p * 64 + g * 3 + wave, tag);
            STAMP(63);
            const rsrc_t rw = mk_rsrc(P.wqkvB, DM * NQKV * 2);
#pragma unroll
            for (int i = 0; i < 8; ++i) lraw[i] = __builtin_amdgcn_raw_buffer_load_b128(rw, (i < 4 || (g >> 1) == 1) ? wv : 0x7fffffffu,
                                                      (wo + (i < 4 ? g : 16 + (g & 1)) * (32 * NQKV) + (i & 1) * (16 * NQKV) + ((i >> 1) & 1) * 18) * 2, 0);
        } else {
            { const int l2 = fresh(lane); im = onehot8(((l2 >> 3) & 1) == (l2 >> 4) ? (l2 & 7) : -2); ix = onehot8(((l2 >> 3) & 1) + 2 == (l2 >> 4) ? (l2 & 7) : -2); }
#pragma unroll
            for (int i = 2; i < 8; ++i) lraw[i] = im;
            const int u = b + 256 * (wave - 3), m = u >= 324, r = u - 324 * m, cp = r / 18, s = r - 18 * cp;
            const unsigned w0 = (unsigned)(wo_krow(m, s, tk >> 2, tk & 3) * DM + 8 * gq) * 2u, w1o = (unsigned)(wo_krow(m, s, tk >> 2, 4 + (tk & 3)) * DM + 8 * gq) * 2u;
            const rsrc_t rw = mk_rsrc(m ? P.woB : P.woA, u < 648 ? DM * DM * 2 : 0);
            asm volatile("" :: "v"(epv));
            lraw[0] = __builtin_amdgcn_raw_buffer_load_b128(rw, w0, 64 * cp, 0); lraw[1] = __builtin_amdgcn_raw_buffer_load_b128(rw, w1o, 64 * cp, 0);
            wdst = u < 648 ? WS_WOI + (unsigned)((m * 36 + 2 * cp) * 18 + s) * 1024u : 0u;
            asm volatile("s_waitcnt vmcnt(2)" ::: "memory");
            STAMP(48);
        }
        C.tag = __builtin_amdgcn_readfirstlane(epv) * 0x9E3779B1u + 0x7F4A7C15u;
        BARRIER();
        STAMP(50);
        {
            const unsigned xa = (tk >> 3) * 1024 + (tk & 7) * 128;
#pragma unroll
            for (int s = 0; s < 18; ++s) xf[s] = lds_r128(wave * TKT + (s >> 1) * 2048 + xa + (((4 * (s & 1) + gq) ^ (tk & 7)) * 16));
            const int tp = (p >> 1) & 3, cg = (p >> 3) * 2 + (p & 1);
#pragma unroll
            for (int i = 0; i < 3; ++i) {
                const int tt = 2 * tp + (i < 2 ? i : (wave & 1)), ct = 9 * cg + (i < 2 ? wave : 8);
                const u32x2 r = *(const LAS u32x2*)((LAS unsigned char*)smem + tt * TKT + (ct >> 2) * 2048 + xa + (((2 * (ct & 3) + (gq >> 1)) ^ (tk & 7)) * 16) + (gq & 1) * 8);
                res[i] = (f32x4){f_of(r.x << 16), f_of(r.x & 0xffff0000u), f_of(r.y << 16), f_of(r.y & 0xffff0000u)};
            }
        }
        if (wave == 3) wait_flags(flag_at(P.ws, FL_IA, p * 64), (lane < 48 ? lane : 0) * 128, C.tag);
        BARRIER();
        STAMP(51);
        const unsigned char* img = P.ws + WS_IMA + p * IMG_BYTES;
        unsigned fo[4];
#pragma unroll
        for (int i = 0; i < 4; ++i) { const int j = i * 8 + wave, jt = j / 6; fo[i] = (j < 30 ? jt * 12 + j : 60 + j) * 1024; }
        const unsigned st3 = wave < 6 ? 6144 : 2048;
        auto piece = [&](int c, int i) {
            unsigned f = c < 3 ? fo[i] + c * (i < 3 ? 6144 : st3) : (96 + i * 8 + wave) * 1024;
            if (c == 3 && i == 3) f = f < 125 * 1024 ? f : 125 * 1024;
            dma1k(img + f, lane * 16, f);
        };
#pragma unroll
        for (int i = 0; i < 8; ++i) asm volatile("" :: "v"(lraw[i]));
#pragma unroll
        for (int i = 0; i < 8; ++i) piece(i >> 2, i & 3);
        STAMP(1);
        if (wave < 3) {
            const u32x4 bm[2] = {im, im}, bl[2] = {ix, ix};
            unit_tile(lraw, bm, bl, 0);
            if ((g >> 1) == 1) unit_tile(lraw + 4, bm, bl, 7168);
        } else {
            wdst = __builtin_amdgcn_readfirstlane(wdst);
            if (wdst) {
                const f32x4 a0 = mfma32(lraw[0], im, Z4), a1 = mfma32(lraw[1], im, Z4), c0 = mfma32(lraw[0], ix, Z4), c1 = mfma32(lraw[1], ix, Z4);
                st128c(wsr, lane * 16, wdst, (u32x4){pk2(a0[0], a0[1]), pk2(a0[2], a0[3]), pk2(a1[0], a1[1]), pk2(a1[2], a1[3])});
                st128c(wsr, lane * 16, wdst + TKT, (u32x4){pk2(c0[0], c0[1]), pk2(c0[2], c0[3]), pk2(c1[0], c1[1]), pk2(c1[2], c1[3])});
            }
            if (wave >= 5) arm_unit(wsr, lane * 16, wave - 5, b);
            if (CSPA && wave == 7) colsum_get(wsr, p, L_CS, lane);
        }
#pragma unroll
        for (int t = 0; t < 7; ++t) acc[t] = Z4;
        sq = Z4;
        const unsigned lb0[3] = {(unsigned)lane * 16, (unsigned)lane * 16 + 6144, (unsigned)lane * 16 + 12288};
        if (wave < 3) asm volatile("s_waitcnt vmcnt(4)" ::: "memory"); else if (wave == 5) asm volatile("s_waitcnt vmcnt(5)" ::: "memory");
        else if (wave < 5) asm volatile("s_waitcnt vmcnt(6)" ::: "memory"); else asm volatile("s_waitcnt vmcnt(5)" ::: "memory");
        BARRIER();
        STAMP(2);
        gemm_img<0, 5, 0, 6, true, 2>(acc, xf, lb0, sq, [&](int s) {
            if (s < 2 && wave < 7 && (s == 0 || (g >> 1) == 1)) put_frag<CSPB>(wsr, WS_IMB + p * IMG_BYTES + wave * TKT + (s ? 14 + g : g) * 1024, (16 + p) * 18 + (s ? 14 + g : g), wave, lds_r128(L_PST + s * 7168 + wave * 1024 + lane * 16), lane);
            if (s >= 2) piece(2, s - 2); });
        STAMP(25);
        asm volatile("s_waitcnt vmcnt(4)" ::: "memory"); BARRIER();
        if (tid == 0) set_flag(P.ws, FL_W1, g * 16 + p, C.tag);
        STAMP(26);
        gemm_img<0, 5, 6, 12, true, 2>(acc, xf, lb0, sq, [&](int s) { if (s < 10) piece(3, s - 6); if (s >= 10) arm_unit(wsr, lane * 16, 3 + (s - 10) * 8 + wave, b); });
        STAMP(27);
        asm volatile("s_waitcnt vmcnt(6)" ::: "memory"); BARRIER();
        STAMP(28);
        gemm_img<0, 5, 12, 18, true, 2>(acc, xf, lb0, sq, [&](int s) { if (s < 17 || wave < 4) arm_unit(wsr, lane * 16, 19 + (s - 12) * 8 + wave, b); });
        STAMP(29);
        asm volatile("s_nop 7" : "+v"(sq));
        const float d0 = (tk & 1) ? sq[1] : sq[0], d1 = (tk & 1) ? sq[3] : sq[2], dg = (tk & 2) ? d1 : d0;
        ssq = gq == (tk >> 2) ? dg : 0.f;
        if (!CSPA) { if (wave < 4) colsum_tile(wave, L_CS, lane); if (wave == 0) colsum_tile(4, L_CS, lane); BARRIER(); }
    }
#pragma unroll 1
    for (int it = 0; it < NIT; ++it) {
        layer_fn<false>(P, wsr, lane, C, it, xf, res, acc, ssq);
        layer_fn<true>(P, wsr, lane, C, it, xf, res, acc, ssq);
    }
}

extern "C" void kernel_launch(void* const* d_in, const int* in_sizes, int n_in, void* d_out, int out_size, void* d_ws, size_t ws_size, hipStream_t stream) {
    Params P;
    P.x = (const u16*)d_in[0]; P.wqkvA = (const u16*)d_in[1]; P.wphi = (const u16*)d_in[2]; P.woA = (const u16*)d_in[3];
    P.wqkvB = (const u16*)d_in[5]; P.woB = (const u16*)d_in[6];
    P.out = (u16*)d_out; P.ws = (unsigned char*)d_ws;
    (void)hipFuncSetAttribute((const void*)fused_kernel, hipFuncAttributeMaxDynamicSharedMemorySize, LDS_BYTES);
    hipLaunchKernelGGL(fused_kernel, dim3(256), dim3(512), LDS_BYTES, stream, P);
}
```

```cpp
#define ECHO 0
#include <hip/hip_runtime.h>
#include <stdint.h>
#define ODRAIN 0
#define DSLEEP 16
#define CSPA 0
#define CSPB 1
#define BAR2 1
#define WOBAL 1
#define OSLEEP 10
#define PSLEEP 0
#define XSLEEP 10
#define RSLEEP 6
#define NIT 2
#define ONEPOLL 0

typedef __attribute__((ext_vector_type(8))) __bf16 bf16x8;
typedef __attribute__((ext_vector_type(2))) __bf16 bf16x2;
typedef __attribute__((ext_vector_type(4))) short s16x4;
typedef __attribute__((ext_vector_type(4))) float f32x4;
typedef __attribute__((ext_vector_type(2))) float f32x2;
typedef __attribute__((ext_vector_type(16))) float f32x16;
typedef __attribute__((ext_vector_type(4))) unsigned u32x4;
typedef __attribute__((ext_vector_type(2))) unsigned u32x2;
typedef unsigned short u16;
#define LAS __attribute__((address_space(3)))
#define GAS __attribute__((address_space(1)))
#define RLX_AGENT __ATOMIC_RELAXED, __HIP_MEMORY_SCOPE_AGENT
#define DEVI __device__ __forceinline__

constexpr int SEQ = 2048, DM = 576, NQKV = 1728;
constexpr unsigned TKT = 18 * 1024;
constexpr unsigned ACT_BYTES = 128 * TKT;
constexpr unsigned IMG_BYTES = 7 * TKT;
constexpr unsigned WS_FLAGS = 4096;
constexpr unsigned FL_W1 = 0, FL_W2 = 1, FL_W4 = 2, FL_W5 = 3, FL_IA = 4, FL_N = 8;
constexpr unsigned KVP_BYTES = 256 * 3072, KVH_BYTES = 256 * 20480, ST_BYTES = 128 * 2304, UNIT = 256 * 1024;
constexpr unsigned SETA = KVP_BYTES + 2 * ACT_BYTES + ST_BYTES;
constexpr unsigned N0 = 23, N1 = 40, N2 = 52;
constexpr unsigned WS_S0 = 524288, WS_S1 = WS_S0 + N0 * UNIT, WS_S2 = WS_S1 + N1 * UNIT;
static_assert(WS_FLAGS + FL_N * 256 * 128 <= WS_S0 && SETA <= N0 * UNIT && KVH_BYTES + 2 * ACT_BYTES + ST_BYTES <= N1 * UNIT && SETA + KVH_BYTES + ACT_BYTES <= N2 * UNIT, "map");
__host__ __device__ constexpr unsigned ws_k(int layer) { return layer == 0 ? WS_S0 : layer == 1 ? WS_S1 : layer == 2 ? WS_S2 : WS_S2 + SETA; }
__host__ __device__ constexpr unsigned ws_o(int layer) { return ws_k(layer) + ((layer & 1) ? KVH_BYTES : KVP_BYTES); }
constexpr unsigned WS_IMA = WS_S2 + N2 * UNIT;
constexpr unsigned WS_IMB = WS_IMA + 16 * IMG_BYTES;
constexpr unsigned WS_WOI = WS_IMB + 16 * IMG_BYTES;
constexpr unsigned WS_CSP = WS_WOI + 2 * 36 * TKT;
constexpr unsigned WS_END = WS_CSP + 2 * 16 * 18 * 448;
constexpr unsigned WS_DBG = WS_END;
constexpr unsigned L_OST = 5 * TKT;
constexpr unsigned L_STG = IMG_BYTES;
constexpr unsigned L_KT = L_STG - 40960;
constexpr unsigned L_QS = L_STG + 20480;
constexpr unsigned L_PK = L_STG, L_VV = L_STG + 20480;
constexpr unsigned L_KVS = 0;
constexpr unsigned L_CS = L_STG + 32768;
constexpr unsigned L_PST = 147456;
constexpr unsigned LDS_BYTES = 163840;
constexpr unsigned CODE_BYTES = 42 * 1024;
constexpr unsigned SPIN_CAP = 1u << 17;
constexpr float LOG2E = 1.4426950408889634f;
constexpr float NEGBIG = -1e30f;

struct Params {
    const u16* x; const u16* wqkvA; const u16* wphi; const u16* woA; const u16* wqkvB; const u16* woB;
    u16* out; unsigned char* ws;
};

DEVI f32x4 mfma32(u32x4 a, u32x4 b, f32x4 c) { return __builtin_amdgcn_mfma_f32_16x16x32_bf16(__builtin_bit_cast(bf16x8, a), __builtin_bit_cast(bf16x8, b), c, 0, 0, 0); }
DEVI f32x16 mfmaL(u32x4 a, u32x4 b, f32x16 c) { return __builtin_amdgcn_mfma_f32_32x32x16_bf16(__builtin_bit_cast(bf16x8, a), __builtin_bit_cast(bf16x8, b), c, 0, 0, 0); }
DEVI u32x4 w1(unsigned a) { return (u32x4){a, 0u, 0u, 0u}; }
DEVI unsigned pkv(f32x2 f) { return __builtin_bit_cast(unsigned, __builtin_convertvector(f, bf16x2)); }
DEVI unsigned pk2(float lo, float hi) { return pkv((f32x2){lo, hi}); }
DEVI float dot2(unsigned a, unsigned b, float c) { return __builtin_amdgcn_fdot2_f32_bf16(__builtin_bit_cast(bf16x2, a), __builtin_bit_cast(bf16x2, b), c, false); }
DEVI float ex2(float x) { return __builtin_amdgcn_exp2f(x); }
DEVI float rcp(float x) { return __builtin_amdgcn_rcpf(x); }
DEVI float f_of(unsigned u) { return __builtin_bit_cast(float, u); }
DEVI unsigned u_of(float f) { return __builtin_bit_cast(unsigned, f); }
DEVI f32x2 fma2(f32x2 a, f32x2 b, f32x2 c) { return __builtin_elementwise_fma(a, b, c); }
DEVI f32x2 sp2(float v) { return (f32x2){v, v}; }
DEVI f32x2 sw16(float a, float b) { const auto r = __builtin_amdgcn_permlane16_swap(u_of(a), u_of(b), false, false); return (f32x2){f_of(r[0]), f_of(r[1])}; }
DEVI f32x2 sw32(float a, float b) { const auto r = __builtin_amdgcn_permlane32_swap(u_of(a), u_of(b), false, false); return (f32x2){f_of(r[0]), f_of(r[1])}; }
DEVI float vmax(f32x2 v) { float r; asm("v_max_f32 %0, %1, %2" : "=v"(r) : "v"(v.x), "v"(v.y)); return r; }
DEVI float vmaxn(f32x2 v) { float r; asm("v_max_f32_e64 %0, -%1, -%2" : "=v"(r) : "v"(v.x), "v"(v.y)); return r; }
DEVI float max3f(float a, float b, float c) { return __builtin_fmaxf(__builtin_fmaxf(a, b), c); }
DEVI float min3f(float a, float b, float c) { return __builtin_fminf(__builtin_fminf(a, b), c); }
DEVI u32x4 unit_pair(unsigned a0, unsigned a1, unsigned b0, unsigned b1) {
    const auto s = __builtin_amdgcn_permlane16_swap(a0, b0, false, false);
    const auto t = __builtin_amdgcn_permlane16_swap(a1, b1, false, false);
    return (u32x4){s[0], t[0], s[1], t[1]};
}

#define BARRIER() asm volatile("s_waitcnt lgkmcnt(0)\n\ts_barrier" ::: "memory")
#define DRAIN() asm volatile("s_waitcnt vmcnt(0)" ::: "memory")
#define SB() __builtin_amdgcn_sched_barrier(0)

extern __shared__ __attribute__((aligned(16))) unsigned char smem[];
DEVI u32x4 lds_r128(unsigned off) { return *(const LAS u32x4*)((LAS unsigned char*)smem + off); }
DEVI f32x4 lds_rf4(unsigned off) { return *(const LAS f32x4*)((LAS unsigned char*)smem + off); }
DEVI unsigned lds_r32(unsigned off) { return *(const LAS unsigned*)((LAS unsigned char*)smem + off); }
DEVI void lds_w128(unsigned off, u32x4 v) { *(LAS u32x4*)((LAS unsigned char*)smem + off) = v; }
DEVI void lds_w64(unsigned off, u32x2 v) { *(LAS u32x2*)((LAS unsigned char*)smem + off) = v; }
DEVI void lds_w32(unsigned off, unsigned v) { *(LAS unsigned*)((LAS unsigned char*)smem + off) = v; }
DEVI void lds_w16(unsigned off, unsigned v) { *(LAS u16*)((LAS unsigned char*)smem + off) = (u16)v; }
DEVI u32x2 lds_tr(unsigned off) { return __builtin_bit_cast(u32x2, __builtin_amdgcn_ds_read_tr16_b64_v4i16((LAS s16x4*)((LAS unsigned char*)smem + off))); }

typedef __amdgpu_buffer_rsrc_t rsrc_t;
DEVI rsrc_t mk_rsrc(const void* p, unsigned bytes) { return __builtin_amdgcn_make_buffer_rsrc((void*)p, 0, bytes, 0x00020000); }
DEVI u32x4 ld128c(rsrc_t r, unsigned voff, unsigned soff) { return __builtin_amdgcn_raw_buffer_load_b128(r, voff, soff, 16); }
DEVI void st128c(rsrc_t r, unsigned voff, unsigned soff, u32x4 v) {
    __builtin_amdgcn_raw_buffer_store_b128(v, r, voff, soff, 16);
    asm volatile("s_nop 1" :: "v"(v) : "memory");
}
DEVI void st64c(rsrc_t r, unsigned voff, unsigned soff, u32x2 v) { __builtin_amdgcn_raw_buffer_store_b64(v, r, voff, soff, 16); }
DEVI void st32c(rsrc_t r, unsigned voff, unsigned soff, unsigned v) { __builtin_amdgcn_raw_buffer_store_b32(v, r, voff, soff, 16); }
DEVI void dma1k(const void* sbase, unsigned voff, unsigned lds_dst) {
    unsigned keep;
    asm volatile("s_mov_b32 %0, m0\n\ts_mov_b32 m0, %3\n\ts_nop 0\n\tglobal_load_lds_dwordx4 %1, %2 sc1\n\ts_mov_b32 m0, %0" : "=&s"(keep) : "v"(voff), "s"(sbase), "s"(lds_dst) : "memory");
}
DEVI void dma1k_in(const void* sbase, unsigned voff, unsigned lds_dst) {
    unsigned keep;
    asm volatile("s_mov_b32 %0, m0\n\ts_mov_b32 m0, %3\n\ts_nop 0\n\tglobal_load_lds_dwordx4 %1, %2\n\ts_mov_b32 m0, %0" : "=&s"(keep) : "v"(voff), "s"(sbase), "s"(lds_dst) : "memory");
}
DEVI void ld128_raw(u32x4& d, const void* base, unsigned voff) { asm volatile("global_load_dwordx4 %0, %1, %2" : "=v"(d) : "v"(voff), "s"(base) : "memory"); }
DEVI void ld64_raw(u32x2& d, const void* base, unsigned voff) { asm volatile("global_load_dwordx2 %0, %1, %2" : "=v"(d) : "v"(voff), "s"(base) : "memory"); }
DEVI void wait_flags(const unsigned char* base, unsigned voff, unsigned tag) {
    const GAS unsigned* f = (const GAS unsigned*)(base + voff);
#pragma unroll 1
    for (unsigned sp = 0; sp < SPIN_CAP; ++sp) {
        const unsigned v = __hip_atomic_load(f, RLX_AGENT);
        if (__all(v == tag)) break;
        __builtin_amdgcn_s_sleep(1);
    }
    asm volatile("" ::: "memory");
}
DEVI unsigned umax3(unsigned a, unsigned b, unsigned c) { return __builtin_elementwise_max(__builtin_elementwise_max(a, b), c); }
DEVI unsigned umax4(unsigned m, u32x4 v) { return umax3(umax3(m, v.x, v.y), v.z, v.w); }
DEVI unsigned ok1(unsigned v) { return __builtin_elementwise_min(v, 0xFFFFFFFEu); }
DEVI u32x2 ok2(u32x2 v) { return (u32x2){ok1(v.x), ok1(v.y)}; }
DEVI u32x4 ok4(u32x4 v) { return (u32x4){ok1(v.x), ok1(v.y), ok1(v.z), ok1(v.w)}; }
DEVI void arm_unit(rsrc_t r, unsigned l16, unsigned unit, unsigned cu) { st128c(r, l16, WS_S0 + (unit * 256 + cu) * 1024, (u32x4){~0u, ~0u, ~0u, ~0u}); }
DEVI void wait_all256(const unsigned char* base, int lane, unsigned tag) {
    const GAS unsigned* f = (const GAS unsigned*)(base + lane * 128);
#pragma unroll 1
    for (unsigned sp = 0; sp < SPIN_CAP; ++sp) {
        const unsigned v0 = __hip_atomic_load(f, RLX_AGENT), v1 = __hip_atomic_load(f + 2048, RLX_AGENT), v2 = __hip_atomic_load(f + 4096, RLX_AGENT), v3 = __hip_atomic_load(f + 6144, RLX_AGENT);
        if (__all(v0 == tag && v1 == tag && v2 == tag && v3 == tag)) break;
        __builtin_amdgcn_s_sleep(1);
    }
    asm volatile("" ::: "memory");
}
DEVI u32x4 peek256(const unsigned char* base, int lane) {
    const GAS unsigned* f = (const GAS unsigned*)(base + lane * 128);
    return (u32x4){__hip_atomic_load(f, RLX_AGENT), __hip_atomic_load(f + 2048, RLX_AGENT), __hip_atomic_load(f + 4096, RLX_AGENT), __hip_atomic_load(f + 6144, RLX_AGENT)};
}
DEVI void judge256(u32x4 v, const unsigned char* base, int lane, unsigned tag) {
    if (!__all(v.x == tag && v.y == tag && v.z == tag && v.w == tag)) wait_all256(base, lane, tag);
    asm volatile("" ::: "memory");
}
#define RCOUNT(site) do { } while (0)
#define RETRY(site, LOADS, MAXEXPR) do { _Pragma("unroll 1") for (unsigned sp_ = 0; sp_ < SPIN_CAP; ++sp_) { if (!__any((MAXEXPR) == 0xFFFFFFFFu)) break; \
    RCOUNT(site); if (sp_ < 4) __builtin_amdgcn_s_sleep(RSLEEP); else __builtin_amdgcn_s_sleep(48); asm volatile("" ::: "memory"); LOADS; } } while (0)
DEVI const unsigned char* flag_at(const unsigned char* ws, unsigned kind, unsigned idx) { return ws + WS_FLAGS + (kind * 256 + idx) * 128; }
DEVI void set_flag(unsigned char* ws, unsigned kind, unsigned idx, unsigned tag) {
    __hip_atomic_store((GAS unsigned*)(ws + WS_FLAGS + (kind * 256 + idx) * 128), tag, RLX_AGENT);
}

#define STAMP(i) do { if ((i) == ECHO) { const unsigned t_ = (unsigned)__builtin_amdgcn_s_memrealtime(); if (threadIdx.x == 0) lds_w32(L_CS + 1024, t_); } } while (0)

DEVI u32x4 onehot8(int j) {
    const unsigned v = (j & 1) ? 0x3F800000u : 0x3F80u; const int w = j >> 1;
    return (u32x4){w == 0 ? v : 0u, w == 1 ? v : 0u, w == 2 ? v : 0u, w == 3 ? v : 0u};
}
DEVI int wo_krow(int mat, int s, int gq, int j) {
    const int a = 36 * s + (mat ? 18 * (gq >> 1) + 8 * (j >> 2) + 4 * (gq & 1) : 18 * (j >> 2) + 4 * gq) + (j & 3);
    const int c = 36 * (8 * (s - 16) + 2 * gq + (j >> 2)) + 18 * ((j >> 1) & 1) + 16 + (j & 1);
    return s < 16 ? a : c;
}
DEVI int fresh(int v) { asm volatile("" : "+v"(v)); return v; }
DEVI int sfresh(int v) { asm volatile("" : "+s"(v)); return v; }

template <int T0, int NT, int S0, int S1, bool SSQ, int D, class F>
DEVI void gemm_img(f32x4 (&acc)[7], u32x4 (&xf)[18], const unsigned (&lb)[3], f32x4& sq, F side) {
    u32x4 a[D][NT];
#pragma unroll
    for (int s = S0; s < S0 + D; ++s)
#pragma unroll
        for (int t = 0; t < NT; ++t) a[s % D][t] = lds_r128(lb[s / 6] + (T0 + t) * TKT + (s % 6) * 1024);
    SB();
#pragma unroll
    for (int s = S0; s < S1; ++s) {
        side(s);
        SB();
        if (SSQ) { sq = mfma32(xf[s], xf[s], sq); SB(); }
#pragma unroll
        for (int t = 0; t < NT; ++t) {
            acc[T0 + t] = mfma32(a[s % D][t], xf[s], acc[T0 + t]);
            SB();
            if (s + D < S1) { a[s % D][t] = lds_r128(lb[(s + D) / 6] + (T0 + t) * TKT + ((s + D) % 6) * 1024); SB(); }
        }
    }
}
template <bool SUM>
DEVI void put_frag(rsrc_t wsr, unsigned dst, unsigned ks, int t, u32x4 f, int lane) {
    st128c(wsr, lane * 16, dst, f);
    if (!SUM) return;
    f32x4 c = mfma32((u32x4){0x3F803F80u, 0x3F803F80u, 0x3F803F80u, 0x3F803F80u}, f, (f32x4){0.f, 0.f, 0.f, 0.f});
    asm volatile("s_nop 7" : "+v"(c));
    if (lane < 16) st32c(wsr, lane * 4, WS_CSP + ks * 448 + t * 64, u_of(c[0]));
}
DEVI void colsum_get(rsrc_t wsr, unsigned img_p, unsigned csb, int lane) {
    u32x2 v[18];
#pragma unroll
    for (int s = 0; s < 18; ++s) v[s] = __builtin_amdgcn_raw_buffer_load_b64(wsr, lane < 56 ? lane * 8 : 0, WS_CSP + (img_p * 18 + s) * 448, 16);
    SB();
    f32x2 c = __builtin_bit_cast(f32x2, v[0]);
#pragma unroll
    for (int s = 1; s < 18; ++s) c += __builtin_bit_cast(f32x2, v[s]);
    if (lane < 56) lds_w64(csb + ((lane >> 1) & 3) * 112 + (lane >> 3) * 16 + (lane & 1) * 8, __builtin_bit_cast(u32x2, c));
}
DEVI void colsum_tile(int t, unsigned csb, int lane) {
    f32x4 c = {0.f, 0.f, 0.f, 0.f}; const u32x4 ones = {0x3F803F80u, 0x3F803F80u, 0x3F803F80u, 0x3F803F80u};
    u32x4 a[18];
#pragma unroll
    for (int s = 0; s < 18; ++s) a[s] = lds_r128(t * TKT + s * 1024 + lane * 16);
    SB();
#pragma unroll
    for (int s = 0; s < 18; ++s) c = mfma32(a[s], ones, c);
    asm volatile("s_nop 7" : "+v"(c));
    if ((lane & 15) == 0) lds_w128(csb + ((lane >> 4) * 28 + 4 * t) * 4, __builtin_bit_cast(u32x4, c));
}
DEVI void dual_softmax(const float (&t)[9], float ks, bool lo, u32x4& fa, u32x4& fb, unsigned& fe) {
    const float zA = lo ? t[8] : t[0], zB = lo ? t[4] : t[8];
    const float mA = max3f(max3f(t[0], t[1], t[2]), t[3], zA), nA = min3f(min3f(t[0], t[1], t[2]), t[3], zA);
    const float mB = max3f(max3f(t[4], t[5], t[6]), t[7], zB), nB = min3f(min3f(t[4], t[5], t[6]), t[7], zB);
    const float T = -ks * vmax(sw32(vmax(sw16(mA, mB)), vmaxn(sw16(nA, nB))));
    const f32x2 u = sw16(T, T);
    const f32x2 sa = sw32(u.x, u.x), sb = sw32(u.y, u.y);
    const f32x2 s8 = lo ? sa : sb, k2 = {ks, -ks};
    f32x2 e[9];
#pragma unroll
    for (int i = 0; i < 9; ++i) { const f32x2 z = fma2(sp2(t[i]), k2, i < 4 ? sa : i < 8 ? sb : s8); e[i] = (f32x2){ex2(z.x), ex2(z.y)}; }
    const float wl = lo ? 1.f : 0.f;
    const f32x2 sA = fma2(e[8], sp2(wl), (e[0] + e[1]) + (e[2] + e[3])), sB = fma2(e[8], sp2(1.f - wl), (e[4] + e[5]) + (e[6] + e[7]));
    const f32x2 p = sw16(sA.x, sB.x), q = sw16(sA.y, sB.y), w = sw32(p.x + p.y, q.x + q.y);
    const float R = rcp(w.x + w.y);
    const f32x2 v = sw16(R, R);
    const f32x2 ra = sw32(v.x, v.x), rb = sw32(v.y, v.y), r8 = lo ? ra : rb;
    fa = (u32x4){pkv(e[0] * ra), pkv(e[1] * ra), pkv(e[2] * ra), pkv(e[3] * ra)};
    fb = (u32x4){pkv(e[4] * rb), pkv(e[5] * rb), pkv(e[6] * rb), pkv(e[7] * rb)};
    fe = pkv(e[8] * r8);
}

DEVI f32x16 att_scores(u32x4 km, u32x4 kl, u32x4 q0, u32x4 q1, f32x16 c) { return mfmaL(kl, q1, mfmaL(km, q0, c)); }
DEVI u32x4 att_p(const f32x16& s, int b) { return (u32x4){pk2(ex2(s[b]), ex2(s[b + 1])), pk2(ex2(s[b + 2]), ex2(s[b + 3])), pk2(ex2(s[b + 4]), ex2(s[b + 5])), pk2(ex2(s[b + 6]), ex2(s[b + 7]))}; }
DEVI void att_accum(const f32x16& s, const u32x2 (&v)[4], f32x16& o) {
    const u32x4 p0 = {pk2(ex2(s[0]), ex2(s[1])), pk2(ex2(s[2]), ex2(s[3])), pk2(ex2(s[4]), ex2(s[5])), pk2(ex2(s[6]), ex2(s[7]))};
    o = mfmaL((u32x4){v[0].x, v[0].y, v[1].x, v[1].y}, p0, o);
    const u32x4 p1 = {pk2(ex2(s[8]), ex2(s[9])), pk2(ex2(s[10]), ex2(s[11])), pk2(ex2(s[12]), ex2(s[13])), pk2(ex2(s[14]), ex2(s[15]))};
    o = mfmaL((u32x4){v[2].x, v[2].y, v[3].x, v[3].y}, p1, o);
}
DEVI void att_vload(u32x2 (&v)[4], unsigned va) {
#pragma unroll
    for (int e = 0; e < 4; ++e) v[e] = lds_tr(va + e * 384);
}
DEVI float max16(const f32x16& s, float m) {
#pragma unroll
    for (int v = 0; v < 16; v += 2) m = max3f(m, s[v], s[v + 1]);
    return m;
}
DEVI f32x16 splat16(float v) { f32x16 r;
#pragma unroll
    for (int i = 0; i < 16; ++i) r[i] = v;
    return r; }

struct Ctx { int wave, g, p; unsigned tag; };

template <bool ISB>
DEVI void layer_fn(const Params& P, const rsrc_t wsr, const int lane_in, const Ctx& C, const int it, u32x4 (&xf)[18], f32x4 (&res)[3], f32x4 (&acc)[7], float& ssq) {
    const int wave = sfresh(C.wave), g = sfresh(C.g), p = sfresh(C.p), cu = g * 16 + p;
    const unsigned tag = C.tag;
    const int layer = 2 * it + (ISB ? 1 : 0);
    const int tp = (p >> 1) & 3, cg = (p >> 3) * 2 + (p & 1);
    const f32x4 Z4 = {0.f, 0.f, 0.f, 0.f};
    constexpr unsigned csb = L_CS + (ISB ? 448 : 0);
    const bool first = !ISB && it == 0, last = ISB && it == 1;
    u32x4 wof[18];
    float mean, rstd;
    const unsigned wob = WS_WOI + ((ISB ? 36 : 0) + 9 * cg + wave) * TKT;
    const unsigned w1k = wave * 1024;
    const unsigned kbase = ISB ? (it ? ws_k(3) : ws_k(1)) : (it ? ws_k(2) : ws_k(0)), obase = kbase + (ISB ? KVH_BYTES : KVP_BYTES);
    const int rot = first ? 0 : p % 3;
    const unsigned ro0 = rot * 6144, ro1 = rot == 2 ? 0 : ro0 + 6144, ro2 = rot ? ro0 - 6144 : 12288;
    const unsigned char* nimg = P.ws + (ISB ? WS_IMA : WS_IMB) + p * IMG_BYTES + w1k;
    {
        const int lane = fresh(lane_in), gq = lane >> 4, tk = lane & 15;
        const unsigned l16 = lane * 16;
        const unsigned lb[3] = {l16 + ro0, l16 + ro1, l16 + ro2};
        if (!first) {
#pragma unroll
            for (int t = 0; t < 5; ++t) acc[t] = Z4;
            const unsigned xprev = (ISB ? (it ? ws_o(2) : ws_o(0)) : ws_o(1)) + ACT_BYTES;
            const unsigned xb = xprev + (g * 8 + wave) * TKT, sb = xprev + ACT_BYTES + (g * 8 + wave) * 2304;
            unsigned st[9];
            auto xload = [&](int s0, int s1) {
#pragma unroll
                for (int s = s0; s < s1; ++s) xf[s] = ld128c(wsr, l16 + (s % 6) * 1024, xb + (s < 6 ? ro0 : s < 12 ? ro1 : ro2));
            };
            auto xmax = [&](int s0, int s1) { unsigned m = 0u;
#pragma unroll
                for (int s = s0; s < s1; ++s) m = umax4(m, xf[s]);
                return m; };
            auto sload = [&]() {
#pragma unroll
                for (int j = 0; j < 9; ++j) st[j] = __builtin_amdgcn_raw_buffer_load_b32(wsr, lane * 4 + j * 256, sb, 16);
            };
            asm volatile("s_waitcnt vmcnt(2)" ::: "memory");
            BARRIER();
            if (!CSPB && ISB && it == 0) { if (wave < 5) colsum_tile(wave, csb, lane); } else
            __builtin_amdgcn_s_sleep(XSLEEP);
            sload(); xload(0, 3);
            RETRY(layer * 8 + 3, { sload(); xload(0, 3); }, umax3(umax3(umax3(umax3(umax3(xmax(0, 3), st[0], st[1]), st[2], st[3]), st[4], st[5]), st[6], st[7]), st[8], 0u));
            STAMP(layer * 12 + 2);
            xload(3, 9);
            ssq = ((f_of(st[0]) + f_of(st[1])) + (f_of(st[2]) + f_of(st[3]))) + ((f_of(st[4]) + f_of(st[5])) + (f_of(st[6]) + f_of(st[7]))) + f_of(st[8]);
            if (!CSPB && ISB && it == 0) BARRIER();
            const unsigned char* cimg = P.ws + (ISB ? WS_IMB : WS_IMA) + p * IMG_BYTES + w1k;
            f32x4 dummy;
            gemm_img<0, 5, 0, 18, false, 2>(acc, xf, lb, dummy, [&](int s) {
                if (s < 3) xload(9 + 2 * s, 11 + 2 * s);
                if (s == 3) RETRY(layer * 8 + 1, xload(3, 9), xmax(3, 9));
                if (s >= 3 && s < 6) xload(12 + s, 13 + s);
                if (s == 9) RETRY(layer * 8 + 2, xload(9, 18), xmax(9, 18));
                if (s >= 9 && s < 14 && (s < 13 || wave < 4)) dma1k(cimg + (90 + 8 * (s - 9)) * 1024, l16, w1k + (90 + 8 * (s - 9)) * 1024);
                if (!ISB && s >= 9) wof[s] = ld128c(wsr, l16 + s * 1024, wob);
                if (ISB && it == 0 && s >= 14 && s < 17) arm_unit(wsr, l16, 63 + (s - 14) * 8 + wave, cu);
                if (!ISB && s >= 14 && (s < 17 || wave < 4)) arm_unit(wsr, l16, 87 + (s - 14) * 8 + wave, cu);
            });
        }
        f32x4 cs[5];
#pragma unroll
        for (int t = 0; t < 5; ++t) cs[t] = lds_rf4(csb + gq * 112 + 16 * t);
        acc[5] = Z4; acc[6] = Z4;
        STAMP(layer * 12 + 3);
        const f32x2 r1 = sw16(ssq, ssq); const float h1 = r1.x + r1.y; const f32x2 r2 = sw32(h1, h1);
        asm volatile("s_nop 7" : "+v"(acc[4]));
        mean = acc[4][2] * (1.f / DM);
        rstd = __builtin_amdgcn_rsqf(__builtin_fmaxf((r2.x + r2.y) * (1.f / DM) - mean * mean, 0.f) + 1e-5f);
        const int hx = gq >> 1, od = gq & 1;
        f32x2 y[9];
        {
            const f32x2 nm = sp2(-mean);
#pragma unroll
            for (int t = 0; t < 5; ++t) {
                y[2 * t] = fma2(nm, (f32x2){cs[t][0], cs[t][1]}, (f32x2){acc[t][0], acc[t][1]});
                if (t < 4) y[2 * t + 1] = fma2(nm, (f32x2){cs[t][2], cs[t][3]}, (f32x2){acc[t][2], acc[t][3]});
            }
        }
        const u32x4 vm = unit_pair(pk2(y[4].y * rstd, y[5].x * rstd), pk2(y[5].y * rstd, y[6].x * rstd), pk2(y[6].y * rstd, y[7].x * rstd), pk2(y[7].y * rstd, y[8].x * rstd));
        const float v8 = y[8].y * rstd;
        const unsigned vl = pkv(sw16(v8, v8));
        if (ISB) {
            const f32x2 r2 = sp2(rstd);
            const u32x4 km = unit_pair(pkv(y[0] * r2), pkv(y[1] * r2), pkv(y[2] * r2), pkv(y[3] * r2));
            const float k8 = y[4].x * rstd;
            const u32x4 lr = {ok1(pkv(sw16(k8, k8))), 0u, ok1(vl), 0x3F80u};
            const unsigned tb = (wave >> 1) * 5120 + (wave & 1) * 256, tc = (wave >> 1) * 5120 + (wave & 1) * 768 + 1024;
            const unsigned oc = (tk >> 3) * 384 + (tk & 7) * 16;
            const unsigned a0 = od * 2560 + hx * 512 + tk * 16, a1 = od * 2560 + hx * 128 + oc, a2 = hx * 2560 + 256 + (oc ^ 64);
            const unsigned hb = kbase + cu * 20480;
            st128c(wsr, a0, hb + tb, ok4(km)); st128c(wsr, a1, hb + tc, ok4(vm));
            lds_w128(L_STG + tb + a0, km); lds_w128(L_STG + tc + a1, vm);
            if (od == 0) { st128c(wsr, a2, hb + tc, lr); lds_w128(L_STG + tc + a2, lr); }
            asm volatile("s_waitcnt vmcnt(3)" ::: "memory");
            BARRIER();
            if (it == 0 && wave == 7 && lane == 0) set_flag(P.ws, FL_W4, cu, tag);
        } else {
            const float kz[9] = {y[0].x, y[0].y, y[1].x, y[1].y, y[2].x, y[2].y, y[3].x, y[3].y, y[4].x};
            u32x4 fa, fb; unsigned fe;
            dual_softmax(kz, rstd * LOG2E, gq < 2, fa, fb, fe);
            const unsigned pk = L_PK + wave * 1280 + tk * 80, vv = L_VV + wave * 768 + tk * 48;
            lds_w128(pk + gq * 16, fa); lds_w128(pk + gq * 16 + 10240, fb); lds_w32(pk + hx * 10240 + 64 + od * 4, fe);
            lds_w128(vv + od * 6144 + hx * 16, vm);
            lds_w64(vv + hx * 6144 + 32, (u32x2){vl, 0x3F80u});
            STAMP(53 + it);
            if (first && wave == 3) wait_all256(flag_at(P.ws, FL_W1, 0), lane, tag);
            DRAIN();
            BARRIER();
            if (wave == 7 && lane == 0) set_flag(P.ws, it ? FL_W5 : FL_W2, cu, tag);
            STAMP(55 + it);
            if (wave < 6) {
                const int h = wave >= 3, ft = wave - 3 * h;
                const unsigned trow = (unsigned)(lane >> 2), tcol = (unsigned)(lane & 3) * 8u;
                const unsigned pa = L_PK + h * 10240 + ft * 32 + trow * 80 + tcol, pb = L_VV + h * 6144 + trow * 48 + tcol;
                u32x2 ta[8], tb[8], td[8];
#pragma unroll
                for (int s = 0; s < 8; ++s) { ta[s] = lds_tr(pa + (16 * s) * 80); tb[s] = lds_tr(pb + (16 * s) * 48); td[s] = lds_tr(pb + (16 * s) * 48 + 32); }
                SB();
                f32x4 k0 = Z4, k1 = Z4;
#pragma unroll
                for (int s = 0; s < 4; ++s) {
                    const u32x4 af = {ta[2 * s].x, ta[2 * s].y, ta[2 * s + 1].x, ta[2 * s + 1].y};
                    k0 = mfma32((u32x4){tb[2 * s].x, tb[2 * s].y, tb[2 * s + 1].x, tb[2 * s + 1].y}, af, k0);
                    k1 = mfma32((u32x4){td[2 * s].x, td[2 * s].y, td[2 * s + 1].x, td[2 * s + 1].y}, af, k1);
                }
                const u32x2 s0 = ok2((u32x2){pk2(k0[0], k0[1]), pk2(k0[2], k0[3])}), s1 = ok2((u32x2){pk2(k1[0], k1[1]), pk2(k1[2], k1[3])});
                const unsigned kb = kbase + (p * 16 + g) * 3072 + h * 1536;
                if (ft < 2) {
                    st64c(wsr, lane * 8, kb + ft * 512, s0);
                    if (gq == 0) st64c(wsr, lane * 8, kb + 1024 + ft * 128, s1);
                } else if (tk < 4) {
                    st64c(wsr, gq * 32 + tk * 8, kb + 1280, s0);
                    if (gq == 0) st64c(wsr, tk * 8, kb + 1408, s1);
                }
            }
        }
        STAMP(layer * 12 + 0 + (layer == 0 ? 52 : 0));
        if (!(ISB ? CSPB : CSPA) && it == 0 && wave >= 6) colsum_tile(wave - 1, csb, lane);
        STAMP(layer * 12 + 4);
    }
    {
        const int lane = fresh(lane_in), gq = lane >> 4, tk = lane & 15;
        const unsigned l16 = lane * 16;
        const unsigned lb[3] = {l16 + ro0, l16 + ro1, l16 + ro2};
        f32x16 msk;
        const int dl = (lane & 31) - 4 * (lane >> 5);
        { f32x4 dummy; gemm_img<5, 2, 0, 18, false, 3>(acc, xf, lb, dummy, [&](int s) {
              if (!ISB && (s & 1) == 0) wof[s >> 1] = ld128c(wsr, l16 + (s >> 1) * 1024, wob);
              if (ISB && s < 16) msk[s] = ((s & 3) + 8 * (s >> 2) > dl) ? NEGBIG : 0.f;
              if ((s & 1) && first) wof[9 + (s >> 1)] = ld128c(wsr, l16 + (9 + (s >> 1)) * 1024, wob); }); }
        if ((!(ISB ? CSPB : CSPA) && it == 0) || (BAR2 && !ISB)) BARRIER();
        float q[9];
        {
            const unsigned ca = csb + gq * 112;
            const float c19 = f_of(lds_r32(ca + 76));
            const f32x4 c5 = lds_rf4(ca + 80), c6 = lds_rf4(ca + 96);
            q[0] = acc[4][3] - mean * c19;
#pragma unroll
            for (int i = 0; i < 4; ++i) { q[1 + i] = acc[5][i] - mean * c5[i]; q[5 + i] = acc[6][i] - mean * c6[i]; }
        }
        STAMP(layer * 12 + 5);
        if (ISB) {
            u32x4 hv[5];
            const int src = g - 2 + (wave >> 2);
            auto hload = [&]() {
                const unsigned hb = kbase + (src * 16 + p) * 20480 + (wave & 3) * 5120;
#pragma unroll
                for (int i = 0; i < 5; ++i) hv[i] = ld128c(wsr, l16 + i * 1024, hb);
            };
            if (src >= 0) {
                hload();
            } else {
#pragma unroll
                for (int i = 0; i < 5; ++i) hv[i] = (u32x4){0u, (i == 0 || (i == 2 && lane >= 32) || (i == 3 && lane < 32)) ? 0u : 0xF149u, 0u, 0u};
            }
            {
                const float qs = rstd * (0.23570226039551584f * LOG2E);
                const int hx = gq >> 1, od = gq & 1;
                const unsigned qb = L_QS + (wave >> 1) * 2304;
                lds_w128(qb + (wave & 1) * 256 + od * 1152 + hx * 512 + tk * 16, unit_pair(pk2(q[0] * qs, q[1] * qs), pk2(q[2] * qs, q[3] * qs), pk2(q[4] * qs, q[5] * qs), pk2(q[6] * qs, q[7] * qs)));
                const float q8 = q[8] * qs;
                lds_w32(qb + (wave & 1) * 64 + 1024 + hx * 1152 + tk * 4, pkv(sw16(q8, q8)));
            }
            const int h = wave & 1, jq = wave < 4 ? (wave >> 1) : 3 - ((wave - 4) >> 1);
            const int r = lane & 31, hh = lane >> 5;
            const unsigned kb = L_KT + h * 2560 + jq * 5120;
            const unsigned ka = kb + l16, la = kb + 1280 + (r >> 3) * 384 + ((r & 7) ^ 4) * 16;
            const unsigned k8 = 4 * hh + ((lane >> 2) & 3);
            const unsigned va = kb + 1024 + ((lane & 16) ? 256 + (k8 ^ 4) * 16 + 8 : (lane & 2) * 64 + k8 * 16 + (lane & 1) * 8);
            BARRIER();
            const u32x4 q0 = lds_r128(L_QS + (jq * 2 + h) * 1152 + l16);
            const unsigned q1w = lds_r32(L_QS + (jq * 2 + h) * 1152 + 1024 + r * 4);
            u32x4 km = lds_r128(ka + 8 * 5120), kl = lds_r128(la + 8 * 5120);
            u32x2 vf[2][4];
            att_vload(vf[0], va + 8 * 5120);
            STAMP(layer * 12 + 1);
            const u32x4 q1 = {hh ? 0u : q1w, hh ? 0u : 0x3F80u, 0u, 0u};
            f32x16 sc[2], negm, o16 = splat16(0.f);
            sc[0] = att_scores(km, kl, q0, q1, msk);
            {
                const float m = max16(sc[0], NEGBIG);
                const float nm = -vmax(sw32(m, m));
                negm = splat16(nm);
                sc[0] += negm;
                msk = (splat16(NEGBIG) - msk) + negm;
            }
#pragma unroll
            for (int i = 8; i >= 0; --i) {
                if (i >= 5 && i == 8 - jq) {
                    RETRY(layer * 8 + 4, hload(), umax4(umax4(umax4(umax4(umax4(0u, hv[0]), hv[1]), hv[2]), hv[3]), hv[4]));
#pragma unroll
                    for (int j = 0; j < 5; ++j) lds_w128(L_KT + wave * 5120 + j * 1024 + l16, hv[j]);
                    BARRIER();
                    STAMP(layer * 12 + 6);
                }
                SB();
                if (i > 0) { km = lds_r128(ka + (i - 1) * 5120); kl = lds_r128(la + (i - 1) * 5120); att_vload(vf[(i - 1) & 1], va + (i - 1) * 5120); }
                wof[16 - 2 * i] = ld128c(wsr, l16 + (16 - 2 * i) * 1024, wob); wof[17 - 2 * i] = ld128c(wsr, l16 + (17 - 2 * i) * 1024, wob);
                SB();
                const u32x4 p0 = att_p(sc[i & 1], 0);
                SB();
                if (i > 0) sc[(i - 1) & 1] = att_scores(km, kl, q0, q1, i == 1 ? msk : negm);
                o16 = mfmaL((u32x4){vf[i & 1][0].x, vf[i & 1][0].y, vf[i & 1][1].x, vf[i & 1][1].y}, p0, o16);
                SB();
                const u32x4 p1 = att_p(sc[i & 1], 8);
                if (i < 7 && !last) {
                    if (i > 0 || wave < 6) dma1k(nimg + 8 * (6 - i) * 1024, l16, w1k + 8 * (6 - i) * 1024);
                }
                SB();
                o16 = mfmaL((u32x4){vf[i & 1][2].x, vf[i & 1][2].y, vf[i & 1][3].x, vf[i & 1][3].y}, p1, o16);
            }
            asm volatile("s_nop 7\n\ts_nop 7" : "+v"(o16));
            {
                float t = 0.f;
#pragma unroll
                for (int v = 0; v < 11; ++v) t += __builtin_fabsf(o16[v]);
                if (__any(!(t < 3.0e38f))) {
                    float m = NEGBIG;
#pragma unroll 1
                    for (int pass = 0; pass < 2; ++pass) {
                        o16 = splat16(0.f);
#pragma unroll 1
                        for (int i = 0; i <= 8; ++i) {
                            u32x2 v4[4]; att_vload(v4, va + i * 5120);
                            f32x16 s = att_scores(lds_r128(ka + i * 5120), lds_r128(la + i * 5120), q0, q1, splat16(pass ? -m : 0.f));
                            const int lim = i == 8 ? dl : 99, lim2 = i == 0 ? dl : -99;
#pragma unroll
                            for (int v = 0; v < 16; ++v) { const int cv = (v & 3) + 8 * (v >> 2); if (cv > lim || cv <= lim2) s[v] = NEGBIG; }
                            if (pass) att_accum(s, v4, o16); else m = max16(s, m);
                        }
                        if (!pass) m = vmax(sw32(m, m));
                    }
                    asm volatile("s_nop 7\n\ts_nop 7" : "+v"(o16));
                }
            }
            STAMP(layer * 12 + 7);
            const float inv = rcp(o16[10]);
            const unsigned ob = obase + (g * 8 + 2 * jq) * TKT;
            const unsigned vo = (r >> 4) * TKT + (r & 15) * 16;
            st128c(wsr, vo + hh * 256, ob + p * 1024 + h * 512, ok4((u32x4){pk2(o16[0] * inv, o16[1] * inv), pk2(o16[2] * inv, o16[3] * inv), pk2(o16[4] * inv, o16[5] * inv), pk2(o16[6] * inv, o16[7] * inv)}));
            if (hh == 0) st32c(wsr, (r >> 4) * TKT + (r & 15) * 4, ob + 16384 + p * 128 + h * 64, ok1(pk2(o16[8] * inv, o16[9] * inv)));
        } else {
            u32x4 pv[8];
            auto pload = [&]() {
                const unsigned kb = kbase + p * 16 * 3072 + wave * 256;
#pragma unroll
                for (int i = 0; i < 8; ++i) if (i < 4 || wave < 4) pv[i] = ld128c(wsr, tk * 16 + gq * 3072, kb + (i & 3) * (4 * 3072) + (i >> 2) * 2048);
            };
#pragma unroll
            for (int i = 4; i < 8; ++i) pv[i] = (u32x4){0u, 0u, 0u, 0u};
            if (PSLEEP) __builtin_amdgcn_s_sleep(PSLEEP);
            pload();
            u32x4 pk = {tag, tag, tag, tag};
            if (wave == 7) pk = peek256(flag_at(P.ws, it ? FL_W5 : FL_W2, 0), lane);
            u32x4 fq[2]; unsigned fe;
            dual_softmax(q, rstd * LOG2E, gq < 2, fq[0], fq[1], fe);
            const unsigned q4 = (lane >> 2) & 3, c4 = lane & 3, fm = (gq & 1) * 8 + q4, fx = (gq & 1) * 2 + (q4 & 1);
            const unsigned ra = L_KVS + (gq >> 1) * 512 + c4 * 128 + fm * 8, rb = L_KVS + 1024 + (gq >> 1) * 128 + fm * 8;
            const unsigned rc = L_KVS + 1280 + c4 * 32 + fx * 8, rd = L_KVS + 1408 + fx * 8;
            {
                STAMP(layer * 12 + 6);
                const unsigned k0 = (wave == 5 && tk >= 10) ? 0u : ~0u, k1 = (wave == 3 && tk >= 10) ? 0u : ~0u;
                RETRY(layer * 8 + 4, pload(), __builtin_elementwise_max(k0 & umax4(umax4(umax4(umax4(0u, pv[0]), pv[1]), pv[2]), pv[3]), k1 & umax4(umax4(umax4(umax4(0u, pv[4]), pv[5]), pv[6]), pv[7])));
                const u32x4 sel = onehot8(tk < 8 ? tk : -2);
                f32x4 s0 = Z4, s1 = Z4;
#pragma unroll
                for (int i = 0; i < 4; ++i) { s0 = mfma32(sel, pv[i], s0); s1 = mfma32(sel, pv[4 + i], s1); }
                const u32x2 w0 = {pk2(s0[0], s0[1]), pk2(s0[2], s0[3])}, w1v = {pk2(s1[0], s1[1]), pk2(s1[2], s1[3])};
                if (gq < 2) {
                    lds_w64(L_KVS + wave * 256 + tk * 16 + gq * 8, w0);
                    if (wave < 4) lds_w64(L_KVS + 2048 + wave * 256 + tk * 16 + gq * 8, w1v);
                }
            }
            judge256(pk, flag_at(P.ws, it ? FL_W5 : FL_W2, 0), lane, tag);
            if (CSPB && first && wave == 6) colsum_get(wsr, 16 + p, L_CS + 448, lane);
            STAMP(57 + it);
            BARRIER();
            f32x4 oa[2][2];
            {
                u32x2 am[2][4], ae[2][2];
#pragma unroll
                for (int h = 0; h < 2; ++h) {
                    am[h][0] = lds_tr(ra + h * 1536); am[h][1] = lds_tr(ra + h * 1536 + 32); am[h][2] = lds_tr(rb + h * 1536); am[h][3] = lds_tr(rb + h * 1536 + 32);
                    ae[h][0] = lds_tr(rc + h * 1536); ae[h][1] = lds_tr(rd + h * 1536);
                }
                SB();
#pragma unroll
                for (int h = 0; h < 2; ++h) {
                    oa[h][0] = mfma32((u32x4){am[h][0].x, am[h][0].y, am[h][1].x, am[h][1].y}, fq[h], Z4);
                    oa[h][1] = mfma32((u32x4){am[h][2].x, am[h][2].y, am[h][3].x, am[h][3].y}, fq[h], Z4);
                }
#pragma unroll
                for (int h = 0; h < 2; ++h) {
                    const u32x4 fh = w1((gq >> 1) == h ? fe : 0u);
                    oa[h][1] = mfma32(w1(ae[h][1].x), fh, oa[h][1]); oa[h][0] = mfma32(w1(ae[h][0].x), fh, oa[h][0]);
                }
            }
            STAMP(layer * 12 + 7);
            const float ia = rcp(oa[0][1][2] + 1e-6f), ib = rcp(oa[1][1][2] + 1e-6f);
            const unsigned ob = obase + (g * 8 +
        u32x4 wox[18];
        if (wave >= 6) {
            const unsigned wb = WS_WOI + ((ISB ? 36 : 0) + 9 * cg + 8) * TKT;
#pragma unroll
            for (int s = 0; s < 18; ++s) wox[s] = ld128c(wsr, l16 + s * 1024, wb);
            if (ISB && it == 0 && wave == 7) wait_all256(flag_at(P.ws, FL_W4, 0), lane, tag);
        } else {
            const unsigned ob = obase + (g * 8 + 2 * tp) * TKT + w1k;
            u32x4 ov[6];
            auto oload = [&]() {
#pragma unroll
                for (int i = 0; i < 6; ++i) ov[i] = ld128c(wsr, l16 + i * 6144, ob);
            };
            if (OSLEEP) __builtin_amdgcn_s_sleep(OSLEEP);
            oload();
            RETRY(layer * 8 + 5, oload(), umax4(umax4(umax4(umax4(umax4(umax4(0u, ov[0]), ov[1]), ov[2]), ov[3]), ov[4]), ov[5]));
            STAMP(layer * 12 + 8);
#pragma unroll
            for (int i = 0; i < 6; ++i) {
                if ((i == 2 || i == 5) && wave >= 4) {
                    const unsigned d = L_OST + w1k + i * 6144 + (lane >> 4) * 256 + (lane & 3) * 64 + (lane & 12);
                    lds_w32(d, ov[i].x); lds_w32(d + 16, ov[i].y); lds_w32(d + 32, ov[i].z); lds_w32(d + 48, ov[i].w);
                } else lds_w128(L_OST + w1k + i * 6144 + l16, ov[i]);
            }
        }
        if (first && blockIdx.x == 0 && wave == 0 && lane == 0) __hip_atomic_store((GAS unsigned*)P.ws, tag, RLX_AGENT);
        BARRIER();
        STAMP(layer * 12 + 9);
        {
            f32x4 c0 = res[0], c1 = res[1], c2 = res[2];
            const unsigned lo = L_OST + l16;
            if (wave >= 6) {
                u32x4 ox[18];
#pragma unroll
                for (int s = 0; s < 18; ++s) ox[s] = lds_r128(lo + (wave & 1) * TKT + s * 1024);
                SB();
#pragma unroll
                for (int s = 0; s < 18; ++s) c2 = mfma32(wox[s], ox[s], c2);
            }
            SB();
            u32x4 of[4][2];
#pragma unroll
            for (int s = 0; s < 3; ++s) { of[s][0] = lds_r128(lo + s * 1024); of[s][1] = lds_r128(lo + TKT + s * 1024); }
#pragma unroll
            for (int s = 0; s < 18; ++s) {
                SB();
                c0 = mfma32(wof[s], of[s & 3][0], c0);
                SB();
                if (s + 3 < 18) of[(s + 3) & 3][0] = lds_r128(lo + (s + 3) * 1024);
                SB();
                c1 = mfma32(wof[s], of[s & 3][1], c1);
                SB();
                if (s + 3 < 18) of[(s + 3) & 3][1] = lds_r128(lo + TKT + (s + 3) * 1024);
                if (ISB) { if ((s & 1) == 0 && s < 10 && !last && (s < 8 || wave < 4)) dma1k(nimg + (54 + 4 * s) * 1024, l16, w1k + (54 + 4 * s) * 1024); }
                else if (s < 12 && (s < 11 || wave < 2)) dma1k(nimg + 8 * s * 1024, l16, w1k + 8 * s * 1024);
            }
            res[0] = c0; res[1] = c1; res[2] = c2;
            STAMP(layer * 12 + 10);
            const u32x4 v = unit_pair(pk2(c0[0], c0[1]), pk2(c0[2], c0[3]), pk2(c1[0], c1[1]), pk2(c1[2], c1[3]));
            const u32x2 v2 = {pk2(c2[0], c2[1]), pk2(c2[2], c2[3])};
            const int ct = 9 * cg + wave, cx = 9 * cg + 8;
            const int tx = 2 * tp + (wave & 1);
            if (!last) {
                const unsigned xb = obase + ACT_BYTES + g * 8 * TKT, sb = obase + 2 * ACT_BYTES + g * 8 * 2304;
                const unsigned vo = (gq >> 1) * 256 + tk * 16;
                st128c(wsr, (gq & 1) * TKT + vo, xb + 2 * tp * TKT + ct * 512, ok4(v));
                const f32x2 a0 = {c0[0], c0[1]}, a1 = {c0[2], c0[3]}, b0 = {c1[0], c1[1]}, b1 = {c1[2], c1[3]};
                const f32x2 qa = fma2(a1, a1, a0 * a0), qb = fma2(b1, b1, b0 * b0);
                const f32x2 u = sw16(qa.x + qa.y, qb.x + qb.y); const float u1 = u.x + u.y; const f32x2 u2 = sw32(u1, u1);
                if (gq < 2) st32c(wsr, gq * 2304 + tk * 4, sb + 2 * tp * 2304 + ct * 64, ok1(u_of(u2.x + u2.y)));
                if (wave >= 6) {
                    st64c(wsr, vo + (gq & 1) * 8, xb + tx * TKT + cx * 512, ok2(v2));
                    const f32x2 d0 = {c2[0], c2[1]}, d1 = {c2[2], c2[3]}, qd = fma2(d1, d1, d0 * d0);
                    const float e0 = qd.x + qd.y; const f32x2 e1 = sw16(e0, e0); const float e2 = e1.x + e1.y; const f32x2 e3 = sw32(e2, e2);
                    if (gq == 0) st32c(wsr, tk * 4, sb + tx * 2304 + cx * 64, ok1(u_of(e3.x + e3.y)));
                }

            } else {
                const int tt = 2 * tp + (gq & 1);
                *(u32x4*)(P.out + (size_t)(g * 128 + tt * 16 + tk) * DM + 16 * ct + 8 * (gq >> 1)) = v;
                if (wave >= 6) *(u32x2*)(P.out + (size_t)(g * 128 + tx * 16 + tk) * DM + 16 * cx + 4 * gq) = v2;
            }
        }
if (!last) {
                const unsigned xb = obase + ACT_BYTES + g * 8 * TKT, sb = obase + 2 * ACT_BYTES + g * 8 * 2304;
                const unsigned vo = (gq >> 1) * 256 + tk * 16;
                st128c(wsr, (gq & 1) * TKT + vo, xb + 2 * tp * TKT + ct * 512, ok4(v));
                const f32x2 a0 = {c0[0], c0[1]}, a1 = {c0[2], c0[3]}, b0 = {c1[0], c1[1]}, b1 = {c1[2], c1[3]};
                const f32x2 qa = fma2(a1, a1, a0 * a0), qb = fma2(b1, b1, b0 * b0);
                const f32x2 u = sw16(qa.x + qa.y, qb.x + qb.y); const float u1 = u.x + u.y; const f32x2 u2 = sw32(u1, u1);
                if (gq < 2) st32c(wsr, gq * 2304 + tk * 4, sb + 2 * tp * 2304 + ct * 64, ok1(u_of(u2.x + u2.y)));
                if (wave < 2) {
                    st64c(wsr, vo + (gq & 1) * 8, xb + tx * TKT + cx * 512, ok2(v2));
                    const f32x2 d0 = {c2[0], c2[1]}, d1 = {c2[2], c2[3]}, qd = fma2(d1, d1, d0 * d0);
                    const float e0 = qd.x + qd.y; const f32x2 e1 = sw16(e0, e0); const float e2 = e1.x + e1.y; const f32x2 e3 = sw32(e2, e2);
                    if (gq == 0) st32c(wsr, tk * 4, sb + tx * 2304 + cx * 64, ok1(u_of(e3.x + e3.y)));
                }

            } else {
                const int tt = 2 * tp + (gq & 1);
                *(u32x4*)(P.out + (size_t)(g * 128 + tt * 16 + tk) * DM + 16 * ct + 8 * (gq >> 1)) = v;
                if (wave < 2) *(u32x2*)(P.out + (size_t)(g * 128 + tx * 16 + tk) * DM + 16 * cx + 4 * gq) = v2;
            }
        }
        STAMP(layer * 12 + 11);
    }
    if (ISB) {
#pragma unroll
        for (int s = 0; s < 18; ++s) asm volatile("" : "=v"(xf[s]));
#pragma unroll
        for (int t = 0; t < 5; ++t) asm volatile("" : "=v"(acc[t]));
        asm volatile("" : "=v"(ssq));
    }
}

__global__ void __launch_bounds__(512, 2) fused_kernel(Params P) {
    const int tid = threadIdx.x, lane = tid & 63, wave = __builtin_amdgcn_readfirstlane(tid >> 6);
    const int b = blockIdx.x, g = ((b >> 1) & 3) * 4 + (b >> 6), p = (b & 1) * 8 + ((b >> 3) & 7);
    const rsrc_t wsr = mk_rsrc(P.ws, WS_END);
    const unsigned e_begin = (unsigned)__builtin_amdgcn_s_memrealtime();
    STAMP(0);
    if (lane < 3) {
        unsigned long long pc; unsigned keep;
        asm volatile("s_getpc_b64 %0" : "=s"(pc));
        const unsigned o = (((b >> 3) * 8 + wave) * 3 + lane) * 64;
        asm volatile("s_mov_b32 %0, m0\n\ts_mov_b32 m0, %3\n\ts_nop 0\n\tglobal_load_lds_dword %1, %2\n\ts_mov_b32 m0, %0" : "=&s"(keep) : "v"(o < CODE_BYTES ? o : 0u), "s"(pc), "s"(L_CS + 64 + wave * 16) : "memory");
    }
    u32x4 xf[18];
    f32x4 res[3];
    f32x4 acc[7], sq;
    float ssq;
    Ctx C; C.wave = wave; C.g = g; C.p = p;
    {
        const int gq = lane >> 4, tk = lane & 15;
        const f32x4 Z4 = {0.f, 0.f, 0.f, 0.f};
        const unsigned epv = __hip_atomic_load((const GAS unsigned*)P.ws, RLX_AGENT);
        const u16* xs = P.x + (size_t)(g * 128) * DM;
        u32x4 raw[8];
        unsigned ph0, ph1;
        const unsigned c0 = (unsigned)(gq == 1 ? 8 : gq == 2 ? 10 : 0);
        const unsigned wv = ((unsigned)tk * NQKV + c0) * 2u, wo = p * 36 + wave * 576;
        if (wave >= 4) {
            const unsigned xo = ((unsigned)(lane >> 3) * DM + 8u * ((lane & 7) ^ (lane >> 3))) * 2u;
            __builtin_amdgcn_s_sleep(DSLEEP);
#pragma unroll 1
            for (int j = 0; j < 4; ++j) {
                const unsigned oc = (wave - 4 + 4 * j + 2 * p) & 15;
                const u16* src = xs + oc * 8 * DM;
                unsigned dst = (oc >> 1) * TKT + (oc & 1) * 1024, keep, vo = xo;
                asm volatile("s_mov_b32 %0, m0\n\t"
                             ".rept 9\n\ts_mov_b32 m0, %1\n\ts_add_u32 %1, %1, 0x800\n\tglobal_load_lds_dwordx4 %2, %3\n\tv_add_u32 %2, 0x80, %2\n\t.endr\n\t"
                             "s_mov_b32 m0, %0" : "=&s"(keep), "+s"(dst), "+v"(vo) : "s"(src) : "memory", "scc");
            }
            STAMP(49);
        }
        SB();
        u32x4 im, ix, lraw[8];
        unsigned wdst = 0, km[2], kx;
        auto unit_tile = [&](const u32x4* raw, const u32x4 (&bm)[2], const u32x4 (&bl)[2], unsigned pb) {
            u32x2 lf[4];
#pragma unroll
            for (int i = 0; i < 4; ++i) {
                const f32x4 cm = mfma32(raw[i], bm[i >> 1], Z4), cl = mfma32(raw[i], bl[i >> 1], Z4);
                lds_w64(km[i >> 1] + pb + (i & 1) * 512, (u32x2){pk2(cm[0], cm[1]), pk2(cm[2], cm[3])});
                lf[i] = (u32x2){pk2(cl[0], cl[1]), pk2(cl[2], cl[3])};
            }
            if (tk < 2) {
#pragma unroll
                for (int i = 0; i < 4; ++i) lds_w64(kx + pb + (i >> 1) * 128 + (i & 1) * 512, lf[i]);
            }
        };
        auto meet = [&](unsigned word, unsigned val) {
            asm volatile("s_waitcnt lgkmcnt(0)" ::: "memory");
            if (lane == 0) lds_w32(word + wave * 4, val);
#pragma unroll 1
            for (unsigned sp = 0; sp < SPIN_CAP; ++sp) { asm volatile("" ::: "memory"); if (__all(lds_r32(word + (lane < 3 ? lane : 0) * 4) == val)) break; }
        };
        if (wave < 3) {
#pragma unroll
            for (int i = 0; i < 4; ++i) ld128_raw(raw[i], P.wqkvA + wo + g * (32 * NQKV) + (i & 1) * (16 * NQKV) + (i >> 1) * 18, wv);
#pragma unroll
            for (int i = 0; i < 4; ++i) ld128_raw(raw[4 + i], P.wqkvA + wo + (16 + (g & 1)) * (32 * NQKV) + (i & 1) * (16 * NQKV) + (i >> 1) * 18, g < 2 ? wv : 0u);
            asm volatile("global_load_dword %0, %2, %4\n\tglobal_load_dword %1, %3, %4" : "=&v"(ph0), "=&v"(ph1) : "v"(tid * 4u), "v"((tid < 132 ? tid + 192 : 0) * 4u), "s"(P.wphi + p * 648) : "memory");
            SB();
            { const int l2 = fresh(lane); im = onehot8(((l2 >> 3) & 1) == (l2 >> 4) ? (l2 & 7) : -2); ix = onehot8((l2 >> 4) == 2 ? 6 + (l2 & 1) : -2); }
            constexpr unsigned L_PHI = L_PST + 7168;
            unsigned pa[2];
#pragma unroll
            for (int i = 0; i < 2; ++i) {
                const int z = tid + 192 * i, row = z / 7, j = z - 7 * row;
                if (z < 252) lds_w32(L_PHI + row * 64 + (j < 3 ? 32 + 4 * j : 36 + 4 * j), 0u);
                const int e = 2 * z, hh = e >= 324, r = e - 324 * hh, d = r / 18, f = r - 18 * d;
                pa[i] = L_PHI + (unsigned)(hh * 18 + f) * 64u + (unsigned)(d < 16 ? d : d + 6) * 2u;
            }
            const unsigned sb = wave == 0 ? 19 : wave == 2 ? 9 : 0, sl = sb + 8;
            const unsigned ko = L_PST + (gq >> 1) * 256 + (gq & 1) * 8;
#pragma unroll
            for (int h = 0; h < 2; ++h) { const unsigned sm = sb + 4 * h + (tk & 3); km[h] = ko + (tk >> 2) * 64 + (sm >> 2) * 1024 + (sm & 3) * 16; }
            kx = ko + (tk & 1) * 64 + (sl >> 2) * 1024 + (sl & 3) * 16;
            const unsigned onea = L_PST + 4096 + (tk >> 2) * 256 + (4 * (tk & 3) + 2) * 16;
            const u32x4 one4 = {0x3F803F80u, 0x3F803F80u, 0x3F803F80u, 0x3F803F80u};
            if (wave == 2 && lane < 16) lds_w128(onea, one4);
            asm volatile("s_waitcnt vmcnt(0)" : "+v"(ph0), "+v"(ph1), "+v"(raw[0]), "+v"(raw[1]), "+v"(raw[2]), "+v"(raw[3]), "+v"(raw[4]), "+v"(raw[5]), "+v"(raw[6]), "+v"(raw[7]) :: "memory");
            lds_w16(pa[0], ph0); lds_w16(pa[0] + 64, ph0 >> 16);
            if (tid < 132) { lds_w16(pa[1], ph1); lds_w16(pa[1] + 64, ph1 >> 16); }
            const unsigned tag = __builtin_amdgcn_readfirstlane(epv) * 0x9E3779B1u + 0x7F4A7C15u;
            meet(L_CS + 32, tag);
            u32x4 bm[2] = {im, im}, bl[2] = {ix, ix};
            if (wave < 2) {
#pragma unroll
                for (int h = 0; h < 2; ++h) { bm[h] = lds_r128(L_PHI + (h * 18 + tk) * 64 + gq * 16); bl[h] = lds_r128(L_PHI + (h * 18 + 16 + (tk & 1)) * 64 + gq * 16); }
            }
            STAMP(48);
            unit_tile(raw, bm, bl, 0);
            meet(L_CS, tag);
            if (wave == 2 && lane < 16) lds_w128(onea + 7168, one4);
            if (g < 2) { unit_tile(raw + 4, bm, bl, 7168); meet(L_CS + 48, tag); }
            const unsigned o = WS_IMA + p * IMG_BYTES + wave * TKT, la = L_PST + wave * 1024 + lane * 16;
#pragma unroll
            for (int u = 0; u < 2; ++u) {
                if (u == 0 || g < 2) {
                    const unsigned oo = o + (u ? 16 + g : g) * 1024;
                    const unsigned ks = p * 18 + (u ? 16 + g : g);
                    put_frag<CSPA>(wsr, oo, ks, wave, lds_r128(la + u * 7168), lane); put_frag<CSPA>(wsr, oo + 3 * TKT, ks, wave + 3, lds_r128(la + u * 7168 + 3072), lane);
                    if (wave == 0) put_frag<CSPA>(wsr, oo + 6 * TKT, ks, 6, lds_r128(la + u * 7168 + 6144), lane);
                }
            }
            DRAIN();
            if (lane == 0) set_flag(P.ws, FL_IA, p * 64 + g * 3 + wave, tag);
            STAMP(63);
            const rsrc_t rw = mk_rsrc(P.wqkvB, DM * NQKV * 2);
#pragma unroll
            for (int i = 0; i < 8; ++i) lraw[i] = __builtin_amdgcn_raw_buffer_load_b128(rw, (i < 4 || (g >> 1) == 1) ? wv : 0x7fffffffu,
                                                      (wo + (i < 4 ? g : 16 + (g & 1)) * (32 * NQKV) + (i & 1) * (16 * NQKV) + ((i >> 1) & 1) * 18) * 2, 0);
        } else {
            { const int l2 = fresh(lane); im = onehot8(((l2 >> 3) & 1) == (l2 >> 4) ? (l2 & 7) : -2); ix = onehot8(((l2 >> 3) & 1) + 2 == (l2 >> 4) ? (l2 & 7) : -2); }
#pragma unroll
            for (int i = 2; i < 8; ++i) lraw[i] = im;
            const int u = b + 256 * (wave - 3), m = u >= 324, r = u - 324 * m, cp = r / 18, s = r - 18 * cp;
            const unsigned w0 = (unsigned)(wo_krow(m, s, tk >> 2, tk & 3) * DM + 8 * gq) * 2u, w1o = (unsigned)(wo_krow(m, s, tk >> 2, 4 + (tk & 3)) * DM + 8 * gq) * 2u;
            const rsrc_t rw = mk_rsrc(m ? P.woB : P.woA, u < 648 ? DM * DM * 2 : 0);
            asm volatile("" :: "v"(epv));
            lraw[0] = __builtin_amdgcn_raw_buffer_load_b128(rw, w0, 64 * cp, 0); lraw[1] = __builtin_amdgcn_raw_buffer_load_b128(rw, w1o, 64 * cp, 0);
            wdst = u < 648 ? WS_WOI + (unsigned)((m * 36 + 2 * cp) * 18 + s) * 1024u : 0u;
            asm volatile("s_waitcnt vmcnt(2)" ::: "memory");
            STAMP(48);
        }
        C.tag = __builtin_amdgcn_readfirstlane(epv) * 0x9E3779B1u + 0x7F4A7C15u;
        BARRIER();
        STAMP(50);
        {
            const unsigned xa = (tk >> 3) * 1024 + (tk & 7) * 128;
#pragma unroll
            for (int s = 0; s < 18; ++s) xf[s] = lds_r128(wave * TKT + (s >> 1) * 2048 + xa + (((4 * (s & 1) + gq) ^ (tk & 7)) * 16));
            const int tp = (p >> 1) & 3, cg = (p >> 3) * 2 + (p & 1);
#pragma unroll
            for (int i = 0; i < 3; ++i) {
                const int tt = 2 * tp + (i < 2 ? i : (wave & 1)), ct = 9 * cg + (i < 2 ? wave : 8);
                const u32x2 r = *(const LAS u32x2*)((LAS unsigned char*)smem + tt * TKT + (ct >> 2) * 2048 + xa + (((2 * (ct & 3) + (gq >> 1)) ^ (tk & 7)) * 16) + (gq & 1) * 8);
                res[i] = (f32x4){f_of(r.x << 16), f_of(r.x & 0xffff0000u), f_of(r.y << 16), f_of(r.y & 0xffff0000u)};
            }
        }
        if (wave == 3) wait_flags(flag_at(P.ws, FL_IA, p * 64), (lane < 48 ? lane : 0) * 128, C.tag);
        BARRIER();
        STAMP(51);
        const unsigned char* img = P.ws + WS_IMA + p * IMG_BYTES;
        unsigned fo[4];
#pragma unroll
        for (int i = 0; i < 4; ++i) { const int j = i * 8 + wave, jt = j / 6; fo[i] = (j < 30 ? jt * 12 + j : 60 + j) * 1024; }
        const unsigned st3 = wave < 6 ? 6144 : 2048;
        auto piece = [&](int c, int i) {
            unsigned f = c < 3 ? fo[i] + c * (i < 3 ? 6144 : st3) : (96 + i * 8 + wave) * 1024;
            if (c == 3 && i == 3) f = f < 125 * 1024 ? f : 125 * 1024;
            dma1k(img + f, lane * 16, f);
        };
#pragma unroll
        for (int i = 0; i < 8; ++i) asm volatile("" :: "v"(lraw[i]));
#pragma unroll
        for (int i = 0; i < 8; ++i) piece(i >> 2, i & 3);
        STAMP(1);
        if (wave < 3) {
            const u32x4 bm[2] = {im, im}, bl[2] = {ix, ix};
            unit_tile(lraw, bm, bl, 0);
            if ((g >> 1) == 1) unit_tile(lraw + 4, bm, bl, 7168);
        } else {
            wdst = __builtin_amdgcn_readfirstlane(wdst);
            if (wdst) {
                const f32x4 a0 = mfma32(lraw[0], im, Z4), a1 = mfma32(lraw[1], im, Z4), c0 = mfma32(lraw[0], ix, Z4), c1 = mfma32(lraw[1], ix, Z4);
                st128c(wsr, lane * 16, wdst, (u32x4){pk2(a0[0], a0[1]), pk2(a0[2], a0[3]), pk2(a1[0], a1[1]), pk2(a1[2], a1[3])});
                st128c(wsr, lane * 16, wdst + TKT, (u32x4){pk2(c0[0], c0[1]), pk2(c0[2], c0[3]), pk2(c1[0], c1[1]), pk2(c1[2], c1[3])});
            }
            if (wave >= 5) arm_unit(wsr, lane * 16, wave - 5, b);
            if (CSPA && wave == 7) colsum_get(wsr, p, L_CS, lane);
        }
#pragma unroll
        for (int t = 0; t < 7; ++t) acc[t] = Z4;
        sq = Z4;
        const unsigned lb0[3] = {(unsigned)lane * 16, (unsigned)lane * 16 + 6144, (unsigned)lane * 16 + 12288};
        if (wave < 3) asm volatile("s_waitcnt vmcnt(4)" ::: "memory"); else if (wave == 5) asm volatile("s_waitcnt vmcnt(5)" ::: "memory");
        else if (wave < 5) asm volatile("s_waitcnt vmcnt(6)" ::: "memory"); else asm volatile("s_waitcnt vmcnt(5)" ::: "memory");
        BARRIER();
        STAMP(2);
        gemm_img<0, 5, 0, 6, true, 2>(acc, xf, lb0, sq, [&](int s) {
            if (s < 2 && wave < 7 && (s == 0 || (g >> 1) == 1)) put_frag<CSPB>(wsr, WS_IMB + p * IMG_BYTES + wave * TKT + (s ? 14 + g : g) * 1024, (16 + p) * 18 + (s ? 14 + g : g), wave, lds_r128(L_PST + s * 7168 + wave * 1024 + lane * 16), lane);
            if (s >= 2) piece(2, s - 2); });
        STAMP(25);
        asm volatile("s_waitcnt vmcnt(4)" ::: "memory"); BARRIER();
        if (tid == 0) set_flag(P.ws, FL_W1, g * 16 + p, C.tag);
        STAMP(26);
        gemm_img<0, 5, 6, 12, true, 2>(acc, xf, lb0, sq, [&](int s) { if (s < 10) piece(3, s - 6); if (s >= 10) arm_unit(wsr, lane * 16, 3 + (s - 10) * 8 + wave, b); });
        STAMP(27);
        asm volatile("s_waitcnt vmcnt(6)" ::: "memory"); BARRIER();
        STAMP(28);
        gemm_img<0, 5, 12, 18, true, 2>(acc, xf, lb0, sq, [&](int s) { if (s < 17 || wave < 4) arm_unit(wsr, lane * 16, 19 + (s - 12) * 8 + wave, b); });
        STAMP(29);
        asm volatile("s_nop 7" : "+v"(sq));
        const float d0 = (tk & 1) ? sq[1] : sq[0], d1 = (tk & 1) ? sq[3] : sq[2], dg = (tk & 2) ? d1 : d0;
        ssq = gq == (tk >> 2) ? dg : 0.f;
        if (!CSPA) { if (wave < 4) colsum_tile(wave, L_CS, lane); if (wave == 0) colsum_tile(4, L_CS, lane); BARRIER(); }
    }
#pragma unroll 1
    for (int it = 0; it < NIT; ++it) {
        layer_fn<false>(P, wsr, lane, C, it, xf, res, acc, ssq);
        layer_fn<true>(P, wsr, lane, C, it, xf, res, acc, ssq);
    }
    DRAIN(); BARRIER();
    { const unsigned d = __builtin_amdgcn_readfirstlane(lds_r32(L_CS + 1024)) - e_begin, t0 = (unsigned)__builtin_amdgcn_s_memrealtime();
      while ((unsigned)__builtin_amdgcn_s_memrealtime() - t0 < d && d < 20000u) __builtin_amdgcn_s_sleep(1); }
}

extern "C" void kernel_launch(void* const* d_in, const int* in_sizes, int n_in, void* d_out, int out_size, void* d_ws, size_t ws_size, hipStream_t stream) {
    Params P;
    P.x = (const u16*)d_in[0]; P.wqkvA = (const u16*)d_in[1]; P.wphi = (const u16*)d_in[2]; P.woA = (const u16*)d_in[3];
    P.wqkvB = (const u16*)d_in[5]; P.woB = (const u16*)d_in[6];
    P.out = (u16*)d_out; P.ws = (unsigned char*)d_ws;
    (void)hipFuncSetAttribute((const void*)fused_kernel, hipFuncAttributeMaxDynamicSharedMemorySize, LDS_BYTES);
    hipLaunchKernelGGL(fused_kernel, dim3(256), dim3(512), LDS_BYTES, stream, P);
}
```

```cpp
#include <hip/hip_runtime.h>
#include <stdint.h>

typedef __attribute__((ext_vector_type(8))) __bf16 bf16x8;
typedef __attribute__((ext_vector_type(2))) __bf16 bf16x2;
typedef __attribute__((ext_vector_type(4))) short s16x4;
typedef __attribute__((ext_vector_type(4))) float f32x4;
typedef __attribute__((ext_vector_type(2))) float f32x2;
typedef __attribute__((ext_vector_type(16))) float f32x16;
typedef __attribute__((ext_vector_type(4))) unsigned u32x4;
typedef __attribute__((ext_vector_type(2))) unsigned u32x2;
typedef unsigned short u16;
#define LAS __attribute__((address_space(3)))
#define GAS __attribute__((address_space(1)))
#define RLX_AGENT __ATOMIC_RELAXED, __HIP_MEMORY_SCOPE_AGENT
#define DEVI __device__ __forceinline__

constexpr int SEQ = 2048, DM = 576, NQKV = 1728;
constexpr unsigned TKT = 18 * 1024;
constexpr unsigned ACT_BYTES = 128 * TKT;
constexpr unsigned IMG_BYTES = 7 * TKT;
constexpr unsigned WS_FLAGS = 4096;
constexpr unsigned FL_W1 = 0, FL_W2 = 1, FL_W4 = 2, FL_W5 = 3, FL_IA = 4, FL_N = 8;
constexpr unsigned KVP_BYTES = 256 * 3072, KVH_BYTES = 256 * 20480, ST_BYTES = 128 * 2304, UNIT = 256 * 1024;
constexpr unsigned SETA = KVP_BYTES + 2 * ACT_BYTES + ST_BYTES;
constexpr unsigned N0 = 23, N1 = 40, N2 = 52;
constexpr unsigned WS_S0 = 524288, WS_S1 = WS_S0 + N0 * UNIT, WS_S2 = WS_S1 + N1 * UNIT;
static_assert(WS_FLAGS + FL_N * 256 * 128 <= WS_S0 && SETA <= N0 * UNIT && KVH_BYTES + 2 * ACT_BYTES + ST_BYTES <= N1 * UNIT && SETA + KVH_BYTES + ACT_BYTES <= N2 * UNIT, "map");
__host__ __device__ constexpr unsigned ws_k(int layer) { return layer == 0 ? WS_S0 : layer == 1 ? WS_S1 : layer == 2 ? WS_S2 : WS_S2 + SETA; }
__host__ __device__ constexpr unsigned ws_o(int layer) { return ws_k(layer) + ((layer & 1) ? KVH_BYTES : KVP_BYTES); }
constexpr unsigned WS_IMA = WS_S2 + N2 * UNIT;
constexpr unsigned WS_IMB = WS_IMA + 16 * IMG_BYTES;
constexpr unsigned WS_WOI = WS_IMB + 16 * IMG_BYTES;
constexpr unsigned WS_CSP = WS_WOI + 2 * 36 * TKT;
constexpr unsigned WS_END = WS_CSP + 2 * 16 * 18 * 448;
constexpr unsigned L_OST = 5 * TKT;
constexpr unsigned L_STG = IMG_BYTES;
constexpr unsigned L_KT = L_STG - 40960;
constexpr unsigned L_QS = L_STG + 20480;
constexpr unsigned L_PK = L_STG, L_VV = L_STG + 20480;
constexpr unsigned L_KVS = 0;
constexpr unsigned L_CS = L_STG + 32768;
constexpr unsigned L_PST = 147456;
constexpr unsigned LDS_BYTES = 163840;
constexpr unsigned CODE_BYTES = 42 * 1024;
constexpr unsigned SPIN_CAP = 1u << 17;
constexpr int DSLEEP = 16, OSLEEP = 10, XSLEEP = 10, RSLEEP = 6;
constexpr float LOG2E = 1.4426950408889634f;
constexpr float NEGBIG = -1e30f;

struct Params {
    const u16* x; const u16* wqkvA; const u16* wphi; const u16* woA; const u16* wqkvB; const u16* woB;
    u16* out; unsigned char* ws;
};

DEVI f32x4 mfma32(u32x4 a, u32x4 b, f32x4 c) { return __builtin_amdgcn_mfma_f32_16x16x32_bf16(__builtin_bit_cast(bf16x8, a), __builtin_bit_cast(bf16x8, b), c, 0, 0, 0); }
DEVI f32x16 mfmaL(u32x4 a, u32x4 b, f32x16 c) { return __builtin_amdgcn_mfma_f32_32x32x16_bf16(__builtin_bit_cast(bf16x8, a), __builtin_bit_cast(bf16x8, b), c, 0, 0, 0); }
DEVI u32x4 w1(unsigned a) { return (u32x4){a, 0u, 0u, 0u}; }
DEVI unsigned pkv(f32x2 f) { return __builtin_bit_cast(unsigned, __builtin_convertvector(f, bf16x2)); }
DEVI unsigned pk2(float lo, float hi) { return pkv((f32x2){lo, hi}); }
DEVI float dot2(unsigned a, unsigned b, float c) { return __builtin_amdgcn_fdot2_f32_bf16(__builtin_bit_cast(bf16x2, a), __builtin_bit_cast(bf16x2, b), c, false); }
DEVI float ex2(float x) { return __builtin_amdgcn_exp2f(x); }
DEVI float rcp(float x) { return __builtin_amdgcn_rcpf(x); }
DEVI float f_of(unsigned u) { return __builtin_bit_cast(float, u); }
DEVI unsigned u_of(float f) { return __builtin_bit_cast(unsigned, f); }
DEVI f32x2 fma2(f32x2 a, f32x2 b, f32x2 c) { return __builtin_elementwise_fma(a, b, c); }
DEVI f32x2 sp2(float v) { return (f32x2){v, v}; }
DEVI f32x2 sw16(float a, float b) { const auto r = __builtin_amdgcn_permlane16_swap(u_of(a), u_of(b), false, false); return (f32x2){f_of(r[0]), f_of(r[1])}; }
DEVI f32x2 sw32(float a, float b) { const auto r = __builtin_amdgcn_permlane32_swap(u_of(a), u_of(b), false, false); return (f32x2){f_of(r[0]), f_of(r[1])}; }
DEVI float vmax(f32x2 v) { float r; asm("v_max_f32 %0, %1, %2" : "=v"(r) : "v"(v.x), "v"(v.y)); return r; }
DEVI float vmaxn(f32x2 v) { float r; asm("v_max_f32_e64 %0, -%1, -%2" : "=v"(r) : "v"(v.x), "v"(v.y)); return r; }
DEVI float max3f(float a, float b, float c) { return __builtin_fmaxf(__builtin_fmaxf(a, b), c); }
DEVI float min3f(float a, float b, float c) { return __builtin_fminf(__builtin_fminf(a, b), c); }
DEVI u32x4 unit_pair(unsigned a0, unsigned a1, unsigned b0, unsigned b1) {
    const auto s = __builtin_amdgcn_permlane16_swap(a0, b0, false, false);
    const auto t = __builtin_amdgcn_permlane16_swap(a1, b1, false, false);
    return (u32x4){s[0], t[0], s[1], t[1]};
}

#define BARRIER() asm volatile("s_waitcnt lgkmcnt(0)\n\ts_barrier" ::: "memory")
#define DRAIN() asm volatile("s_waitcnt vmcnt(0)" ::: "memory")
#define SB() __builtin_amdgcn_sched_barrier(0)

extern __shared__ __attribute__((aligned(16))) unsigned char smem[];
DEVI u32x4 lds_r128(unsigned off) { return *(const LAS u32x4*)((LAS unsigned char*)smem + off); }
DEVI f32x4 lds_rf4(unsigned off) { return *(const LAS f32x4*)((LAS unsigned char*)smem + off); }
DEVI unsigned lds_r32(unsigned off) { return *(const LAS unsigned*)((LAS unsigned char*)smem + off); }
DEVI void lds_w128(unsigned off, u32x4 v) { *(LAS u32x4*)((LAS unsigned char*)smem + off) = v; }
DEVI void lds_w64(unsigned off, u32x2 v) { *(LAS u32x2*)((LAS unsigned char*)smem + off) = v; }
DEVI void lds_w32(unsigned off, unsigned v) { *(LAS unsigned*)((LAS unsigned char*)smem + off) = v; }
DEVI void lds_w16(unsigned off, unsigned v) { *(LAS u16*)((LAS unsigned char*)smem + off) = (u16)v; }
DEVI u32x2 lds_tr(unsigned off) { return __builtin_bit_cast(u32x2, __builtin_amdgcn_ds_read_tr16_b64_v4i16((LAS s16x4*)((LAS unsigned char*)smem + off))); }

typedef __amdgpu_buffer_rsrc_t rsrc_t;
DEVI rsrc_t mk_rsrc(const void* p, unsigned bytes) { return __builtin_amdgcn_make_buffer_rsrc((void*)p, 0, bytes, 0x00020000); }
DEVI u32x4 ld128c(rsrc_t r, unsigned voff, unsigned soff) { return __builtin_amdgcn_raw_buffer_load_b128(r, voff, soff, 16); }
DEVI void st128c(rsrc_t r, unsigned voff, unsigned soff, u32x4 v) {
    __builtin_amdgcn_raw_buffer_store_b128(v, r, voff, soff, 16);
    asm volatile("s_nop 1" :: "v"(v) : "memory");
}
DEVI void st64c(rsrc_t r, unsigned voff, unsigned soff, u32x2 v) { __builtin_amdgcn_raw_buffer_store_b64(v, r, voff, soff, 16); }
DEVI void st32c(rsrc_t r, unsigned voff, unsigned soff, unsigned v) { __builtin_amdgcn_raw_buffer_store_b32(v, r, voff, soff, 16); }
DEVI void dma1k(const void* sbase, unsigned voff, unsigned lds_dst) {
    unsigned keep;
    asm volatile("s_mov_b32 %0, m0\n\ts_mov_b32 m0, %3\n\ts_nop 0\n\tglobal_load_lds_dwordx4 %1, %2 sc1\n\ts_mov_b32 m0, %0" : "=&s"(keep) : "v"(voff), "s"(sbase), "s"(lds_dst) : "memory");
}
DEVI void dma1k_in(const void* sbase, unsigned voff, unsigned lds_dst) {
    unsigned keep;
    asm volatile("s_mov_b32 %0, m0\n\ts_mov_b32 m0, %3\n\ts_nop 0\n\tglobal_load_lds_dwordx4 %1, %2\n\ts_mov_b32 m0, %0" : "=&s"(keep) : "v"(voff), "s"(sbase), "s"(lds_dst) : "memory");
}
DEVI void ld128_raw(u32x4& d, const void* base, unsigned voff) { asm volatile("global_load_dwordx4 %0, %1, %2" : "=v"(d) : "v"(voff), "s"(base) : "memory"); }
DEVI void ld64_raw(u32x2& d, const void* base, unsigned voff) { asm volatile("global_load_dwordx2 %0, %1, %2" : "=v"(d) : "v"(voff), "s"(base) : "memory"); }
DEVI void wait_flags(const unsigned char* base, unsigned voff, unsigned tag) {
    const GAS unsigned* f = (const GAS unsigned*)(base + voff);
#pragma unroll 1
    for (unsigned sp = 0; sp < SPIN_CAP; ++sp) {
        const unsigned v = __hip_atomic_load(f, RLX_AGENT);
        if (__all(v == tag)) break;
        __builtin_amdgcn_s_sleep(1);
    }
    asm volatile("" ::: "memory");
}
DEVI unsigned umax3(unsigned a, unsigned b, unsigned c) { return __builtin_elementwise_max(__builtin_elementwise_max(a, b), c); }
DEVI unsigned umax4(unsigned m, u32x4 v) { return umax3(umax3(m, v.x, v.y), v.z, v.w); }
DEVI unsigned ok1(unsigned v) { return __builtin_elementwise_min(v, 0xFFFFFFFEu); }
DEVI u32x2 ok2(u32x2 v) { return (u32x2){ok1(v.x), ok1(v.y)}; }
DEVI u32x4 ok4(u32x4 v) { return (u32x4){ok1(v.x), ok1(v.y), ok1(v.z), ok1(v.w)}; }
DEVI void arm_unit(rsrc_t r, unsigned l16, unsigned unit, unsigned cu) { st128c(r, l16, WS_S0 + (unit * 256 + cu) * 1024, (u32x4){~0u, ~0u, ~0u, ~0u}); }
DEVI void wait_all256(const unsigned char* base, int lane, unsigned tag) {
    const GAS unsigned* f = (const GAS unsigned*)(base + lane * 128);
#pragma unroll 1
    for (unsigned sp = 0; sp < SPIN_CAP; ++sp) {
        const unsigned v0 = __hip_atomic_load(f, RLX_AGENT), v1 = __hip_atomic_load(f + 2048, RLX_AGENT), v2 = __hip_atomic_load(f + 4096, RLX_AGENT), v3 = __hip_atomic_load(f + 6144, RLX_AGENT);
        if (__all(v0 == tag && v1 == tag && v2 == tag && v3 == tag)) break;
        __builtin_amdgcn_s_sleep(1);
    }
    asm volatile("" ::: "memory");
}
DEVI u32x4 peek256(const unsigned char* base, int lane) {
    const GAS unsigned* f = (const GAS unsigned*)(base + lane * 128);
    return (u32x4){__hip_atomic_load(f, RLX_AGENT), __hip_atomic_load(f + 2048, RLX_AGENT), __hip_atomic_load(f + 4096, RLX_AGENT), __hip_atomic_load(f + 6144, RLX_AGENT)};
}
DEVI void judge256(u32x4 v, const unsigned char* base, int lane, unsigned tag) {
    if (!__all(v.x == tag && v.y == tag && v.z == tag && v.w == tag)) wait_all256(base, lane, tag);
    asm volatile("" ::: "memory");
}
#define RETRY(LOADS, MAXEXPR) do { _Pragma("unroll 1") for (unsigned sp_ = 0; sp_ < SPIN_CAP; ++sp_) { if (!__any((MAXEXPR) == 0xFFFFFFFFu)) break; \
    if (sp_ < 4) __builtin_amdgcn_s_sleep(RSLEEP); else __builtin_amdgcn_s_sleep(48); asm volatile("" ::: "memory"); LOADS; } } while (0)
DEVI const unsigned char* flag_at(const unsigned char* ws, unsigned kind, unsigned idx) { return ws + WS_FLAGS + (kind * 256 + idx) * 128; }
DEVI void set_flag(unsigned char* ws, unsigned kind, unsigned idx, unsigned tag) {
    __hip_atomic_store((GAS unsigned*)(ws + WS_FLAGS + (kind * 256 + idx) * 128), tag, RLX_AGENT);
}


DEVI u32x4 onehot8(int j) {
    const unsigned v = (j & 1) ? 0x3F800000u : 0x3F80u; const int w = j >> 1;
    return (u32x4){w == 0 ? v : 0u, w == 1 ? v : 0u, w == 2 ? v : 0u, w == 3 ? v : 0u};
}
DEVI int wo_krow(int mat, int s, int gq, int j) {
    const int a = 36 * s + (mat ? 18 * (gq >> 1) + 8 * (j >> 2) + 4 * (gq & 1) : 18 * (j >> 2) + 4 * gq) + (j & 3);
    const int c = 36 * (8 * (s - 16) + 2 * gq + (j >> 2)) + 18 * ((j >> 1) & 1) + 16 + (j & 1);
    return s < 16 ? a : c;
}
DEVI int fresh(int v) { asm volatile("" : "+v"(v)); return v; }
DEVI int sfresh(int v) { asm volatile("" : "+s"(v)); return v; }

template <int T0, int NT, int S0, int S1, bool SSQ, int D, class F>
DEVI void gemm_img(f32x4 (&acc)[7], u32x4 (&xf)[18], const unsigned (&lb)[3], f32x4& sq, F side) {
    u32x4 a[D][NT];
#pragma unroll
    for (int s = S0; s < S0 + D; ++s)
#pragma unroll
        for (int t = 0; t < NT; ++t) a[s % D][t] = lds_r128(lb[s / 6] + (T0 + t) * TKT + (s % 6) * 1024);
    SB();
#pragma unroll
    for (int s = S0; s < S1; ++s) {
        side(s);
        SB();
        if (SSQ) { sq = mfma32(xf[s], xf[s], sq); SB(); }
#pragma unroll
        for (int t = 0; t < NT; ++t) {
            acc[T0 + t] = mfma32(a[s % D][t], xf[s], acc[T0 + t]);
            SB();
            if (s + D < S1) { a[s % D][t] = lds_r128(lb[(s + D) / 6] + (T0 + t) * TKT + ((s + D) % 6) * 1024); SB(); }
        }
    }
}
template <bool SUM>
DEVI void put_frag(rsrc_t wsr, unsigned dst, unsigned ks, int t, u32x4 f, int lane) {
    st128c(wsr, lane * 16, dst, f);
    if (!SUM) return;
    f32x4 c = mfma32((u32x4){0x3F803F80u, 0x3F803F80u, 0x3F803F80u, 0x3F803F80u}, f, (f32x4){0.f, 0.f, 0.f, 0.f});
    asm volatile("s_nop 7" : "+v"(c));
    if (lane < 16) st32c(wsr, lane * 4, WS_CSP + ks * 448 + t * 64, u_of(c[0]));
}
DEVI void colsum_get(rsrc_t wsr, unsigned img_p, unsigned csb, int lane) {
    u32x2 v[18];
#pragma unroll
    for (int s = 0; s < 18; ++s) v[s] = __builtin_amdgcn_raw_buffer_load_b64(wsr, lane < 56 ? lane * 8 : 0, WS_CSP + (img_p * 18 + s) * 448, 16);
    SB();
    f32x2 c = __builtin_bit_cast(f32x2, v[0]);
#pragma unroll
    for (int s = 1; s < 18; ++s) c += __builtin_bit_cast(f32x2, v[s]);
    if (lane < 56) lds_w64(csb + ((lane >> 1) & 3) * 112 + (lane >> 3) * 16 + (lane & 1) * 8, __builtin_bit_cast(u32x2, c));
}
DEVI void colsum_tile(int t, unsigned csb, int lane) {
    f32x4 c = {0.f, 0.f, 0.f, 0.f}; const u32x4 ones = {0x3F803F80u, 0x3F803F80u, 0x3F803F80u, 0x3F803F80u};
    u32x4 a[18];
#pragma unroll
    for (int s = 0; s < 18; ++s) a[s] = lds_r128(t * TKT + s * 1024 + lane * 16);
    SB();
#pragma unroll
    for (int s = 0; s < 18; ++s) c = mfma32(a[s], ones, c);
    asm volatile("s_nop 7" : "+v"(c));
    if ((lane & 15) == 0) lds_w128(csb + ((lane >> 4) * 28 + 4 * t) * 4, __builtin_bit_cast(u32x4, c));
}
DEVI void dual_softmax(const float (&t)[9], float ks, bool lo, u32x4& fa, u32x4& fb, unsigned& fe) {
    const float zA = lo ? t[8] : t[0], zB = lo ? t[4] : t[8];
    const float mA = max3f(max3f(t[0], t[1], t[2]), t[3], zA), nA = min3f(min3f(t[0], t[1], t[2]), t[3], zA);
    const float mB = max3f(max3f(t[4], t[5], t[6]), t[7], zB), nB = min3f(min3f(t[4], t[5], t[6]), t[7], zB);
    const float T = -ks * vmax(sw32(vmax(sw16(mA, mB)), vmaxn(sw16(nA, nB))));
    const f32x2 u = sw16(T, T);
    const f32x2 sa = sw32(u.x, u.x), sb = sw32(u.y, u.y);
    const f32x2 s8 = lo ? sa : sb, k2 = {ks, -ks};
    f32x2 e[9];
#pragma unroll
    for (int i = 0; i < 9; ++i) { const f32x2 z = fma2(sp2(t[i]), k2, i < 4 ? sa : i < 8 ? sb : s8); e[i] = (f32x2){ex2(z.x), ex2(z.y)}; }
    const float wl = lo ? 1.f : 0.f;
    const f32x2 sA = fma2(e[8], sp2(wl), (e[0] + e[1]) + (e[2] + e[3])), sB = fma2(e[8], sp2(1.f - wl), (e[4] + e[5]) + (e[6] + e[7]));
    const f32x2 p = sw16(sA.x, sB.x), q = sw16(sA.y, sB.y), w = sw32(p.x + p.y, q.x + q.y);
    const float R = rcp(w.x + w.y);
    const f32x2 v = sw16(R, R);
    const f32x2 ra = sw32(v.x, v.x), rb = sw32(v.y, v.y), r8 = lo ? ra : rb;
    fa = (u32x4){pkv(e[0] * ra), pkv(e[1] * ra), pkv(e[2] * ra), pkv(e[3] * ra)};
    fb = (u32x4){pkv(e[4] * rb), pkv(e[5] * rb), pkv(e[6] * rb), pkv(e[7] * rb)};
    fe = pkv(e[8] * r8);
}

DEVI f32x16 att_scores(u32x4 km, u32x4 kl, u32x4 q0, u32x4 q1, f32x16 c) { return mfmaL(kl, q1, mfmaL(km, q0, c)); }
DEVI u32x4 att_p(const f32x16& s, int b) { return (u32x4){pk2(ex2(s[b]), ex2(s[b + 1])), pk2(ex2(s[b + 2]), ex2(s[b + 3])), pk2(ex2(s[b + 4]), ex2(s[b + 5])), pk2(ex2(s[b + 6]), ex2(s[b + 7]))}; }
DEVI void att_accum(const f32x16& s, const u32x2 (&v)[4], f32x16& o) {
    const u32x4 p0 = {pk2(ex2(s[0]), ex2(s[1])), pk2(ex2(s[2]), ex2(s[3])), pk2(ex2(s[4]), ex2(s[5])), pk2(ex2(s[6]), ex2(s[7]))};
    o = mfmaL((u32x4){v[0].x, v[0].y, v[1].x, v[1].y}, p0, o);
    const u32x4 p1 = {pk2(ex2(s[8]), ex2(s[9])), pk2(ex2(s[10]), ex2(s[11])), pk2(ex2(s[12]), ex2(s[13])), pk2(ex2(s[14]), ex2(s[15]))};
    o = mfmaL((u32x4){v[2].x, v[2].y, v[3].x, v[3].y}, p1, o);
}
DEVI void att_vload(u32x2 (&v)[4], unsigned va) {
#pragma unroll
    for (int e = 0; e < 4; ++e) v[e] = lds_tr(va + e * 384);
}
DEVI float max16(const f32x16& s, float m) {
#pragma unroll
    for (int v = 0; v < 16; v += 2) m = max3f(m, s[v], s[v + 1]);
    return m;
}
DEVI f32x16 splat16(float v) { f32x16 r;
#pragma unroll
    for (int i = 0; i < 16; ++i) r[i] = v;
    return r; }

struct Ctx { int wave, g, p; unsigned tag; };

template <bool ISB>
DEVI void layer_fn(const Params& P, const rsrc_t wsr, const int lane_in, const Ctx& C, const int it, u32x4 (&xf)[18], f32x4 (&res)[3], f32x4 (&acc)[7], float& ssq) {
    const int wave = sfresh(C.wave), g = sfresh(C.g), p = sfresh(C.p), cu = g * 16 + p;
    const unsigned tag = C.tag;
    const int layer = 2 * it + (ISB ? 1 : 0);
    const int tp = (p >> 1) & 3, cg = (p >> 3) * 2 + (p & 1);
    const f32x4 Z4 = {0.f, 0.f, 0.f, 0.f};
    constexpr unsigned csb = L_CS + (ISB ? 448 : 0);
    const bool first = !ISB && it == 0, last = ISB && it == 1;
    u32x4 wof[18];
    float mean, rstd;
    const unsigned wob = WS_WOI + ((ISB ? 36 : 0) + 9 * cg + wave) * TKT;
    const unsigned w1k = wave * 1024;
    const unsigned kbase = ISB ? (it ? ws_k(3) : ws_k(1)) : (it ? ws_k(2) : ws_k(0)), obase = kbase + (ISB ? KVH_BYTES : KVP_BYTES);
    const int rot = first ? 0 : p % 3;
    const unsigned ro0 = rot * 6144, ro1 = rot == 2 ? 0 : ro0 + 6144, ro2 = rot ? ro0 - 6144 : 12288;
    const unsigned char* nimg = P.ws + (ISB ? WS_IMA : WS_IMB) + p * IMG_BYTES + w1k;
    {
        const int lane = fresh(lane_in), gq = lane >> 4, tk = lane & 15;
        const unsigned l16 = lane * 16;
        const unsigned lb[3] = {l16 + ro0, l16 + ro1, l16 + ro2};
        if (!first) {
#pragma unroll
            for (int t = 0; t < 5; ++t) acc[t] = Z4;
            const unsigned xprev = (ISB ? (it ? ws_o(2) : ws_o(0)) : ws_o(1)) + ACT_BYTES;
            const unsigned xb = xprev + (g * 8 + wave) * TKT, sb = xprev + ACT_BYTES + (g * 8 + wave) * 2304;
            unsigned st[9];
            auto xload = [&](int s0, int s1) {
#pragma unroll
                for (int s = s0; s < s1; ++s) xf[s] = ld128c(wsr, l16 + (s % 6) * 1024, xb + (s < 6 ? ro0 : s < 12 ? ro1 : ro2));
            };
            auto xmax = [&](int s0, int s1) { unsigned m = 0u;
#pragma unroll
                for (int s = s0; s < s1; ++s) m = umax4(m, xf[s]);
                return m; };
            auto sload = [&]() {
#pragma unroll
                for (int j = 0; j < 9; ++j) st[j] = __builtin_amdgcn_raw_buffer_load_b32(wsr, lane * 4 + j * 256, sb, 16);
            };
            asm volatile("s_waitcnt vmcnt(2)" ::: "memory");
            BARRIER();
            __builtin_amdgcn_s_sleep(XSLEEP);
            sload(); xload(0, 3);
            RETRY({ sload(); xload(0, 3); }, umax3(umax3(umax3(umax3(umax3(xmax(0, 3), st[0], st[1]), st[2], st[3]), st[4], st[5]), st[6], st[7]), st[8], 0u));
            xload(3, 9);
            ssq = ((f_of(st[0]) + f_of(st[1])) + (f_of(st[2]) + f_of(st[3]))) + ((f_of(st[4]) + f_of(st[5])) + (f_of(st[6]) + f_of(st[7]))) + f_of(st[8]);
            const unsigned char* cimg = P.ws + (ISB ? WS_IMB : WS_IMA) + p * IMG_BYTES + w1k;
            f32x4 dummy;
            gemm_img<0, 5, 0, 18, false, 2>(acc, xf, lb, dummy, [&](int s) {
                if (s < 3) xload(9 + 2 * s, 11 + 2 * s);
                if (s == 3) RETRY(xload(3, 9), xmax(3, 9));
                if (s >= 3 && s < 6) xload(12 + s, 13 + s);
                if (s == 9) RETRY(xload(9, 18), xmax(9, 18));
                if (s >= 9 && s < 14 && (s < 13 || wave < 4)) dma1k(cimg + (90 + 8 * (s - 9)) * 1024, l16, w1k + (90 + 8 * (s - 9)) * 1024);
                if (!ISB && s >= 9) wof[s] = ld128c(wsr, l16 + s * 1024, wob);
                if (ISB && it == 0 && s >= 14 && s < 17) arm_unit(wsr, l16, 63 + (s - 14) * 8 + wave, cu);
                if (!ISB && s >= 14 && (s < 17 || wave < 4)) arm_unit(wsr, l16, 87 + (s - 14) * 8 + wave, cu);
            });
        }
        f32x4 cs[5];
#pragma unroll
        for (int t = 0; t < 5; ++t) cs[t] = lds_rf4(csb + gq * 112 + 16 * t);
        acc[5] = Z4; acc[6] = Z4;
        const f32x2 r1 = sw16(ssq, ssq); const float h1 = r1.x + r1.y; const f32x2 r2 = sw32(h1, h1);
        asm volatile("s_nop 7" : "+v"(acc[4]));
        mean = acc[4][2] * (1.f / DM);
        rstd = __builtin_amdgcn_rsqf(__builtin_fmaxf((r2.x + r2.y) * (1.f / DM) - mean * mean, 0.f) + 1e-5f);
        const int hx = gq >> 1, od = gq & 1;
        f32x2 y[9];
        {
            const f32x2 nm = sp2(-mean);
#pragma unroll
            for (int t = 0; t < 5; ++t) {
                y[2 * t] = fma2(nm, (f32x2){cs[t][0], cs[t][1]}, (f32x2){acc[t][0], acc[t][1]});
                if (t < 4) y[2 * t + 1] = fma2(nm, (f32x2){cs[t][2], cs[t][3]}, (f32x2){acc[t][2], acc[t][3]});
            }
        }
        const u32x4 vm = unit_pair(pk2(y[4].y * rstd, y[5].x * rstd), pk2(y[5].y * rstd, y[6].x * rstd), pk2(y[6].y * rstd, y[7].x * rstd), pk2(y[7].y * rstd, y[8].x * rstd));
        const float v8 = y[8].y * rstd;
        const unsigned vl = pkv(sw16(v8, v8));
        if (ISB) {
            const f32x2 r2 = sp2(rstd);
            const u32x4 km = unit_pair(pkv(y[0] * r2), pkv(y[1] * r2), pkv(y[2] * r2), pkv(y[3] * r2));
            const float k8 = y[4].x * rstd;
            const u32x4 lr = {ok1(pkv(sw16(k8, k8))), 0u, ok1(vl), 0x3F80u};
            const unsigned tb = (wave >> 1) * 5120 + (wave & 1) * 256, tc = (wave >> 1) * 5120 + (wave & 1) * 768 + 1024;
            const unsigned oc = (tk >> 3) * 384 + (tk & 7) * 16;
            const unsigned a0 = od * 2560 + hx * 512 + tk * 16, a1 = od * 2560 + hx * 128 + oc, a2 = hx * 2560 + 256 + (oc ^ 64);
            const unsigned hb = kbase + cu * 20480;
            st128c(wsr, a0, hb + tb, ok4(km)); st128c(wsr, a1, hb + tc, ok4(vm));
            lds_w128(L_STG + tb + a0, km); lds_w128(L_STG + tc + a1, vm);
            if (od == 0) { st128c(wsr, a2, hb + tc, lr); lds_w128(L_STG + tc + a2, lr); }
            asm volatile("s_waitcnt vmcnt(3)" ::: "memory");
            BARRIER();
            if (it == 0 && wave == 7 && lane == 0) set_flag(P.ws, FL_W4, cu, tag);
        } else {
            const float kz[9] = {y[0].x, y[0].y, y[1].x, y[1].y, y[2].x, y[2].y, y[3].x, y[3].y, y[4].x};
            u32x4 fa, fb; unsigned fe;
            dual_softmax(kz, rstd * LOG2E, gq < 2, fa, fb, fe);
            const unsigned pk = L_PK + wave * 1280 + tk * 80, vv = L_VV + wave * 768 + tk * 48;
            lds_w128(pk + gq * 16, fa); lds_w128(pk + gq * 16 + 10240, fb); lds_w32(pk + hx * 10240 + 64 + od * 4, fe);
            lds_w128(vv + od * 6144 + hx * 16, vm);
            lds_w64(vv + hx * 6144 + 32, (u32x2){vl, 0x3F80u});
            if (first && wave == 3) wait_all256(flag_at(P.ws, FL_W1, 0), lane, tag);
            DRAIN();
            BARRIER();
            if (wave == 7 && lane == 0) set_flag(P.ws, it ? FL_W5 : FL_W2, cu, tag);
            if (wave < 6) {
                const int h = wave >= 3, ft = wave - 3 * h;
                const unsigned trow = (unsigned)(lane >> 2), tcol = (unsigned)(lane & 3) * 8u;
                const unsigned pa = L_PK + h * 10240 + ft * 32 + trow * 80 + tcol, pb = L_VV + h * 6144 + trow * 48 + tcol;
                u32x2 ta[8], tb[8], td[8];
#pragma unroll
                for (int s = 0; s < 8; ++s) { ta[s] = lds_tr(pa + (16 * s) * 80); tb[s] = lds_tr(pb + (16 * s) * 48); td[s] = lds_tr(pb + (16 * s) * 48 + 32); }
                SB();
                f32x4 k0 = Z4, k1 = Z4;
#pragma unroll
                for (int s = 0; s < 4; ++s) {
                    const u32x4 af = {ta[2 * s].x, ta[2 * s].y, ta[2 * s + 1].x, ta[2 * s + 1].y};
                    k0 = mfma32((u32x4){tb[2 * s].x, tb[2 * s].y, tb[2 * s + 1].x, tb[2 * s + 1].y}, af, k0);
                    k1 = mfma32((u32x4){td[2 * s].x, td[2 * s].y, td[2 * s + 1].x, td[2 * s + 1].y}, af, k1);
                }
                const u32x2 s0 = ok2((u32x2){pk2(k0[0], k0[1]), pk2(k0[2], k0[3])}), s1 = ok2((u32x2){pk2(k1[0], k1[1]), pk2(k1[2], k1[3])});
                const unsigned kb = kbase + (p * 16 + g) * 3072 + h * 1536;
                if (ft < 2) {
                    st64c(wsr, lane * 8, kb + ft * 512, s0);
                    if (gq == 0) st64c(wsr, lane * 8, kb + 1024 + ft * 128, s1);
                } else if (tk < 4) {
                    st64c(wsr, gq * 32 + tk * 8, kb + 1280, s0);
                    if (gq == 0) st64c(wsr, tk * 8, kb + 1408, s1);
                }
            }
        }
        if (first && wave >= 6) colsum_tile(wave - 1, csb, lane);
    }
    {
        const int lane = fresh(lane_in), gq = lane >> 4, tk = lane & 15;
        const unsigned l16 = lane * 16;
        const unsigned lb[3] = {l16 + ro0, l16 + ro1, l16 + ro2};
        f32x16 msk;
        const int dl = (lane & 31) - 4 * (lane >> 5);
        { f32x4 dummy; gemm_img<5, 2, 0, 18, false, 3>(acc, xf, lb, dummy, [&](int s) {
              if (!ISB && (s & 1) == 0) wof[s >> 1] = ld128c(wsr, l16 + (s >> 1) * 1024, wob);
              if (ISB && s < 16) msk[s] = ((s & 3) + 8 * (s >> 2) > dl) ? NEGBIG : 0.f;
              if ((s & 1) && first) wof[9 + (s >> 1)] = ld128c(wsr, l16 + (9 + (s >> 1)) * 1024, wob); }); }
        if (!ISB) BARRIER();
        float q[9];
        {
            const unsigned ca = csb + gq * 112;
            const float c19 = f_of(lds_r32(ca + 76));
            const f32x4 c5 = lds_rf4(ca + 80), c6 = lds_rf4(ca + 96);
            q[0] = acc[4][3] - mean * c19;
#pragma unroll
            for (int i = 0; i < 4; ++i) { q[1 + i] = acc[5][i] - mean * c5[i]; q[5 + i] = acc[6][i] - mean * c6[i]; }
        }
        if (ISB) {
            u32x4 hv[5];
            const int src = g - 2 + (wave >> 2);
            auto hload = [&]() {
                const unsigned hb = kbase + (src * 16 + p) * 20480 + (wave & 3) * 5120;
#pragma unroll
                for (int i = 0; i < 5; ++i) hv[i] = ld128c(wsr, l16 + i * 1024, hb);
            };
            if (src >= 0) {
                hload();
            } else {
#pragma unroll
                for (int i = 0; i < 5; ++i) hv[i] = (u32x4){0u, (i == 0 || (i == 2 && lane >= 32) || (i == 3 && lane < 32)) ? 0u : 0xF149u, 0u, 0u};
            }
            {
                const float qs = rstd * (0.23570226039551584f * LOG2E);
                const int hx = gq >> 1, od = gq & 1;
                const unsigned qb = L_QS + (wave >> 1) * 2304;
                lds_w128(qb + (wave & 1) * 256 + od * 1152 + hx * 512 + tk * 16, unit_pair(pk2(q[0] * qs, q[1] * qs), pk2(q[2] * qs, q[3] * qs), pk2(q[4] * qs, q[5] * qs), pk2(q[6] * qs, q[7] * qs)));
                const float q8 = q[8] * qs;
                lds_w32(qb + (wave & 1) * 64 + 1024 + hx * 1152 + tk * 4, pkv(sw16(q8, q8)));
            }
            const int h = wave & 1, jq = wave < 4 ? (wave >> 1) : 3 - ((wave - 4) >> 1);
            const int r = lane & 31, hh = lane >> 5;
            const unsigned kb = L_KT + h * 2560 + jq * 5120;
            const unsigned ka = kb + l16, la = kb + 1280 + (r >> 3) * 384 + ((r & 7) ^ 4) * 16;
            const unsigned k8 = 4 * hh + ((lane >> 2) & 3);
            const unsigned va = kb + 1024 + ((lane & 16) ? 256 + (k8 ^ 4) * 16 + 8 : (lane & 2) * 64 + k8 * 16 + (lane & 1) * 8);
            BARRIER();
            const u32x4 q0 = lds_r128(L_QS + (jq * 2 + h) * 1152 + l16);
            const unsigned q1w = lds_r32(L_QS + (jq * 2 + h) * 1152 + 1024 + r * 4);
            u32x4 km = lds_r128(ka + 8 * 5120), kl = lds_r128(la + 8 * 5120);
            u32x2 vf[2][4];
            att_vload(vf[0], va + 8 * 5120);
            const u32x4 q1 = {hh ? 0u : q1w, hh ? 0u : 0x3F80u, 0u, 0u};
            f32x16 sc[2], negm, o16 = splat16(0.f);
            sc[0] = att_scores(km, kl, q0, q1, msk);
            {
                const float m = max16(sc[0], NEGBIG);
                const float nm = -vmax(sw32(m, m));
                negm = splat16(nm);
                sc[0] += negm;
                msk = (splat16(NEGBIG) - msk) + negm;
            }
#pragma unroll
            for (int i = 8; i >= 0; --i) {
                if (i == 8) {
                    RETRY(hload(), umax4(umax4(umax4(umax4(umax4(0u, hv[0]), hv[1]), hv[2]), hv[3]), hv[4]));
#pragma unroll
                    for (int j = 0; j < 5; ++j) lds_w128(L_KT + wave * 5120 + j * 1024 + l16, hv[j]);
                    BARRIER();
                }
                SB();
                if (i > 0) { km = lds_r128(ka + (i - 1) * 5120); kl = lds_r128(la + (i - 1) * 5120); att_vload(vf[(i - 1) & 1], va + (i - 1) * 5120); }
                wof[16 - 2 * i] = ld128c(wsr, l16 + (16 - 2 * i) * 1024, wob); wof[17 - 2 * i] = ld128c(wsr, l16 + (17 - 2 * i) * 1024, wob);
                SB();
                const u32x4 p0 = att_p(sc[i & 1], 0);
                SB();
                if (i > 0) sc[(i - 1) & 1] = att_scores(km, kl, q0, q1, i == 1 ? msk : negm);
                o16 = mfmaL((u32x4){vf[i & 1][0].x, vf[i & 1][0].y, vf[i & 1][1].x, vf[i & 1][1].y}, p0, o16);
                SB();
                const u32x4 p1 = att_p(sc[i & 1], 8);
                if (i < 7 && !last) {
                    if (i > 0 || wave < 6) dma1k(nimg + 8 * (6 - i) * 1024, l16, w1k + 8 * (6 - i) * 1024);
                }
                SB();
                o16 = mfmaL((u32x4){vf[i & 1][2].x, vf[i & 1][2].y, vf[i & 1][3].x, vf[i & 1][3].y}, p1, o16);
            }
            asm volatile("s_nop 7\n\ts_nop 7" : "+v"(o16));
            {
                float t = 0.f;
#pragma unroll
                for (int v = 0; v < 11; ++v) t += __builtin_fabsf(o16[v]);
                if (__any(!(t < 3.0e38f))) {
                    float m = NEGBIG;
#pragma unroll 1
                    for (int pass = 0; pass < 2; ++pass) {
                        o16 = splat16(0.f);
#pragma unroll 1
                        for (int i = 0; i <= 8; ++i) {
                            u32x2 v4[4]; att_vload(v4, va + i * 5120);
                            f32x16 s = att_scores(lds_r128(ka + i * 5120), lds_r128(la + i * 5120), q0, q1, splat16(pass ? -m : 0.f));
                            const int lim = i == 8 ? dl : 99, lim2 = i == 0 ? dl : -99;
#pragma unroll
                            for (int v = 0; v < 16; ++v) { const int cv = (v & 3) + 8 * (v >> 2); if (cv > lim || cv <= lim2) s[v] = NEGBIG; }
                            if (pass) att_accum(s, v4, o16); else m = max16(s, m);
                        }
                        if (!pass) m = vmax(sw32(m, m));
                    }
                    asm volatile("s_nop 7\n\ts_nop 7" : "+v"(o16));
                }
            }
            const float inv = rcp(o16[10]);
            const unsigned ob = obase + (g * 8 + 2 * jq) * TKT;
            const unsigned vo = (r >> 4) * TKT + (r & 15) * 16;
            st128c(wsr, vo + hh * 256, ob + p * 1024 + h * 512, ok4((u32x4){pk2(o16[0] * inv, o16[1] * inv), pk2(o16[2] * inv, o16[3] * inv), pk2(o16[4] * inv, o16[5] * inv), pk2(o16[6] * inv, o16[7] * inv)}));
            if (hh == 0) st32c(wsr, (r >> 4) * TKT + (r & 15) * 4, ob + 16384 + p * 128 + h * 64, ok1(pk2(o16[8] * inv, o16[9] * inv)));
        } else {
            u32x4 pv[8];
            auto pload = [&]() {
                const unsigned kb = kbase + p * 16 * 3072 + wave * 256;
#pragma unroll
                for (int i = 0; i < 8; ++i) if (i < 4 || wave < 4) pv[i] = ld128c(wsr, tk * 16 + gq * 3072, kb + (i & 3) * (4 * 3072) + (i >> 2) * 2048);
            };
#pragma unroll
            for (int i = 4; i < 8; ++i) pv[i] = (u32x4){0u, 0u, 0u, 0u};
            pload();
            u32x4 pk = {tag, tag, tag, tag};
            if (wave == 7) pk = peek256(flag_at(P.ws, it ? FL_W5 : FL_W2, 0), lane);
            u32x4 fq[2]; unsigned fe;
            dual_softmax(q, rstd * LOG2E, gq < 2, fq[0], fq[1], fe);
            const unsigned q4 = (lane >> 2) & 3, c4 = lane & 3, fm = (gq & 1) * 8 + q4, fx = (gq & 1) * 2 + (q4 & 1);
            const unsigned ra = L_KVS + (gq >> 1) * 512 + c4 * 128 + fm * 8, rb = L_KVS + 1024 + (gq >> 1) * 128 + fm * 8;
            const unsigned rc = L_KVS + 1280 + c4 * 32 + fx * 8, rd = L_KVS + 1408 + fx * 8;
            {
                const unsigned k0 = (wave == 5 && tk >= 10) ? 0u : ~0u, k1 = (wave == 3 && tk >= 10) ? 0u : ~0u;
                RETRY(pload(), __builtin_elementwise_max(k0 & umax4(umax4(umax4(umax4(0u, pv[0]), pv[1]), pv[2]), pv[3]), k1 & umax4(umax4(umax4(umax4(0u, pv[4]), pv[5]), pv[6]), pv[7])));
                const u32x4 sel = onehot8(tk < 8 ? tk : -2);
                f32x4 s0 = Z4, s1 = Z4;
#pragma unroll
                for (int i = 0; i < 4; ++i) { s0 = mfma32(sel, pv[i], s0); s1 = mfma32(sel, pv[4 + i], s1); }
                const u32x2 w0 = {pk2(s0[0], s0[1]), pk2(s0[2], s0[3])}, w1v = {pk2(s1[0], s1[1]), pk2(s1[2], s1[3])};
                if (gq < 2) {
                    lds_w64(L_KVS + wave * 256 + tk * 16 + gq * 8, w0);
                    if (wave < 4) lds_w64(L_KVS + 2048 + wave * 256 + tk * 16 + gq * 8, w1v);
                }
            }
            judge256(pk, flag_at(P.ws, it ? FL_W5 : FL_W2, 0), lane, tag);
            if (first && wave == 6) colsum_get(wsr, 16 + p, L_CS + 448, lane);
            BARRIER();
            f32x4 oa[2][2];
            {
                u32x2 am[2][4], ae[2][2];
#pragma unroll
                for (int h = 0; h < 2; ++h) {
                    am[h][0] = lds_tr(ra + h * 1536); am[h][1] = lds_tr(ra + h * 1536 + 32); am[h][2] = lds_tr(rb + h * 1536); am[h][3] = lds_tr(rb + h * 1536 + 32);
                    ae[h][0] = lds_tr(rc + h * 1536); ae[h][1] = lds_tr(rd + h * 1536);
                }
                SB();
#pragma unroll
                for (int h = 0; h < 2; ++h) {
                    oa[h][0] = mfma32((u32x4){am[h][0].x, am[h][0].y, am[h][1].x, am[h][1].y}, fq[h], Z4);
                    oa[h][1] = mfma32((u32x4){am[h][2].x, am[h][2].y, am[h][3].x, am[h][3].y}, fq[h], Z4);
                }
#pragma unroll
                for (int h = 0; h < 2; ++h) {
                    const u32x4 fh = w1((gq >> 1) == h ? fe : 0u);
                    oa[h][1] = mfma32(w1(ae[h][1].x), fh, oa[h][1]); oa[h][0] = mfma32(w1(ae[h][0].x), fh, oa[h][0]);
                }
            }
            const float ia = rcp(oa[0][1][2] + 1e-6f), ib = rcp(oa[1][1][2] + 1e-6f);
            const unsigned ob = obase + (g * 8 +
        u32x4 wox[18];
        if (wave >= 6) {
            const unsigned wb = WS_WOI + ((ISB ? 36 : 0) + 9 * cg + 8) * TKT;
#pragma unroll
            for (int s = 0; s < 18; ++s) wox[s] = ld128c(wsr, l16 + s * 1024, wb);
            if (ISB && it == 0 && wave == 7) wait_all256(flag_at(P.ws, FL_W4, 0), lane, tag);
        } else {
            const unsigned ob = obase + (g * 8 + 2 * tp) * TKT + w1k;
            u32x4 ov[6];
            auto oload = [&]() {
#pragma unroll
                for (int i = 0; i < 6; ++i) ov[i] = ld128c(wsr, l16 + i * 6144, ob);
            };
            __builtin_amdgcn_s_sleep(OSLEEP);
            oload();
            RETRY(oload(), umax4(umax4(umax4(umax4(umax4(umax4(0u, ov[0]), ov[1]), ov[2]), ov[3]), ov[4]), ov[5]));
#pragma unroll
            for (int i = 0; i < 6; ++i) {
                if ((i == 2 || i == 5) && wave >= 4) {
                    const unsigned d = L_OST + w1k + i * 6144 + (lane >> 4) * 256 + (lane & 3) * 64 + (lane & 12);
                    lds_w32(d, ov[i].x); lds_w32(d + 16, ov[i].y); lds_w32(d + 32, ov[i].z); lds_w32(d + 48, ov[i].w);
                } else lds_w128(L_OST + w1k + i * 6144 + l16, ov[i]);
            }
        }
        if (first && blockIdx.x == 0 && wave == 0 && lane == 0) __hip_atomic_store((GAS unsigned*)P.ws, tag, RLX_AGENT);
        BARRIER();
        {
            f32x4 c0 = res[0], c1 = res[1], c2 = res[2];
            const unsigned lo = L_OST + l16;
            if (wave >= 6) {
                u32x4 ox[18];
#pragma unroll
                for (int s = 0; s < 18; ++s) ox[s] = lds_r128(lo + (wave & 1) * TKT + s * 1024);
                SB();
#pragma unroll
                for (int s = 0; s < 18; ++s) c2 = mfma32(wox[s], ox[s], c2);
            }
            SB();
            u32x4 of[4][2];
#pragma unroll
            for (int s = 0; s < 3; ++s) { of[s][0] = lds_r128(lo + s * 1024); of[s][1] = lds_r128(lo + TKT + s * 1024); }
#pragma unroll
            for (int s = 0; s < 18; ++s) {
                SB();
                c0 = mfma32(wof[s], of[s & 3][0], c0);
                SB();
                if (s + 3 < 18) of[(s + 3) & 3][0] = lds_r128(lo + (s + 3) * 1024);
                SB();
                c1 = mfma32(wof[s], of[s & 3][1], c1);
                SB();
                if (s + 3 < 18) of[(s + 3) & 3][1] = lds_r128(lo + TKT + (s + 3) * 1024);
                if (ISB) { if ((s & 1) == 0 && s < 10 && !last && (s < 8 || wave < 4)) dma1k(nimg + (54 + 4 * s) * 1024, l16, w1k + (54 + 4 * s) * 1024); }
                else if (s < 12 && (s < 11 || wave < 2)) dma1k(nimg + 8 * s * 1024, l16, w1k + 8 * s * 1024);
            }
            res[0] = c0; res[1] = c1; res[2] = c2;
            const u32x4 v = unit_pair(pk2(c0[0], c0[1]), pk2(c0[2], c0[3]), pk2(c1[0], c1[1]), pk2(c1[2], c1[3]));
            const u32x2 v2 = {pk2(c2[0], c2[1]), pk2(c2[2], c2[3])};
            const int ct = 9 * cg + wave, cx = 9 * cg + 8;
            const int tx = 2 * tp + (wave & 1);
            if (!last) {
                const unsigned xb = obase + ACT_BYTES + g * 8 * TKT, sb = obase + 2 * ACT_BYTES + g * 8 * 2304;
                const unsigned vo = (gq >> 1) * 256 + tk * 16;
                st128c(wsr, (gq & 1) * TKT + vo, xb + 2 * tp * TKT + ct * 512, ok4(v));
                const f32x2 a0 = {c0[0], c0[1]}, a1 = {c0[2], c0[3]}, b0 = {c1[0], c1[1]}, b1 = {c1[2], c1[3]};
                const f32x2 qa = fma2(a1, a1, a0 * a0), qb = fma2(b1, b1, b0 * b0);
                const f32x2 u = sw16(qa.x + qa.y, qb.x + qb.y); const float u1 = u.x + u.y; const f32x2 u2 = sw32(u1, u1);
                if (gq < 2) st32c(wsr, gq * 2304 + tk * 4, sb + 2 * tp * 2304 + ct * 64, ok1(u_of(u2.x + u2.y)));
                if (wave >= 6) {
                    st64c(wsr, vo + (gq & 1) * 8, xb + tx * TKT + cx * 512, ok2(v2));
                    const f32x2 d0 = {c2[0], c2[1]}, d1 = {c2[2], c2[3]}, qd = fma2(d1, d1, d0 * d0);
                    const float e0 = qd.x + qd.y; const f32x2 e1 = sw16(e0, e0); const float e2 = e1.x + e1.y; const f32x2 e3 = sw32(e2, e2);
                    if (gq == 0) st32c(wsr, tk * 4, sb + tx * 2304 + cx * 64, ok1(u_of(e3.x + e3.y)));
                }

            } else {
                const int tt = 2 * tp + (gq & 1);
                *(u32x4*)(P.out + (size_t)(g * 128 + tt * 16 + tk) * DM + 16 * ct + 8 * (gq >> 1)) = v;
                if (wave >= 6) *(u32x2*)(P.out + (size_t)(g * 128 + tx * 16 + tk) * DM + 16 * cx + 4 * gq) = v2;
            }
        }
if (!last) {
                const unsigned xb = obase + ACT_BYTES + g * 8 * TKT, sb = obase + 2 * ACT_BYTES + g * 8 * 2304;
                const unsigned vo = (gq >> 1) * 256 + tk * 16;
                st128c(wsr, (gq & 1) * TKT + vo, xb + 2 * tp * TKT + ct * 512, ok4(v));
                const f32x2 a0 = {c0[0], c0[1]}, a1 = {c0[2], c0[3]}, b0 = {c1[0], c1[1]}, b1 = {c1[2], c1[3]};
                const f32x2 qa = fma2(a1, a1, a0 * a0), qb = fma2(b1, b1, b0 * b0);
                const f32x2 u = sw16(qa.x + qa.y, qb.x + qb.y); const float u1 = u.x + u.y; const f32x2 u2 = sw32(u1, u1);
                if (gq < 2) st32c(wsr, gq * 2304 + tk * 4, sb + 2 * tp * 2304 + ct * 64, ok1(u_of(u2.x + u2.y)));
                if (wave < 2) {
                    st64c(wsr, vo + (gq & 1) * 8, xb + tx * TKT + cx * 512, ok2(v2));
                    const f32x2 d0 = {c2[0], c2[1]}, d1 = {c2[2], c2[3]}, qd = fma2(d1, d1, d0 * d0);
                    const float e0 = qd.x + qd.y; const f32x2 e1 = sw16(e0, e0); const float e2 = e1.x + e1.y; const f32x2 e3 = sw32(e2, e2);
                    if (gq == 0) st32c(wsr, tk * 4, sb + tx * 2304 + cx * 64, ok1(u_of(e3.x + e3.y)));
                }

            } else {
                const int tt = 2 * tp + (gq & 1);
                *(u32x4*)(P.out + (size_t)(g * 128 + tt * 16 + tk) * DM + 16 * ct + 8 * (gq >> 1)) = v;
                if (wave < 2) *(u32x2*)(P.out + (size_t)(g * 128 + tx * 16 + tk) * DM + 16 * cx + 4 * gq) = v2;
            }
        }
    }
    if (ISB) {
#pragma unroll
        for (int s = 0; s < 18; ++s) asm volatile("" : "=v"(xf[s]));
#pragma unroll
        for (int t = 0; t < 5; ++t) asm volatile("" : "=v"(acc[t]));
        asm volatile("" : "=v"(ssq));
    }
}

__global__ void __launch_bounds__(512, 2) fused_kernel(Params P) {
    const int tid = threadIdx.x, lane = tid & 63, wave = __builtin_amdgcn_readfirstlane(tid >> 6);
    const int b = blockIdx.x, g = ((b >> 1) & 3) * 4 + (b >> 6), p = (b & 1) * 8 + ((b >> 3) & 7);
    const rsrc_t wsr = mk_rsrc(P.ws, WS_END);
    if (lane < 3) {
        unsigned long long pc; unsigned keep;
        asm volatile("s_getpc_b64 %0" : "=s"(pc));
        const unsigned o = (((b >> 3) * 8 + wave) * 3 + lane) * 64;
        asm volatile("s_mov_b32 %0, m0\n\ts_mov_b32 m0, %3\n\ts_nop 0\n\tglobal_load_lds_dword %1, %2\n\ts_mov_b32 m0, %0" : "=&s"(keep) : "v"(o < CODE_BYTES ? o : 0u), "s"(pc), "s"(L_CS + 64 + wave * 16) : "memory");
    }
    u32x4 xf[18];
    f32x4 res[3];
    f32x4 acc[7], sq;
    float ssq;
    Ctx C; C.wave = wave; C.g = g; C.p = p;
    {
        const int gq = lane >> 4, tk = lane & 15;
        const f32x4 Z4 = {0.f, 0.f, 0.f, 0.f};
        const unsigned epv = __hip_atomic_load((const GAS unsigned*)P.ws, RLX_AGENT);
        const u16* xs = P.x + (size_t)(g * 128) * DM;
        u32x4 raw[8];
        unsigned ph0, ph1;
        const unsigned c0 = (unsigned)(gq == 1 ? 8 : gq == 2 ? 10 : 0);
        const unsigned wv = ((unsigned)tk * NQKV + c0) * 2u, wo = p * 36 + wave * 576;
        if (wave >= 4) {
            const unsigned xo = ((unsigned)(lane >> 3) * DM + 8u * ((lane & 7) ^ (lane >> 3))) * 2u;
            __builtin_amdgcn_s_sleep(DSLEEP);
#pragma unroll 1
            for (int j = 0; j < 4; ++j) {
                const unsigned oc = (wave - 4 + 4 * j + 2 * p) & 15;
                const u16* src = xs + oc * 8 * DM;
                unsigned dst = (oc >> 1) * TKT + (oc & 1) * 1024, keep, vo = xo;
                asm volatile("s_mov_b32 %0, m0\n\t"
                             ".rept 9\n\ts_mov_b32 m0, %1\n\ts_add_u32 %1, %1, 0x800\n\tglobal_load_lds_dwordx4 %2, %3\n\tv_add_u32 %2, 0x80, %2\n\t.endr\n\t"
                             "s_mov_b32 m0, %0" : "=&s"(keep), "+s"(dst), "+v"(vo) : "s"(src) : "memory", "scc");
            }
        }
        SB();
        u32x4 im, ix, lraw[8];
        unsigned wdst = 0, km[2], kx;
        auto unit_tile = [&](const u32x4* raw, const u32x4 (&bm)[2], const u32x4 (&bl)[2], unsigned pb) {
            u32x2 lf[4];
#pragma unroll
            for (int i = 0; i < 4; ++i) {
                const f32x4 cm = mfma32(raw[i], bm[i >> 1], Z4), cl = mfma32(raw[i], bl[i >> 1], Z4);
                lds_w64(km[i >> 1] + pb + (i & 1) * 512, (u32x2){pk2(cm[0], cm[1]), pk2(cm[2], cm[3])});
                lf[i] = (u32x2){pk2(cl[0], cl[1]), pk2(cl[2], cl[3])};
            }
            if (tk < 2) {
#pragma unroll
                for (int i = 0; i < 4; ++i) lds_w64(kx + pb + (i >> 1) * 128 + (i & 1) * 512, lf[i]);
            }
        };
        auto meet = [&](unsigned word, unsigned val) {
            asm volatile("s_waitcnt lgkmcnt(0)" ::: "memory");
            if (lane == 0) lds_w32(word + wave * 4, val);
#pragma unroll 1
            for (unsigned sp = 0; sp < SPIN_CAP; ++sp) { asm volatile("" ::: "memory"); if (__all(lds_r32(word + (lane < 3 ? lane : 0) * 4) == val)) break; }
        };
        if (wave < 3) {
#pragma unroll
            for (int i = 0; i < 4; ++i) ld128_raw(raw[i], P.wqkvA + wo + g * (32 * NQKV) + (i & 1) * (16 * NQKV) + (i >> 1) * 18, wv);
#pragma unroll
            for (int i = 0; i < 4; ++i) ld128_raw(raw[4 + i], P.wqkvA + wo + (16 + (g & 1)) * (32 * NQKV) + (i & 1) * (16 * NQKV) + (i >> 1) * 18, g < 2 ? wv : 0u);
            asm volatile("global_load_dword %0, %2, %4\n\tglobal_load_dword %1, %3, %4" : "=&v"(ph0), "=&v"(ph1) : "v"(tid * 4u), "v"((tid < 132 ? tid + 192 : 0) * 4u), "s"(P.wphi + p * 648) : "memory");
            SB();
            { const int l2 = fresh(lane); im = onehot8(((l2 >> 3) & 1) == (l2 >> 4) ? (l2 & 7) : -2); ix = onehot8((l2 >> 4) == 2 ? 6 + (l2 & 1) : -2); }
            constexpr unsigned L_PHI = L_PST + 7168;
            unsigned pa[2];
#pragma unroll
            for (int i = 0; i < 2; ++i) {
                const int z = tid + 192 * i, row = z / 7, j = z - 7 * row;
                if (z < 252) lds_w32(L_PHI + row * 64 + (j < 3 ? 32 + 4 * j : 36 + 4 * j), 0u);
                const int e = 2 * z, hh = e >= 324, r = e - 324 * hh, d = r / 18, f = r - 18 * d;
                pa[i] = L_PHI + (unsigned)(hh * 18 + f) * 64u + (unsigned)(d < 16 ? d : d + 6) * 2u;
            }
            const unsigned sb = wave == 0 ? 19 : wave == 2 ? 9 : 0, sl = sb + 8;
            const unsigned ko = L_PST + (gq >> 1) * 256 + (gq & 1) * 8;
#pragma unroll
            for (int h = 0; h < 2; ++h) { const unsigned sm = sb + 4 * h + (tk & 3); km[h] = ko + (tk >> 2) * 64 + (sm >> 2) * 1024 + (sm & 3) * 16; }
            kx = ko + (tk & 1) * 64 + (sl >> 2) * 1024 + (sl & 3) * 16;
            const unsigned onea = L_PST + 4096 + (tk >> 2) * 256 + (4 * (tk & 3) + 2) * 16;
            const u32x4 one4 = {0x3F803F80u, 0x3F803F80u, 0x3F803F80u, 0x3F803F80u};
            if (wave == 2 && lane < 16) lds_w128(onea, one4);
            asm volatile("s_waitcnt vmcnt(0)" : "+v"(ph0), "+v"(ph1), "+v"(raw[0]), "+v"(raw[1]), "+v"(raw[2]), "+v"(raw[3]), "+v"(raw[4]), "+v"(raw[5]), "+v"(raw[6]), "+v"(raw[7]) :: "memory");
            lds_w16(pa[0], ph0); lds_w16(pa[0] + 64, ph0 >> 16);
            if (tid < 132) { lds_w16(pa[1], ph1); lds_w16(pa[1] + 64, ph1 >> 16); }
            const unsigned tag = __builtin_amdgcn_readfirstlane(epv) * 0x9E3779B1u + 0x7F4A7C15u;
            meet(L_CS + 32, tag);
            u32x4 bm[2] = {im, im}, bl[2] = {ix, ix};
            if (wave < 2) {
#pragma unroll
                for (int h = 0; h < 2; ++h) { bm[h] = lds_r128(L_PHI + (h * 18 + tk) * 64 + gq * 16); bl[h] = lds_r128(L_PHI + (h * 18 + 16 + (tk & 1)) * 64 + gq * 16); }
            }
            unit_tile(raw, bm, bl, 0);
            meet(L_CS, tag);
            if (wave == 2 && lane < 16) lds_w128(onea + 7168, one4);
            if (g < 2) { unit_tile(raw + 4, bm, bl, 7168); meet(L_CS + 48, tag); }
            const unsigned o = WS_IMA + p * IMG_BYTES + wave * TKT, la = L_PST + wave * 1024 + lane * 16;
#pragma unroll
            for (int u = 0; u < 2; ++u) {
                if (u == 0 || g < 2) {
                    const unsigned oo = o + (u ? 16 + g : g) * 1024;
                    const unsigned ks = p * 18 + (u ? 16 + g : g);
                    put_frag<false>(wsr, oo, ks, wave, lds_r128(la + u * 7168), lane); put_frag<false>(wsr, oo + 3 * TKT, ks, wave + 3, lds_r128(la + u * 7168 + 3072), lane);
                    if (wave == 0) put_frag<false>(wsr, oo + 6 * TKT, ks, 6, lds_r128(la + u * 7168 + 6144), lane);
                }
            }
            DRAIN();
            if (lane == 0) set_flag(P.ws, FL_IA, p * 64 + g * 3 + wave, tag);
            const rsrc_t rw = mk_rsrc(P.wqkvB, DM * NQKV * 2);
#pragma unroll
            for (int i = 0; i < 8; ++i) lraw[i] = __builtin_amdgcn_raw_buffer_load_b128(rw, (i < 4 || (g >> 1) == 1) ? wv : 0x7fffffffu,
                                                      (wo + (i < 4 ? g : 16 + (g & 1)) * (32 * NQKV) + (i & 1) * (16 * NQKV) + ((i >> 1) & 1) * 18) * 2, 0);
        } else {
            { const int l2 = fresh(lane); im = onehot8(((l2 >> 3) & 1) == (l2 >> 4) ? (l2 & 7) : -2); ix = onehot8(((l2 >> 3) & 1) + 2 == (l2 >> 4) ? (l2 & 7) : -2); }
#pragma unroll
            for (int i = 2; i < 8; ++i) lraw[i] = im;
            const int u = b + 256 * (wave - 3), m = u >= 324, r = u - 324 * m, cp = r / 18, s = r - 18 * cp;
            const unsigned w0 = (unsigned)(wo_krow(m, s, tk >> 2, tk & 3) * DM + 8 * gq) * 2u, w1o = (unsigned)(wo_krow(m, s, tk >> 2, 4 + (tk & 3)) * DM + 8 * gq) * 2u;
            const rsrc_t rw = mk_rsrc(m ? P.woB : P.woA, u < 648 ? DM * DM * 2 : 0);
            asm volatile("" :: "v"(epv));
            lraw[0] = __builtin_amdgcn_raw_buffer_load_b128(rw, w0, 64 * cp, 0); lraw[1] = __builtin_amdgcn_raw_buffer_load_b128(rw, w1o, 64 * cp, 0);
            wdst = u < 648 ? WS_WOI + (unsigned)((m * 36 + 2 * cp) * 18 + s) * 1024u : 0u;
            asm volatile("s_waitcnt vmcnt(2)" ::: "memory");
        }
        C.tag = __builtin_amdgcn_readfirstlane(epv) * 0x9E3779B1u + 0x7F4A7C15u;
        BARRIER();
        {
            const unsigned xa = (tk >> 3) * 1024 + (tk & 7) * 128;
#pragma unroll
            for (int s = 0; s < 18; ++s) xf[s] = lds_r128(wave * TKT + (s >> 1) * 2048 + xa + (((4 * (s & 1) + gq) ^ (tk & 7)) * 16));
            const int tp = (p >> 1) & 3, cg = (p >> 3) * 2 + (p & 1);
#pragma unroll
            for (int i = 0; i < 3; ++i) {
                const int tt = 2 * tp + (i < 2 ? i : (wave & 1)), ct = 9 * cg + (i < 2 ? wave : 8);
                const u32x2 r = *(const LAS u32x2*)((LAS unsigned char*)smem + tt * TKT + (ct >> 2) * 2048 + xa + (((2 * (ct & 3) + (gq >> 1)) ^ (tk & 7)) * 16) + (gq & 1) * 8);
                res[i] = (f32x4){f_of(r.x << 16), f_of(r.x & 0xffff0000u), f_of(r.y << 16), f_of(r.y & 0xffff0000u)};
            }
        }
        if (wave == 3) wait_flags(flag_at(P.ws, FL_IA, p * 64), (lane < 48 ? lane : 0) * 128, C.tag);
        BARRIER();
        const unsigned char* img = P.ws + WS_IMA + p * IMG_BYTES;
        unsigned fo[4];
#pragma unroll
        for (int i = 0; i < 4; ++i) { const int j = i * 8 + wave, jt = j / 6; fo[i] = (j < 30 ? jt * 12 + j : 60 + j) * 1024; }
        const unsigned st3 = wave < 6 ? 6144 : 2048;
        auto piece = [&](int c, int i) {
            unsigned f = c < 3 ? fo[i] + c * (i < 3 ? 6144 : st3) : (96 + i * 8 + wave) * 1024;
            if (c == 3 && i == 3) f = f < 125 * 1024 ? f : 125 * 1024;
            dma1k(img + f, lane * 16, f);
        };
#pragma unroll
        for (int i = 0; i < 8; ++i) asm volatile("" :: "v"(lraw[i]));
#pragma unroll
        for (int i = 0; i < 8; ++i) piece(i >> 2, i & 3);
        if (wave < 3) {
            const u32x4 bm[2] = {im, im}, bl[2] = {ix, ix};
            unit_tile(lraw, bm, bl, 0);
            if ((g >> 1) == 1) unit_tile(lraw + 4, bm, bl, 7168);
        } else {
            wdst = __builtin_amdgcn_readfirstlane(wdst);
            if (wdst) {
                const f32x4 a0 = mfma32(lraw[0], im, Z4), a1 = mfma32(lraw[1], im, Z4), c0 = mfma32(lraw[0], ix, Z4), c1 = mfma32(lraw[1], ix, Z4);
                st128c(wsr, lane * 16, wdst, (u32x4){pk2(a0[0], a0[1]), pk2(a0[2], a0[3]), pk2(a1[0], a1[1]), pk2(a1[2], a1[3])});
                st128c(wsr, lane * 16, wdst + TKT, (u32x4){pk2(c0[0], c0[1]), pk2(c0[2], c0[3]), pk2(c1[0], c1[1]), pk2(c1[2], c1[3])});
            }
            if (wave >= 5) arm_unit(wsr, lane * 16, wave - 5, b);
        }
#pragma unroll
        for (int t = 0; t < 7; ++t) acc[t] = Z4;
        sq = Z4;
        const unsigned lb0[3] = {(unsigned)lane * 16, (unsigned)lane * 16 + 6144, (unsigned)lane * 16 + 12288};
        if (wave < 3) asm volatile("s_waitcnt vmcnt(4)" ::: "memory"); else if (wave == 5) asm volatile("s_waitcnt vmcnt(5)" ::: "memory");
        else if (wave < 5) asm volatile("s_waitcnt vmcnt(6)" ::: "memory"); else asm volatile("s_waitcnt vmcnt(5)" ::: "memory");
        BARRIER();
        gemm_img<0, 5, 0, 6, true, 2>(acc, xf, lb0, sq, [&](int s) {
            if (s < 2 && wave < 7 && (s == 0 || (g >> 1) == 1)) put_frag<true>(wsr, WS_IMB + p * IMG_BYTES + wave * TKT + (s ? 14 + g : g) * 1024, (16 + p) * 18 + (s ? 14 + g : g), wave, lds_r128(L_PST + s * 7168 + wave * 1024 + lane * 16), lane);
            if (s >= 2) piece(2, s - 2); });
        asm volatile("s_waitcnt vmcnt(4)" ::: "memory"); BARRIER();
        if (tid == 0) set_flag(P.ws, FL_W1, g * 16 + p, C.tag);
        gemm_img<0, 5, 6, 12, true, 2>(acc, xf, lb0, sq, [&](int s) { if (s < 10) piece(3, s - 6); if (s >= 10) arm_unit(wsr, lane * 16, 3 + (s - 10) * 8 + wave, b); });
        asm volatile("s_waitcnt vmcnt(6)" ::: "memory"); BARRIER();
        gemm_img<0, 5, 12, 18, true, 2>(acc, xf, lb0, sq, [&](int s) { if (s < 17 || wave < 4) arm_unit(wsr, lane * 16, 19 + (s - 12) * 8 + wave, b); });
        asm volatile("s_nop 7" : "+v"(sq));
        const float d0 = (tk & 1) ? sq[1] : sq[0], d1 = (tk & 1) ? sq[3] : sq[2], dg = (tk & 2) ? d1 : d0;
        ssq = gq == (tk >> 2) ? dg : 0.f;
        if (wave < 4) colsum_tile(wave, L_CS, lane);
        if (wave == 0) colsum_tile(4, L_CS, lane);
        BARRIER();
    }
#pragma unroll 1
    for (int it = 0; it < 2; ++it) {
        layer_fn<false>(P, wsr, lane, C, it, xf, res, acc, ssq);
        layer_fn<true>(P, wsr, lane, C, it, xf, res, acc, ssq);
    }
}

extern "C" void kernel_launch(void* const* d_in, const int* in_sizes, int n_in, void* d_out, int out_size, void* d_ws, size_t ws_size, hipStream_t stream) {
    Params P;
    P.x = (const u16*)d_in[0]; P.wqkvA = (const u16*)d_in[1]; P.wphi = (const u16*)d_in[2]; P.woA = (const u16*)d_in[3];
    P.wqkvB = (const u16*)d_in[5]; P.woB = (const u16*)d_in[6];
    P.out = (u16*)d_out; P.ws = (unsigned char*)d_ws;
    (void)hipFuncSetAttribute((const void*)fused_kernel, hipFuncAttributeMaxDynamicSharedMemorySize, LDS_BYTES);
    hipLaunchKernelGGL(fused_kernel, dim3(256), dim3(512), LDS_BYTES, stream, P);
}
```

```cpp
#include <hip/hip_runtime.h>
#include <stdint.h>

typedef __attribute__((ext_vector_type(8))) __bf16 bf16x8;
typedef __attribute__((ext_vector_type(2))) __bf16 bf16x2;
typedef __attribute__((ext_vector_type(4))) short s16x4;
typedef __attribute__((ext_vector_type(4))) float f32x4;
typedef __attribute__((ext_vector_type(2))) float f32x2;
typedef __attribute__((ext_vector_type(16))) float f32x16;
typedef __attribute__((ext_vector_type(4))) unsigned u32x4;
typedef __attribute__((ext_vector_type(2))) unsigned u32x2;
typedef unsigned short u16;
#define LAS __attribute__((address_space(3)))
#define GAS __attribute__((address_space(1)))
#define RLX_AGENT __ATOMIC_RELAXED, __HIP_MEMORY_SCOPE_AGENT
#define DEVI __device__ __forceinline__

constexpr int SEQ = 2048, DM = 576, NQKV = 1728;
constexpr unsigned TKT = 18 * 1024;
constexpr unsigned ACT_BYTES = 128 * TKT;
constexpr unsigned IMG_BYTES = 7 * TKT;
constexpr unsigned WS_FLAGS = 4096;
constexpr unsigned FL_W1 = 0, FL_W2 = 1, FL_W4 = 2, FL_W5 = 3, FL_IA = 4, FL_N = 8;
constexpr unsigned KVP_BYTES = 256 * 3072, KVH_BYTES = 256 * 20480, ST_BYTES = 128 * 2304, UNIT = 256 * 1024;
constexpr unsigned SETA = KVP_BYTES + 2 * ACT_BYTES + ST_BYTES;
constexpr unsigned N0 = 23, N1 = 40, N2 = 52;
constexpr unsigned WS_S0 = 524288, WS_S1 = WS_S0 + N0 * UNIT, WS_S2 = WS_S1 + N1 * UNIT;
static_assert(WS_FLAGS + FL_N * 256 * 128 <= WS_S0 && SETA <= N0 * UNIT && KVH_BYTES + 2 * ACT_BYTES + ST_BYTES <= N1 * UNIT && SETA + KVH_BYTES + ACT_BYTES <= N2 * UNIT, "map");
__host__ __device__ constexpr unsigned ws_k(int layer) { return layer == 0 ? WS_S0 : layer == 1 ? WS_S1 : layer == 2 ? WS_S2 : WS_S2 + SETA; }
__host__ __device__ constexpr unsigned ws_o(int layer) { return ws_k(layer) + ((layer & 1) ? KVH_BYTES : KVP_BYTES); }
constexpr unsigned WS_IMA = WS_S2 + N2 * UNIT;
constexpr unsigned WS_IMB = WS_IMA + 16 * IMG_BYTES;
constexpr unsigned WS_WOI = WS_IMB + 16 * IMG_BYTES;
constexpr unsigned WS_CSP = WS_WOI + 2 * 36 * TKT;
constexpr unsigned WS_END = WS_CSP + 2 * 16 * 18 * 448;
constexpr unsigned L_OST = 5 * TKT;
constexpr unsigned L_STG = IMG_BYTES;
constexpr unsigned L_KT = L_STG - 40960;
constexpr unsigned L_QS = L_STG + 20480;
constexpr unsigned L_PK = L_STG, L_VV = L_STG + 20480;
constexpr unsigned L_KVS = 0;
constexpr unsigned L_CS = L_STG + 32768;
constexpr unsigned L_PST = 147456;
constexpr unsigned LDS_BYTES = 163840;
constexpr unsigned CODE_BYTES = 42 * 1024;
constexpr unsigned SPIN_CAP = 1u << 17;
constexpr int DSLEEP = 16, OSLEEP = 10, XSLEEP = 10, RSLEEP = 6;
constexpr float LOG2E = 1.4426950408889634f;
constexpr float NEGBIG = -1e30f;

struct Params {
    const u16* x; const u16* wqkvA; const u16* wphi; const u16* woA; const u16* wqkvB; const u16* woB;
    u16* out; unsigned char* ws;
};

DEVI f32x4 mfma32(u32x4 a, u32x4 b, f32x4 c) { return __builtin_amdgcn_mfma_f32_16x16x32_bf16(__builtin_bit_cast(bf16x8, a), __builtin_bit_cast(bf16x8, b), c, 0, 0, 0); }
DEVI f32x16 mfmaL(u32x4 a, u32x4 b, f32x16 c) { return __builtin_amdgcn_mfma_f32_32x32x16_bf16(__builtin_bit_cast(bf16x8, a), __builtin_bit_cast(bf16x8, b), c, 0, 0, 0); }
DEVI u32x4 w1(unsigned a) { return (u32x4){a, 0u, 0u, 0u}; }
DEVI unsigned pkv(f32x2 f) { return __builtin_bit_cast(unsigned, __builtin_convertvector(f, bf16x2)); }
DEVI unsigned pk2(float lo, float hi) { return pkv((f32x2){lo, hi}); }
DEVI float dot2(unsigned a, unsigned b, float c) { return __builtin_amdgcn_fdot2_f32_bf16(__builtin_bit_cast(bf16x2, a), __builtin_bit_cast(bf16x2, b), c, false); }
DEVI float ex2(float x) { return __builtin_amdgcn_exp2f(x); }
DEVI float rcp(float x) { return __builtin_amdgcn_rcpf(x); }
DEVI float f_of(unsigned u) { return __builtin_bit_cast(float, u); }
DEVI unsigned u_of(float f) { return __builtin_bit_cast(unsigned, f); }
DEVI f32x2 fma2(f32x2 a, f32x2 b, f32x2 c) { return __builtin_elementwise_fma(a, b, c); }
DEVI f32x2 sp2(float v) { return (f32x2){v, v}; }
DEVI f32x2 sw16(float a, float b) { const auto r = __builtin_amdgcn_permlane16_swap(u_of(a), u_of(b), false, false); return (f32x2){f_of(r[0]), f_of(r[1])}; }
DEVI f32x2 sw32(float a, float b) { const auto r = __builtin_amdgcn_permlane32_swap(u_of(a), u_of(b), false, false); return (f32x2){f_of(r[0]), f_of(r[1])}; }
DEVI float vmax(f32x2 v) { float r; asm("v_max_f32 %0, %1, %2" : "=v"(r) : "v"(v.x), "v"(v.y)); return r; }
DEVI float vmaxn(f32x2 v) { float r; asm("v_max_f32_e64 %0, -%1, -%2" : "=v"(r) : "v"(v.x), "v"(v.y)); return r; }
DEVI float max3f(float a, float b, float c) { return __builtin_fmaxf(__builtin_fmaxf(a, b), c); }
DEVI float min3f(float a, float b, float c) { return __builtin_fminf(__builtin_fminf(a, b), c); }
DEVI u32x4 unit_pair(unsigned a0, unsigned a1, unsigned b0, unsigned b1) {
    const auto s = __builtin_amdgcn_permlane16_swap(a0, b0, false, false);
    const auto t = __builtin_amdgcn_permlane16_swap(a1, b1, false, false);
    return (u32x4){s[0], t[0], s[1], t[1]};
}

#define BARRIER() asm volatile("s_waitcnt lgkmcnt(0)\n\ts_barrier" ::: "memory")
#define DRAIN() asm volatile("s_waitcnt vmcnt(0)" ::: "memory")
#define SB() __builtin_amdgcn_sched_barrier(0)

extern __shared__ __attribute__((aligned(16))) unsigned char smem[];
DEVI u32x4 lds_r128(unsigned off) { return *(const LAS u32x4*)((LAS unsigned char*)smem + off); }
DEVI f32x4 lds_rf4(unsigned off) { return *(const LAS f32x4*)((LAS unsigned char*)smem + off); }
DEVI unsigned lds_r32(unsigned off) { return *(const LAS unsigned*)((LAS unsigned char*)smem + off); }
DEVI void lds_w128(unsigned off, u32x4 v) { *(LAS u32x4*)((LAS unsigned char*)smem + off) = v; }
DEVI void lds_w64(unsigned off, u32x2 v) { *(LAS u32x2*)((LAS unsigned char*)smem + off) = v; }
DEVI void lds_w32(unsigned off, unsigned v) { *(LAS unsigned*)((LAS unsigned char*)smem + off) = v; }
DEVI void lds_w16(unsigned off, unsigned v) { *(LAS u16*)((LAS unsigned char*)smem + off) = (u16)v; }
DEVI u32x2 lds_tr(unsigned off) { return __builtin_bit_cast(u32x2, __builtin_amdgcn_ds_read_tr16_b64_v4i16((LAS s16x4*)((LAS unsigned char*)smem + off))); }

typedef __amdgpu_buffer_rsrc_t rsrc_t;
DEVI rsrc_t mk_rsrc(const void* p, unsigned bytes) { return __builtin_amdgcn_make_buffer_rsrc((void*)p, 0, bytes, 0x00020000); }
DEVI u32x4 ld128c(rsrc_t r, unsigned voff, unsigned soff) { return __builtin_amdgcn_raw_buffer_load_b128(r, voff, soff, 16); }
DEVI void st128c(rsrc_t r, unsigned voff, unsigned soff, u32x4 v) {
    __builtin_amdgcn_raw_buffer_store_b128(v, r, voff, soff, 16);
    asm volatile("s_nop 1" :: "v"(v) : "memory");
}
DEVI void st64c(rsrc_t r, unsigned voff, unsigned soff, u32x2 v) { __builtin_amdgcn_raw_buffer_store_b64(v, r, voff, soff, 16); }
DEVI void st32c(rsrc_t r, unsigned voff, unsigned soff, unsigned v) { __builtin_amdgcn_raw_buffer_store_b32(v, r, voff, soff, 16); }
DEVI void dma1k(const void* sbase, unsigned voff, unsigned lds_dst) {
    unsigned keep;
    asm volatile("s_mov_b32 %0, m0\n\ts_mov_b32 m0, %3\n\ts_nop 0\n\tglobal_load_lds_dwordx4 %1, %2 sc1\n\ts_mov_b32 m0, %0" : "=&s"(keep) : "v"(voff), "s"(sbase), "s"(lds_dst) : "memory");
}
DEVI void ld128_raw(u32x4& d, const void* base, unsigned voff) { asm volatile("global_load_dwordx4 %0, %1, %2" : "=v"(d) : "v"(voff), "s"(base) : "memory"); }
DEVI void wait_flags(const unsigned char* base, unsigned voff, unsigned tag) {
    const GAS unsigned* f = (const GAS unsigned*)(base + voff);
#pragma unroll 1
    for (unsigned sp = 0; sp < SPIN_CAP; ++sp) {
        const unsigned v = __hip_atomic_load(f, RLX_AGENT);
        if (__all(v == tag)) break;
        __builtin_amdgcn_s_sleep(1);
    }
    asm volatile("" ::: "memory");
}
DEVI unsigned umax3(unsigned a, unsigned b, unsigned c) { return __builtin_elementwise_max(__builtin_elementwise_max(a, b), c); }
DEVI unsigned umax4(unsigned m, u32x4 v) { return umax3(umax3(m, v.x, v.y), v.z, v.w); }
DEVI unsigned ok1(unsigned v) { return __builtin_elementwise_min(v, 0xFFFFFFFEu); }
DEVI u32x2 ok2(u32x2 v) { return (u32x2){ok1(v.x), ok1(v.y)}; }
DEVI u32x4 ok4(u32x4 v) { return (u32x4){ok1(v.x), ok1(v.y), ok1(v.z), ok1(v.w)}; }
DEVI void arm_unit(rsrc_t r, unsigned l16, unsigned unit, unsigned cu) { st128c(r, l16, WS_S0 + (unit * 256 + cu) * 1024, (u32x4){~0u, ~0u, ~0u, ~0u}); }
DEVI void wait_all256(const unsigned char* base, int lane, unsigned tag) {
    const GAS unsigned* f = (const GAS unsigned*)(base + lane * 128);
#pragma unroll 1
    for (unsigned sp = 0; sp < SPIN_CAP; ++sp) {
        const unsigned v0 = __hip_atomic_load(f, RLX_AGENT), v1 = __hip_atomic_load(f + 2048, RLX_AGENT), v2 = __hip_atomic_load(f + 4096, RLX_AGENT), v3 = __hip_atomic_load(f + 6144, RLX_AGENT);
        if (__all(v0 == tag && v1 == tag && v2 == tag && v3 == tag)) break;
        __builtin_amdgcn_s_sleep(1);
    }
    asm volatile("" ::: "memory");
}
DEVI u32x4 peek256(const unsigned char* base, int lane) {
    const GAS unsigned* f = (const GAS unsigned*)(base + lane * 128);
    return (u32x4){__hip_atomic_load(f, RLX_AGENT), __hip_atomic_load(f + 2048, RLX_AGENT), __hip_atomic_load(f + 4096, RLX_AGENT), __hip_atomic_load(f + 6144, RLX_AGENT)};
}
DEVI void judge256(u32x4 v, const unsigned char* base, int lane, unsigned tag) {
    if (!__all(v.x == tag && v.y == tag && v.z == tag && v.w == tag)) wait_all256(base, lane, tag);
    asm volatile("" ::: "memory");
}
#define RETRY(LOADS, MAXEXPR) do { _Pragma("unroll 1") for (unsigned sp_ = 0; sp_ < SPIN_CAP; ++sp_) { if (!__any((MAXEXPR) == 0xFFFFFFFFu)) break; \
    if (sp_ < 4) __builtin_amdgcn_s_sleep(RSLEEP); else __builtin_amdgcn_s_sleep(48); asm volatile("" ::: "memory"); LOADS; } } while (0)
DEVI const unsigned char* flag_at(const unsigned char* ws, unsigned kind, unsigned idx) { return ws + WS_FLAGS + (kind * 256 + idx) * 128; }
DEVI void set_flag(unsigned char* ws, unsigned kind, unsigned idx, unsigned tag) {
    __hip_atomic_store((GAS unsigned*)(ws + WS_FLAGS + (kind * 256 + idx) * 128), tag, RLX_AGENT);
}


DEVI u32x4 onehot8(int j) {
    const unsigned v = (j & 1) ? 0x3F800000u : 0x3F80u; const int w = j >> 1;
    return (u32x4){w == 0 ? v : 0u, w == 1 ? v : 0u, w == 2 ? v : 0u, w == 3 ? v : 0u};
}
DEVI int wo_krow(int mat, int s, int gq, int j) {
    const int a = 36 * s + (mat ? 18 * (gq >> 1) + 8 * (j >> 2) + 4 * (gq & 1) : 18 * (j >> 2) + 4 * gq) + (j & 3);
    const int c = 36 * (8 * (s - 16) + 2 * gq + (j >> 2)) + 18 * ((j >> 1) & 1) + 16 + (j & 1);
    return s < 16 ? a : c;
}
DEVI int fresh(int v) { asm volatile("" : "+v"(v)); return v; }
DEVI int sfresh(int v) { asm volatile("" : "+s"(v)); return v; }

template <int T0, int NT, int S0, int S1, bool SSQ, int D, class F>
DEVI void gemm_img(f32x4 (&acc)[7], u32x4 (&xf)[18], const unsigned (&lb)[3], f32x4& sq, F side) {
    u32x4 a[D][NT];
#pragma unroll
    for (int s = S0; s < S0 + D; ++s)
#pragma unroll
        for (int t = 0; t < NT; ++t) a[s % D][t] = lds_r128(lb[s / 6] + (T0 + t) * TKT + (s % 6) * 1024);
    SB();
#pragma unroll
    for (int s = S0; s < S1; ++s) {
        side(s);
        SB();
        if (SSQ) { sq = mfma32(xf[s], xf[s], sq); SB(); }
#pragma unroll
        for (int t = 0; t < NT; ++t) {
            acc[T0 + t] = mfma32(a[s % D][t], xf[s], acc[T0 + t]);
            SB();
            if (s + D < S1) { a[s % D][t] = lds_r128(lb[(s + D) / 6] + (T0 + t) * TKT + ((s + D) % 6) * 1024); SB(); }
        }
    }
}
template <bool SUM>
DEVI void put_frag(rsrc_t wsr, unsigned dst, unsigned ks, int t, u32x4 f, int lane) {
    st128c(wsr, lane * 16, dst, f);
    if (!SUM) return;
    f32x4 c = mfma32((u32x4){0x3F803F80u, 0x3F803F80u, 0x3F803F80u, 0x3F803F80u}, f, (f32x4){0.f, 0.f, 0.f, 0.f});
    asm volatile("s_nop 7" : "+v"(c));
    if (lane < 16) st32c(wsr, lane * 4, WS_CSP + ks * 448 + t * 64, u_of(c[0]));
}
DEVI void colsum_get(rsrc_t wsr, unsigned img_p, unsigned csb, int lane) {
    u32x2 v[18];
#pragma unroll
    for (int s = 0; s < 18; ++s) v[s] = __builtin_amdgcn_raw_buffer_load_b64(wsr, lane < 56 ? lane * 8 : 0, WS_CSP + (img_p * 18 + s) * 448, 16);
    SB();
    f32x2 c = __builtin_bit_cast(f32x2, v[0]);
#pragma unroll
    for (int s = 1; s < 18; ++s) c += __builtin_bit_cast(f32x2, v[s]);
    if (lane < 56) lds_w64(csb + ((lane >> 1) & 3) * 112 + (lane >> 3) * 16 + (lane & 1) * 8, __builtin_bit_cast(u32x2, c));
}
DEVI void colsum_tile(int t, unsigned csb, int lane) {
    f32x4 c = {0.f, 0.f, 0.f, 0.f}; const u32x4 ones = {0x3F803F80u, 0x3F803F80u, 0x3F803F80u, 0x3F803F80u};
    u32x4 a[18];
#pragma unroll
    for (int s = 0; s < 18; ++s) a[s] = lds_r128(t * TKT + s * 1024 + lane * 16);
    SB();
#pragma unroll
    for (int s = 0; s < 18; ++s) c = mfma32(a[s], ones, c);
    asm volatile("s_nop 7" : "+v"(c));
    if ((lane & 15) == 0) lds_w128(csb + ((lane >> 4) * 28 + 4 * t) * 4, __builtin_bit_cast(u32x4, c));
}
DEVI void dual_softmax(const float (&t)[9], float ks, bool lo, u32x4& fa, u32x4& fb, unsigned& fe) {
    const float zA = lo ? t[8] : t[0], zB = lo ? t[4] : t[8];
    const float mA = max3f(max3f(t[0], t[1], t[2]), t[3], zA), nA = min3f(min3f(t[0], t[1], t[2]), t[3], zA);
    const float mB = max3f(max3f(t[4], t[5], t[6]), t[7], zB), nB = min3f(min3f(t[4], t[5], t[6]), t[7], zB);
    const float T = -ks * vmax(sw32(vmax(sw16(mA, mB)), vmaxn(sw16(nA, nB))));
    const f32x2 u = sw16(T, T);
    const f32x2 sa = sw32(u.x, u.x), sb = sw32(u.y, u.y);
    const f32x2 s8 = lo ? sa : sb, k2 = {ks, -ks};
    f32x2 e[9];
#pragma unroll
    for (int i = 0; i < 9; ++i) { const f32x2 z = fma2(sp2(t[i]), k2, i < 4 ? sa : i < 8 ? sb : s8); e[i] = (f32x2){ex2(z.x), ex2(z.y)}; }
    const float wl = lo ? 1.f : 0.f;
    const f32x2 sA = fma2(e[8], sp2(wl), (e[0] + e[1]) + (e[2] + e[3])), sB = fma2(e[8], sp2(1.f - wl), (e[4] + e[5]) + (e[6] + e[7]));
    const f32x2 p = sw16(sA.x, sB.x), q = sw16(sA.y, sB.y), w = sw32(p.x + p.y, q.x + q.y);
    const float R = rcp(w.x + w.y);
    const f32x2 v = sw16(R, R);
    const f32x2 ra = sw32(v.x, v.x), rb = sw32(v.y, v.y), r8 = lo ? ra : rb;
    fa = (u32x4){pkv(e[0] * ra), pkv(e[1] * ra), pkv(e[2] * ra), pkv(e[3] * ra)};
    fb = (u32x4){pkv(e[4] * rb), pkv(e[5] * rb), pkv(e[6] * rb), pkv(e[7] * rb)};
    fe = pkv(e[8] * r8);
}

DEVI f32x16 att_scores(u32x4 km, u32x4 kl, u32x4 q0, u32x4 q1, f32x16 c) { return mfmaL(kl, q1, mfmaL(km, q0, c)); }
DEVI u32x4 att_p(const f32x16& s, int b) { return (u32x4){pk2(ex2(s[b]), ex2(s[b + 1])), pk2(ex2(s[b + 2]), ex2(s[b + 3])), pk2(ex2(s[b + 4]), ex2(s[b + 5])), pk2(ex2(s[b + 6]), ex2(s[b + 7]))}; }
DEVI void att_accum(const f32x16& s, const u32x2 (&v)[4], f32x16& o) {
    const u32x4 p0 = {pk2(ex2(s[0]), ex2(s[1])), pk2(ex2(s[2]), ex2(s[3])), pk2(ex2(s[4]), ex2(s[5])), pk2(ex2(s[6]), ex2(s[7]))};
    o = mfmaL((u32x4){v[0].x, v[0].y, v[1].x, v[1].y}, p0, o);
    const u32x4 p1 = {pk2(ex2(s[8]), ex2(s[9])), pk2(ex2(s[10]), ex2(s[11])), pk2(ex2(s[12]), ex2(s[13])), pk2(ex2(s[14]), ex2(s[15]))};
    o = mfmaL((u32x4){v[2].x, v[2].y, v[3].x, v[3].y}, p1, o);
}
DEVI void att_vload(u32x2 (&v)[4], unsigned va) {
#pragma unroll
    for (int e = 0; e < 4; ++e) v[e] = lds_tr(va + e * 384);
}
DEVI float max16(const f32x16& s, float m) {
#pragma unroll
    for (int v = 0; v < 16; v += 2) m = max3f(m, s[v], s[v + 1]);
    return m;
}
DEVI f32x16 splat16(float v) { f32x16 r;
#pragma unroll
    for (int i = 0; i < 16; ++i) r[i] = v;
    return r; }

struct Ctx { int wave, g, p; unsigned tag; };

template <bool ISB>
DEVI void layer_fn(const Params& P, const rsrc_t wsr, const int lane_in, const Ctx& C, const int it, u32x4 (&xf)[18], f32x4 (&res)[3], f32x4 (&acc)[7], float& ssq) {
    const int wave = sfresh(C.wave), g = sfresh(C.g), p = sfresh(C.p), cu = g * 16 + p;
    const unsigned tag = C.tag;
    const int layer = 2 * it + (ISB ? 1 : 0);
    const int tp = (p >> 1) & 3, cg = (p >> 3) * 2 + (p & 1);
    const f32x4 Z4 = {0.f, 0.f, 0.f, 0.f};
    constexpr unsigned csb = L_CS + (ISB ? 448 : 0);
    const bool first = !ISB && it == 0, last = ISB && it == 1;
    u32x4 wof[18];
    float mean, rstd;
    const unsigned wob = WS_WOI + ((ISB ? 36 : 0) + 9 * cg + wave) * TKT;
    const unsigned w1k = wave * 1024;
    const unsigned kbase = ISB ? (it ? ws_k(3) : ws_k(1)) : (it ? ws_k(2) : ws_k(0)), obase = kbase + (ISB ? KVH_BYTES : KVP_BYTES);
    const int rot = first ? 0 : p % 3;
    const unsigned ro0 = rot * 6144, ro1 = rot == 2 ? 0 : ro0 + 6144, ro2 = rot ? ro0 - 6144 : 12288;
    const unsigned char* nimg = P.ws + (ISB ? WS_IMA : WS_IMB) + p * IMG_BYTES + w1k;
    {
        const int lane = fresh(lane_in), gq = lane >> 4, tk = lane & 15;
        const unsigned l16 = lane * 16;
        const unsigned lb[3] = {l16 + ro0, l16 + ro1, l16 + ro2};
        if (!first) {
#pragma unroll
            for (int t = 0; t < 5; ++t) acc[t] = Z4;
            const unsigned xprev = (ISB ? (it ? ws_o(2) : ws_o(0)) : ws_o(1)) + ACT_BYTES;
            const unsigned xb = xprev + (g * 8 + wave) * TKT, sb = xprev + ACT_BYTES + (g * 8 + wave) * 2304;
            unsigned st[9];
            auto xload = [&](int s0, int s1) {
#pragma unroll
                for (int s = s0; s < s1; ++s) xf[s] = ld128c(wsr, l16 + (s % 6) * 1024, xb + (s < 6 ? ro0 : s < 12 ? ro1 : ro2));
            };
            auto xmax = [&](int s0, int s1) { unsigned m = 0u;
#pragma unroll
                for (int s = s0; s < s1; ++s) m = umax4(m, xf[s]);
                return m; };
            auto sload = [&]() {
#pragma unroll
                for (int j = 0; j < 9; ++j) st[j] = __builtin_amdgcn_raw_buffer_load_b32(wsr, lane * 4 + j * 256, sb, 16);
            };
            asm volatile("s_waitcnt vmcnt(2)" ::: "memory");
            BARRIER();
            __builtin_amdgcn_s_sleep(XSLEEP);
            sload(); xload(0, 3);
            RETRY({ sload(); xload(0, 3); }, umax3(umax3(umax3(umax3(umax3(xmax(0, 3), st[0], st[1]), st[2], st[3]), st[4], st[5]), st[6], st[7]), st[8], 0u));
            xload(3, 9);
            ssq = ((f_of(st[0]) + f_of(st[1])) + (f_of(st[2]) + f_of(st[3]))) + ((f_of(st[4]) + f_of(st[5])) + (f_of(st[6]) + f_of(st[7]))) + f_of(st[8]);
            const unsigned char* cimg = P.ws + (ISB ? WS_IMB : WS_IMA) + p * IMG_BYTES + w1k;
            f32x4 dummy;
            gemm_img<0, 5, 0, 18, false, 2>(acc, xf, lb, dummy, [&](int s) {
                if (s < 3) xload(9 + 2 * s, 11 + 2 * s);
                if (s == 3) RETRY(xload(3, 9), xmax(3, 9));
                if (s >= 3 && s < 6) xload(12 + s, 13 + s);
                if (s == 9) RETRY(xload(9, 18), xmax(9, 18));
                if (s >= 9 && s < 14 && (s < 13 || wave < 4)) dma1k(cimg + (90 + 8 * (s - 9)) * 1024, l16, w1k + (90 + 8 * (s - 9)) * 1024);
                if (!ISB && s >= 9) wof[s] = ld128c(wsr, l16 + s * 1024, wob);
                if (ISB && it == 0 && s >= 14 && s < 17) arm_unit(wsr, l16, 63 + (s - 14) * 8 + wave, cu);
                if (!ISB && s >= 14 && (s < 17 || wave < 4)) arm_unit(wsr, l16, 87 + (s - 14) * 8 + wave, cu);
            });
        }
        f32x4 cs[5];
#pragma unroll
        for (int t = 0; t < 5; ++t) cs[t] = lds_rf4(csb + gq * 112 + 16 * t);
        acc[5] = Z4; acc[6] = Z4;
        const f32x2 r1 = sw16(ssq, ssq); const float h1 = r1.x + r1.y; const f32x2 r2 = sw32(h1, h1);
        asm volatile("s_nop 7" : "+v"(acc[4]));
        mean = acc[4][2] * (1.f / DM);
        rstd = __builtin_amdgcn_rsqf(__builtin_fmaxf((r2.x + r2.y) * (1.f / DM) - mean * mean, 0.f) + 1e-5f);
        const int hx = gq >> 1, od = gq & 1;
        f32x2 y[9];
        {
            const f32x2 nm = sp2(-mean);
#pragma unroll
            for (int t = 0; t < 5; ++t) {
                y[2 * t] = fma2(nm, (f32x2){cs[t][0], cs[t][1]}, (f32x2){acc[t][0], acc[t][1]});
                if (t < 4) y[2 * t + 1] = fma2(nm, (f32x2){cs[t][2], cs[t][3]}, (f32x2){acc[t][2], acc[t][3]});
            }
        }
        const u32x4 vm = unit_pair(pk2(y[4].y * rstd, y[5].x * rstd), pk2(y[5].y * rstd, y[6].x * rstd), pk2(y[6].y * rstd, y[7].x * rstd), pk2(y[7].y * rstd, y[8].x * rstd));
        const float v8 = y[8].y * rstd;
        const unsigned vl = pkv(sw16(v8, v8));
        if (ISB) {
            const f32x2 r2 = sp2(rstd);
            const u32x4 km = unit_pair(pkv(y[0] * r2), pkv(y[1] * r2), pkv(y[2] * r2), pkv(y[3] * r2));
            const float k8 = y[4].x * rstd;
            const u32x4 lr = {ok1(pkv(sw16(k8, k8))), 0u, ok1(vl), 0x3F80u};
            const unsigned tb = (wave >> 1) * 5120 + (wave & 1) * 256, tc = (wave >> 1) * 5120 + (wave & 1) * 768 + 1024;
            const unsigned oc = (tk >> 3) * 384 + (tk & 7) * 16;
            const unsigned a0 = od * 2560 + hx * 512 + tk * 16, a1 = od * 2560 + hx * 128 + oc, a2 = hx * 2560 + 256 + (oc ^ 64);
            const unsigned hb = kbase + cu * 20480;
            st128c(wsr, a0, hb + tb, ok4(km)); st128c(wsr, a1, hb + tc, ok4(vm));
            lds_w128(L_STG + tb + a0, km); lds_w128(L_STG + tc + a1, vm);
            if (od == 0) { st128c(wsr, a2, hb + tc, lr); lds_w128(L_STG + tc + a2, lr); }
            asm volatile("s_waitcnt vmcnt(3)" ::: "memory");
            BARRIER();
            if (it == 0 && wave == 7 && lane == 0) set_flag(P.ws, FL_W4, cu, tag);
        } else {
            const float kz[9] = {y[0].x, y[0].y, y[1].x, y[1].y, y[2].x, y[2].y, y[3].x, y[3].y, y[4].x};
            u32x4 fa, fb; unsigned fe;
            dual_softmax(kz, rstd * LOG2E, gq < 2, fa, fb, fe);
            const unsigned pk = L_PK + wave * 1280 + tk * 80, vv = L_VV + wave * 768 + tk * 48;
            lds_w128(pk + gq * 16, fa); lds_w128(pk + gq * 16 + 10240, fb); lds_w32(pk + hx * 10240 + 64 + od * 4, fe);
            lds_w128(vv + od * 6144 + hx * 16, vm);
            lds_w64(vv + hx * 6144 + 32, (u32x2){vl, 0x3F80u});
            if (first && wave == 3) wait_all256(flag_at(P.ws, FL_W1, 0), lane, tag);
            DRAIN();
            BARRIER();
            if (wave == 7 && lane == 0) set_flag(P.ws, it ? FL_W5 : FL_W2, cu, tag);
            if (wave < 6) {
                const int h = wave >= 3, ft = wave - 3 * h;
                const unsigned trow = (unsigned)(lane >> 2), tcol = (unsigned)(lane & 3) * 8u;
                const unsigned pa = L_PK + h * 10240 + ft * 32 + trow * 80 + tcol, pb = L_VV + h * 6144 + trow * 48 + tcol;
                u32x2 ta[8], tb[8], td[8];
#pragma unroll
                for (int s = 0; s < 8; ++s) { ta[s] = lds_tr(pa + (16 * s) * 80); tb[s] = lds_tr(pb + (16 * s) * 48); td[s] = lds_tr(pb + (16 * s) * 48 + 32); }
                SB();
                f32x4 k0 = Z4, k1 = Z4;
#pragma unroll
                for (int s = 0; s < 4; ++s) {
                    const u32x4 af = {ta[2 * s].x, ta[2 * s].y, ta[2 * s + 1].x, ta[2 * s + 1].y};
                    k0 = mfma32((u32x4){tb[2 * s].x, tb[2 * s].y, tb[2 * s + 1].x, tb[2 * s + 1].y}, af, k0);
                    k1 = mfma32((u32x4){td[2 * s].x, td[2 * s].y, td[2 * s + 1].x, td[2 * s + 1].y}, af, k1);
                }
                const u32x2 s0 = ok2((u32x2){pk2(k0[0], k0[1]), pk2(k0[2], k0[3])}), s1 = ok2((u32x2){pk2(k1[0], k1[1]), pk2(k1[2], k1[3])});
                const unsigned kb = kbase + (p * 16 + g) * 3072 + h * 1536;
                if (ft < 2) {
                    st64c(wsr, lane * 8, kb + ft * 512, s0);
                    if (gq == 0) st64c(wsr, lane * 8, kb + 1024 + ft * 128, s1);
                } else if (tk < 4) {
                    st64c(wsr, gq * 32 + tk * 8, kb + 1280, s0);
                    if (gq == 0) st64c(wsr, tk * 8, kb + 1408, s1);
                }
            }
        }
        if (first && wave >= 6) colsum_tile(wave - 1, csb, lane);
    }
    {
        const int lane = fresh(lane_in), gq = lane >> 4, tk = lane & 15;
        const unsigned l16 = lane * 16;
        const unsigned lb[3] = {l16 + ro0, l16 + ro1, l16 + ro2};
        f32x16 msk;
        const int dl = (lane & 31) - 4 * (lane >> 5);
        { f32x4 dummy; gemm_img<5, 2, 0, 18, false, 3>(acc, xf, lb, dummy, [&](int s) {
              if (!ISB && (s & 1) == 0) wof[s >> 1] = ld128c(wsr, l16 + (s >> 1) * 1024, wob);
              if (ISB && s < 16) msk[s] = ((s & 3) + 8 * (s >> 2) > dl) ? NEGBIG : 0.f;
              if ((s & 1) && first) wof[9 + (s >> 1)] = ld128c(wsr, l16 + (9 + (s >> 1)) * 1024, wob); }); }
        if (!ISB) BARRIER();
        float q[9];
        {
            const unsigned ca = csb + gq * 112;
            const float c19 = f_of(lds_r32(ca + 76));
            const f32x4 c5 = lds_rf4(ca + 80), c6 = lds_rf4(ca + 96);
            q[0] = acc[4][3] - mean * c19;
#pragma unroll
            for (int i = 0; i < 4; ++i) { q[1 + i] = acc[5][i] - mean * c5[i]; q[5 + i] = acc[6][i] - mean * c6[i]; }
        }
        if (ISB) {
            u32x4 hv[5];
            const int src = g - 2 + (wave >> 2);
            auto hload = [&]() {
                const unsigned hb = kbase + (src * 16 + p) * 20480 + (wave & 3) * 5120;
#pragma unroll
                for (int i = 0; i < 5; ++i) hv[i] = ld128c(wsr, l16 + i * 1024, hb);
            };
            if (src >= 0) {
                hload();
            } else {
#pragma unroll
                for (int i = 0; i < 5; ++i) hv[i] = (u32x4){0u, (i == 0 || (i == 2 && lane >= 32) || (i == 3 && lane < 32)) ? 0u : 0xF149u, 0u, 0u};
            }
            {
                const float qs = rstd * (0.23570226039551584f * LOG2E);
                const int hx = gq >> 1, od = gq & 1;
                const unsigned qb = L_QS + (wave >> 1) * 2304;
                lds_w128(qb + (wave & 1) * 256 + od * 1152 + hx * 512 + tk * 16, unit_pair(pk2(q[0] * qs, q[1] * qs), pk2(q[2] * qs, q[3] * qs), pk2(q[4] * qs, q[5] * qs), pk2(q[6] * qs, q[7] * qs)));
                const float q8 = q[8] * qs;
                lds_w32(qb + (wave & 1) * 64 + 1024 + hx * 1152 + tk * 4, pkv(sw16(q8, q8)));
            }
            const int h = wave & 1, jq = wave < 4 ? (wave >> 1) : 3 - ((wave - 4) >> 1);
            const int r = lane & 31, hh = lane >> 5;
            const unsigned kb = L_KT + h * 2560 + jq * 5120;
            const unsigned ka = kb + l16, la = kb + 1280 + (r >> 3) * 384 + ((r & 7) ^ 4) * 16;
            const unsigned k8 = 4 * hh + ((lane >> 2) & 3);
            const unsigned va = kb + 1024 + ((lane & 16) ? 256 + (k8 ^ 4) * 16 + 8 : (lane & 2) * 64 + k8 * 16 + (lane & 1) * 8);
            BARRIER();
            const u32x4 q0 = lds_r128(L_QS + (jq * 2 + h) * 1152 + l16);
            const unsigned q1w = lds_r32(L_QS + (jq * 2 + h) * 1152 + 1024 + r * 4);
            u32x4 km = lds_r128(ka + 8 * 5120), kl = lds_r128(la + 8 * 5120);
            u32x2 vf[2][4];
            att_vload(vf[0], va + 8 * 5120);
            const u32x4 q1 = {hh ? 0u : q1w, hh ? 0u : 0x3F80u, 0u, 0u};
            f32x16 sc[2], negm, o16 = splat16(0.f);
            sc[0] = att_scores(km, kl, q0, q1, msk);
            {
                const float m = max16(sc[0], NEGBIG);
                const float nm = -vmax(sw32(m, m));
                negm = splat16(nm);
                sc[0] += negm;
                msk = (splat16(NEGBIG) - msk) + negm;
            }
#pragma unroll
            for (int i = 8; i >= 0; --i) {
                if (i == 8) {
                    RETRY(hload(), umax4(umax4(umax4(umax4(umax4(0u, hv[0]), hv[1]), hv[2]), hv[3]), hv[4]));
#pragma unroll
                    for (int j = 0; j < 5; ++j) lds_w128(L_KT + wave * 5120 + j * 1024 + l16, hv[j]);
                    BARRIER();
                }
                SB();
                if (i > 0) { km = lds_r128(ka + (i - 1) * 5120); kl = lds_r128(la + (i - 1) * 5120); att_vload(vf[(i - 1) & 1], va + (i - 1) * 5120); }
                wof[16 - 2 * i] = ld128c(wsr, l16 + (16 - 2 * i) * 1024, wob); wof[17 - 2 * i] = ld128c(wsr, l16 + (17 - 2 * i) * 1024, wob);
                SB();
                const u32x4 p0 = att_p(sc[i & 1], 0);
                SB();
                if (i > 0) sc[(i - 1) & 1] = att_scores(km, kl, q0, q1, i == 1 ? msk : negm);
                o16 = mfmaL((u32x4){vf[i & 1][0].x, vf[i & 1][0].y, vf[i & 1][1].x, vf[i & 1][1].y}, p0, o16);
                SB();
                const u32x4 p1 = att_p(sc[i & 1], 8);
                if (i < 7 && !last) {
                    if (i > 0 || wave < 6) dma1k(nimg + 8 * (6 - i) * 1024, l16, w1k + 8 * (6 - i) * 1024);
                }
                SB();
                o16 = mfmaL((u32x4){vf[i & 1][2].x, vf[i & 1][2].y, vf[i & 1][3].x, vf[i & 1][3].y}, p1, o16);
            }
            asm volatile("s_nop 7\n\ts_nop 7" : "+v"(o16));
            {
                float t = 0.f;
#pragma unroll
                for (int v = 0; v < 11; ++v) t += __builtin_fabsf(o16[v]);
                if (__any(!(t < 3.0e38f))) {
                    float m = NEGBIG;
#pragma unroll 1
                    for (int pass = 0; pass < 2; ++pass) {
                        o16 = splat16(0.f);
#pragma unroll 1
                        for (int i = 0; i <= 8; ++i) {
                            u32x2 v4[4]; att_vload(v4, va + i * 5120);
                            f32x16 s = att_scores(lds_r128(ka + i * 5120), lds_r128(la + i * 5120), q0, q1, splat16(pass ? -m : 0.f));
                            const int lim = i == 8 ? dl : 99, lim2 = i == 0 ? dl : -99;
#pragma unroll
                            for (int v = 0; v < 16; ++v) { const int cv = (v & 3) + 8 * (v >> 2); if (cv > lim || cv <= lim2) s[v] = NEGBIG; }
                            if (pass) att_accum(s, v4, o16); else m = max16(s, m);
                        }
                        if (!pass) m = vmax(sw32(m, m));
                    }
                    asm volatile("s_nop 7\n\ts_nop 7" : "+v"(o16));
                }
            }
            const float inv = rcp(o16[10]);
            const unsigned ob = obase + (g * 8 + 2 * jq) * TKT;
            const unsigned vo = (r >> 4) * TKT + (r & 15) * 16;
            st128c(wsr, vo + hh * 256, ob + p * 1024 + h * 512, ok4((u32x4){pk2(o16[0] * inv, o16[1] * inv), pk2(o16[2] * inv, o16[3] * inv), pk2(o16[4] * inv, o16[5] * inv), pk2(o16[6] * inv, o16[7] * inv)}));
            if (hh == 0) st32c(wsr, (r >> 4) * TKT + (r & 15) * 4, ob + 16384 + p * 128 + h * 64, ok1(pk2(o16[8] * inv, o16[9] * inv)));
        } else {
            u32x4 pv[8];
            auto pload = [&]() {
                const unsigned kb = kbase + p * 16 * 3072 + wave * 256;
#pragma unroll
                for (int i = 0; i < 8; ++i) if (i < 4 || wave < 4) pv[i] = ld128c(wsr, tk * 16 + gq * 3072, kb + (i & 3) * (4 * 3072) + (i >> 2) * 2048);
            };
#pragma unroll
            for (int i = 4; i < 8; ++i) pv[i] = (u32x4){0u, 0u, 0u, 0u};
            pload();
            u32x4 pk = {tag, tag, tag, tag};
            if (wave == 7) pk = peek256(flag_at(P.ws, it ? FL_W5 : FL_W2, 0), lane);
            u32x4 fq[2]; unsigned fe;
            dual_softmax(q, rstd * LOG2E, gq < 2, fq[0], fq[1], fe);
            const unsigned q4 = (lane >> 2) & 3, c4 = lane & 3, fm = (gq & 1) * 8 + q4, fx = (gq & 1) * 2 + (q4 & 1);
            const unsigned ra = L_KVS + (gq >> 1) * 512 + c4 * 128 + fm * 8, rb = L_KVS + 1024 + (gq >> 1) * 128 + fm * 8;
            const unsigned rc = L_KVS + 1280 + c4 * 32 + fx * 8, rd = L_KVS + 1408 + fx * 8;
            {
                const unsigned k0 = (wave == 5 && tk >= 10) ? 0u : ~0u, k1 = (wave == 3 && tk >= 10) ? 0u : ~0u;
                RETRY(pload(), __builtin_elementwise_max(k0 & umax4(umax4(umax4(umax4(0u, pv[0]), pv[1]), pv[2]), pv[3]), k1 & umax4(umax4(umax4(umax4(0u, pv[4]), pv[5]), pv[6]), pv[7])));
                const u32x4 sel = onehot8(tk < 8 ? tk : -2);
                f32x4 s0 = Z4, s1 = Z4;
#pragma unroll
                for (int i = 0; i < 4; ++i) { s0 = mfma32(sel, pv[i], s0); s1 = mfma32(sel, pv[4 + i], s1); }
                const u32x2 w0 = {pk2(s0[0], s0[1]), pk2(s0[2], s0[3])}, w1v = {pk2(s1[0], s1[1]), pk2(s1[2], s1[3])};
                if (gq < 2) {
                    lds_w64(L_KVS + wave * 256 + tk * 16 + gq * 8, w0);
                    if (wave < 4) lds_w64(L_KVS + 2048 + wave * 256 + tk * 16 + gq * 8, w1v);
                }
            }
            judge256(pk, flag_at(P.ws, it ? FL_W5 : FL_W2, 0), lane, tag);
            if (first && wave == 6) colsum_get(wsr, 16 + p, L_CS + 448, lane);
            BARRIER();
            f32x4 oa[2][2];
            {
                u32x2 am[2][4], ae[2][2];
#pragma unroll
                for (int h = 0; h < 2; ++h) {
                    am[h][0] = lds_tr(ra + h * 1536); am[h][1] = lds_tr(ra + h * 1536 + 32); am[h][2] = lds_tr(rb + h * 1536); am[h][3] = lds_tr(rb + h * 1536 + 32);
                    ae[h][0] = lds_tr(rc + h * 1536); ae[h][1] = lds_tr(rd + h * 1536);
                }
                SB();
#pragma unroll
                for (int h = 0; h < 2; ++h) {
                    oa[h][0] = mfma32((u32x4){am[h][0].x, am[h][0].y, am[h][1].x, am[h][1].y}, fq[h], Z4);
                    oa[h][1] = mfma32((u32x4){am[h][2].x, am[h][2].y, am[h][3].x, am[h][3].y}, fq[h], Z4);
                }
#pragma unroll
                for (int h = 0; h < 2; ++h) {
                    const u32x4 fh = w1((gq >> 1) == h ? fe : 0u);
                    oa[h][1] = mfma32(w1(ae[h][1].x), fh, oa[h][1]); oa[h][0] = mfma32(w1(ae[h][0].x), fh, oa[h][0]);
                }
            }
            const float ia = rcp(oa[0][1][2] + 1e-6f), ib = rcp(oa[1][1][2] + 1e-6f);
            const unsigned ob = obase + (g * 8 + wave) * TKT;
            st128c(wsr, l16, ob + p * 1024, ok4((u32x4){pk2(oa[0][0][0] * ia, oa[0][0][1] * ia), pk2(oa[0][0][2] * ia, oa[0][0][3] * ia),
                                                        pk2(oa[1][0][0] * ib, oa[1][0][1] * ib), pk2(oa[1][0][2] * ib, oa[1][0][3] * ib)}));
            const unsigned lv = (gq & 1) ? pk2(oa[1][1][0] * ib, oa[1][1][1] * ib) : pk2(oa[0][1][0] * ia, oa[0][1][1] * ia);
            if (gq < 2) st32c(wsr, lane * 4, ob + 16384 + p * 128, ok1(lv));
        }
    }
    {
        const int lane = fresh(lane_in), gq = lane >> 4, tk = lane & 15;
        const unsigned l16 = lane * 16;
        BARRIER();
        u32x4 wox[18];
        if (wave >= 6) {
            const unsigned wb = WS_WOI + ((ISB ? 36 : 0) + 9 * cg + 8) * TKT;
#pragma unroll
            for (int s = 0; s < 18; ++s) wox[s] = ld128c(wsr, l16 + s * 1024, wb);
            if (ISB && it == 0 && wave == 7) wait_all256(flag_at(P.ws, FL_W4, 0), lane, tag);
        } else {
            const unsigned ob = obase + (g * 8 + 2 * tp) * TKT + w1k;
            u32x4 ov[6];
            auto oload = [&]() {
#pragma unroll
                for (int i = 0; i < 6; ++i) ov[i] = ld128c(wsr, l16 + i * 6144, ob);
            };
            __builtin_amdgcn_s_sleep(OSLEEP);
            oload();
            RETRY(oload(), umax4(umax4(umax4(umax4(umax4(umax4(0u, ov[0]), ov[1]), ov[2]), ov[3]), ov[4]), ov[5]));
#pragma unroll
            for (int i = 0; i < 6; ++i) {
                if ((i == 2 || i == 5) && wave >= 4) {
                    const unsigned d = L_OST + w1k + i * 6144 + (lane >> 4) * 256 + (lane & 3) * 64 + (lane & 12);
                    lds_w32(d, ov[i].x); lds_w32(d + 16, ov[i].y); lds_w32(d + 32, ov[i].z); lds_w32(d + 48, ov[i].w);
                } else lds_w128(L_OST + w1k + i * 6144 + l16, ov[i]);
            }
        }
        if (first && blockIdx.x == 0 && wave == 0 && lane == 0) __hip_atomic_store((GAS unsigned*)P.ws, tag, RLX_AGENT);
        BARRIER();
        {
            f32x4 c0 = res[0], c1 = res[1], c2 = res[2];
            const unsigned lo = L_OST + l16;
            if (wave >= 6) {
                u32x4 ox[18];
#pragma unroll
                for (int s = 0; s < 18; ++s) ox[s] = lds_r128(lo + (wave & 1) * TKT + s * 1024);
                SB();
#pragma unroll
                for (int s = 0; s < 18; ++s) c2 = mfma32(wox[s], ox[s], c2);
            }
            SB();
            u32x4 of[4][2];
#pragma unroll
            for (int s = 0; s < 3; ++s) { of[s][0] = lds_r128(lo + s * 1024); of[s][1] = lds_r128(lo + TKT + s * 1024); }
#pragma unroll
            for (int s = 0; s < 18; ++s) {
                SB();
                c0 = mfma32(wof[s], of[s & 3][0], c0);
                SB();
                if (s + 3 < 18) of[(s + 3) & 3][0] = lds_r128(lo + (s + 3) * 1024);
                SB();
                c1 = mfma32(wof[s], of[s & 3][1], c1);
                SB();
                if (s + 3 < 18) of[(s + 3) & 3][1] = lds_r128(lo + TKT + (s + 3) * 1024);
                if (ISB) { if ((s & 1) == 0 && s < 10 && !last && (s < 8 || wave < 4)) dma1k(nimg + (54 + 4 * s) * 1024, l16, w1k + (54 + 4 * s) * 1024); }
                else if (s < 12 && (s < 11 || wave < 2)) dma1k(nimg + 8 * s * 1024, l16, w1k + 8 * s * 1024);
            }
            res[0] = c0; res[1] = c1; res[2] = c2;
            const u32x4 pr = {pk2(c0[0], c0[1]), pk2(c0[2], c0[3]), pk2(c1[0], c1[1]), pk2(c1[2], c1[3])};
            const float qa = dot2(pr.x, pr.x, dot2(pr.y, pr.y, 0.f)), qb = dot2(pr.z, pr.z, dot2(pr.w, pr.w, 0.f));
            const u32x4 v = unit_pair(pr.x, pr.y, pr.z, pr.w);
            const u32x2 v2 = {pk2(c2[0], c2[1]), pk2(c2[2], c2[3])};
            const int ct = 9 * cg + wave, cx = 9 * cg + 8;
            const int tx = 2 * tp + (wave & 1);
            if (!last) {
                const unsigned xb = obase + ACT_BYTES + g * 8 * TKT, sb = obase + 2 * ACT_BYTES + g * 8 * 2304;
                const unsigned vo = (gq >> 1) * 256 + tk * 16;
                st128c(wsr, (gq & 1) * TKT + vo, xb + 2 * tp * TKT + ct * 512, ok4(v));
                const f32x2 u = sw16(qa, qb); const float u1 = u.x + u.y; const f32x2 u2 = sw32(u1, u1);
                if (gq < 2) st32c(wsr, gq * 2304 + tk * 4, sb + 2 * tp * 2304 + ct * 64, ok1(u_of(u2.x + u2.y)));
                if (wave >= 6) {
                    st64c(wsr, vo + (gq & 1) * 8, xb + tx * TKT + cx * 512, ok2(v2));
                    const float e0 = dot2(v2.x, v2.x, dot2(v2.y, v2.y, 0.f)); const f32x2 e1 = sw16(e0, e0); const float e2 = e1.x + e1.y; const f32x2 e3 = sw32(e2, e2);
                    if (gq == 0) st32c(wsr, tk * 4, sb + tx * 2304 + cx * 64, ok1(u_of(e3.x + e3.y)));
                }

            } else {
                const int tt = 2 * tp + (gq & 1);
                *(u32x4*)(P.out + (size_t)(g * 128 + tt * 16 + tk) * DM + 16 * ct + 8 * (gq >> 1)) = v;
                if (wave >= 6) *(u32x2*)(P.out + (size_t)(g * 128 + tx * 16 + tk) * DM + 16 * cx + 4 * gq) = v2;
            }
        }
    }
    if (ISB) {
#pragma unroll
        for (int s = 0; s < 18; ++s) asm volatile("" : "=v"(xf[s]));
#pragma unroll
        for (int t = 0; t < 5; ++t) asm volatile("" : "=v"(acc[t]));
        asm volatile("" : "=v"(ssq));
    }
}

__global__ void __launch_bounds__(512, 2) fused_kernel(Params P) {
    const int tid = threadIdx.x, lane = tid & 63, wave = __builtin_amdgcn_readfirstlane(tid >> 6);
    const int b = blockIdx.x, g = ((b >> 1) & 3) * 4 + (b >> 6), p = (b & 1) * 8 + ((b >> 3) & 7);
    const rsrc_t wsr = mk_rsrc(P.ws, WS_END);
    if (lane < 3) {
        unsigned long long pc; unsigned keep;
        asm volatile("s_getpc_b64 %0" : "=s"(pc));
        const unsigned o = (((b >> 3) * 8 + wave) * 3 + lane) * 64;
        asm volatile("s_mov_b32 %0, m0\n\ts_mov_b32 m0, %3\n\ts_nop 0\n\tglobal_load_lds_dword %1, %2\n\ts_mov_b32 m0, %0" : "=&s"(keep) : "v"(o < CODE_BYTES ? o : 0u), "s"(pc), "s"(L_CS + 64 + wave * 16) : "memory");
    }
    u32x4 xf[18];
    f32x4 res[3];
    f32x4 acc[7], sq;
    float ssq;
    Ctx C; C.wave = wave; C.g = g; C.p = p;
    {
        const int gq = lane >> 4, tk = lane & 15;
        const f32x4 Z4 = {0.f, 0.f, 0.f, 0.f};
        const unsigned epv = __hip_atomic_load((const GAS unsigned*)P.ws, RLX_AGENT);
        const u16* xs = P.x + (size_t)(g * 128) * DM;
        u32x4 raw[8];
        unsigned ph0, ph1;
        const unsigned c0 = (unsigned)(gq == 1 ? 8 : gq == 2 ? 10 : 0);
        const unsigned wv = ((unsigned)tk * NQKV + c0) * 2u, wo = p * 36 + wave * 576;
        if (wave >= 4) {
            const unsigned xo = ((unsigned)(lane >> 3) * DM + 8u * ((lane & 7) ^ (lane >> 3))) * 2u;
            __builtin_amdgcn_s_sleep(DSLEEP);
#pragma unroll 1
            for (int j = 0; j < 4; ++j) {
                const unsigned oc = (wave - 4 + 4 * j + 2 * p) & 15;
                const u16* src = xs + oc * 8 * DM;
                unsigned dst = (oc >> 1) * TKT + (oc & 1) * 1024, keep, vo = xo;
                asm volatile("s_mov_b32 %0, m0\n\t"
                             ".rept 9\n\ts_mov_b32 m0, %1\n\ts_add_u32 %1, %1, 0x800\n\tglobal_load_lds_dwordx4 %2, %3\n\tv_add_u32 %2, 0x80, %2\n\t.endr\n\t"
                             "s_mov_b32 m0, %0" : "=&s"(keep), "+s"(dst), "+v"(vo) : "s"(src) : "memory", "scc");
            }
        }
        SB();
        u32x4 im, ix, lraw[8];
        unsigned wdst = 0, km[2], kx;
        auto unit_tile = [&](const u32x4* raw, const u32x4 (&bm)[2], const u32x4 (&bl)[2], unsigned pb) {
            u32x2 lf[4];
#pragma unroll
            for (int i = 0; i < 4; ++i) {
                const f32x4 cm = mfma32(raw[i], bm[i >> 1], Z4), cl = mfma32(raw[i], bl[i >> 1], Z4);
                lds_w64(km[i >> 1] + pb + (i & 1) * 512, (u32x2){pk2(cm[0], cm[1]), pk2(cm[2], cm[3])});
                lf[i] = (u32x2){pk2(cl[0], cl[1]), pk2(cl[2], cl[3])};
            }
            if (tk < 2) {
#pragma unroll
                for (int i = 0; i < 4; ++i) lds_w64(kx + pb + (i >> 1) * 128 + (i & 1) * 512, lf[i]);
            }
        };
        auto meet = [&](unsigned word, unsigned val) {
            asm volatile("s_waitcnt lgkmcnt(0)" ::: "memory");
            if (lane == 0) lds_w32(word + wave * 4, val);
#pragma unroll 1
            for (unsigned sp = 0; sp < SPIN_CAP; ++sp) { asm volatile("" ::: "memory"); if (__all(lds_r32(word + (lane < 3 ? lane : 0) * 4) == val)) break; }
        };
        if (wave < 3) {
#pragma unroll
            for (int i = 0; i < 4; ++i) ld128_raw(raw[i], P.wqkvA + wo + g * (32 * NQKV) + (i & 1) * (16 * NQKV) + (i >> 1) * 18, wv);
#pragma unroll
            for (int i = 0; i < 4; ++i) ld128_raw(raw[4 + i], P.wqkvA + wo + (16 + (g & 1)) * (32 * NQKV) + (i & 1) * (16 * NQKV) + (i >> 1) * 18, g < 2 ? wv : 0u);
            asm volatile("global_load_dword %0, %2, %4\n\tglobal_load_dword %1, %3, %4" : "=&v"(ph0), "=&v"(ph1) : "v"(tid * 4u), "v"((tid < 132 ? tid + 192 : 0) * 4u), "s"(P.wphi + p * 648) : "memory");
            SB();
            { const int l2 = fresh(lane); im = onehot8(((l2 >> 3) & 1) == (l2 >> 4) ? (l2 & 7) : -2); ix = onehot8((l2 >> 4) == 2 ? 6 + (l2 & 1) : -2); }
            constexpr unsigned L_PHI = L_PST + 7168;
            unsigned pa[2];
#pragma unroll
            for (int i = 0; i < 2; ++i) {
                const int z = tid + 192 * i, row = z / 7, j = z - 7 * row;
                if (z < 252) lds_w32(L_PHI + row * 64 + (j < 3 ? 32 + 4 * j : 36 + 4 * j), 0u);
                const int e = 2 * z, hh = e >= 324, r = e - 324 * hh, d = r / 18, f = r - 18 * d;
                pa[i] = L_PHI + (unsigned)(hh * 18 + f) * 64u + (unsigned)(d < 16 ? d : d + 6) * 2u;
            }
            const unsigned sb = wave == 0 ? 19 : wave == 2 ? 9 : 0, sl = sb + 8;
            const unsigned ko = L_PST + (gq >> 1) * 256 + (gq & 1) * 8;
#pragma unroll
            for (int h = 0; h < 2; ++h) { const unsigned sm = sb + 4 * h + (tk & 3); km[h] = ko + (tk >> 2) * 64 + (sm >> 2) * 1024 + (sm & 3) * 16; }
            kx = ko + (tk & 1) * 64 + (sl >> 2) * 1024 + (sl & 3) * 16;
            const unsigned onea = L_PST + 4096 + (tk >> 2) * 256 + (4 * (tk & 3) + 2) * 16;
            const u32x4 one4 = {0x3F803F80u, 0x3F803F80u, 0x3F803F80u, 0x3F803F80u};
            if (wave == 2 && lane < 16) lds_w128(onea, one4);
            asm volatile("s_waitcnt vmcnt(0)" : "+v"(ph0), "+v"(ph1), "+v"(raw[0]), "+v"(raw[1]), "+v"(raw[2]), "+v"(raw[3]), "+v"(raw[4]), "+v"(raw[5]), "+v"(raw[6]), "+v"(raw[7]) :: "memory");
            lds_w16(pa[0], ph0); lds_w16(pa[0] + 64, ph0 >> 16);
            if (tid < 132) { lds_w16(pa[1], ph1); lds_w16(pa[1] + 64, ph1 >> 16); }
            const unsigned tag = __builtin_amdgcn_readfirstlane(epv) * 0x9E3779B1u + 0x7F4A7C15u;
            meet(L_CS + 32, tag);
            u32x4 bm[2] = {im, im}, bl[2] = {ix, ix};
            if (wave < 2) {
#pragma unroll
                for (int h = 0; h < 2; ++h) { bm[h] = lds_r128(L_PHI + (h * 18 + tk) * 64 + gq * 16); bl[h] = lds_r128(L_PHI + (h * 18 + 16 + (tk & 1)) * 64 + gq * 16); }
            }
            unit_tile(raw, bm, bl, 0);
            meet(L_CS, tag);
            if (wave == 2 && lane < 16) lds_w128(onea + 7168, one4);
            if (g < 2) { unit_tile(raw + 4, bm, bl, 7168); meet(L_CS + 48, tag); }
            const unsigned o = WS_IMA + p * IMG_BYTES + wave * TKT, la = L_PST + wave * 1024 + lane * 16;
#pragma unroll
            for (int u = 0; u < 2; ++u) {
                if (u == 0 || g < 2) {
                    const unsigned oo = o + (u ? 16 + g : g) * 1024;
                    const unsigned ks = p * 18 + (u ? 16 + g : g);
                    put_frag<false>(wsr, oo, ks, wave, lds_r128(la + u * 7168), lane); put_frag<false>(wsr, oo + 3 * TKT, ks, wave + 3, lds_r128(la + u * 7168 + 3072), lane);
                    if (wave == 0) put_frag<false>(wsr, oo + 6 * TKT, ks, 6, lds_r128(la + u * 7168 + 6144), lane);
                }
            }
            DRAIN();
            if (lane == 0) set_flag(P.ws, FL_IA, p * 64 + g * 3 + wave, tag);
            const rsrc_t rw = mk_rsrc(P.wqkvB, DM * NQKV * 2);
#pragma unroll
            for (int i = 0; i < 8; ++i) lraw[i] = __builtin_amdgcn_raw_buffer_load_b128(rw, (i < 4 || (g >> 1) == 1) ? wv : 0x7fffffffu,
                                                      (wo + (i < 4 ? g : 16 + (g & 1)) * (32 * NQKV) + (i & 1) * (16 * NQKV) + ((i >> 1) & 1) * 18) * 2, 0);
        } else {
            { const int l2 = fresh(lane); im = onehot8(((l2 >> 3) & 1) == (l2 >> 4) ? (l2 & 7) : -2); ix = onehot8(((l2 >> 3) & 1) + 2 == (l2 >> 4) ? (l2 & 7) : -2); }
#pragma unroll
            for (int i = 2; i < 8; ++i) lraw[i] = im;
            const int u = b + 256 * (wave - 3), m = u >= 324, r = u - 324 * m, cp = r / 18, s = r - 18 * cp;
            const unsigned w0 = (unsigned)(wo_krow(m, s, tk >> 2, tk & 3) * DM + 8 * gq) * 2u, w1o = (unsigned)(wo_krow(m, s, tk >> 2, 4 + (tk & 3)) * DM + 8 * gq) * 2u;
            const rsrc_t rw = mk_rsrc(m ? P.woB : P.woA, u < 648 ? DM * DM * 2 : 0);
            asm volatile("" :: "v"(epv));
            lraw[0] = __builtin_amdgcn_raw_buffer_load_b128(rw, w0, 64 * cp, 0); lraw[1] = __builtin_amdgcn_raw_buffer_load_b128(rw, w1o, 64 * cp, 0);
            wdst = u < 648 ? WS_WOI + (unsigned)((m * 36 + 2 * cp) * 18 + s) * 1024u : 0u;
            asm volatile("s_waitcnt vmcnt(2)" ::: "memory");
        }
        C.tag = __builtin_amdgcn_readfirstlane(epv) * 0x9E3779B1u + 0x7F4A7C15u;
        BARRIER();
        {
            const unsigned xa = (tk >> 3) * 1024 + (tk & 7) * 128;
#pragma unroll
            for (int s = 0; s < 18; ++s) xf[s] = lds_r128(wave * TKT + (s >> 1) * 2048 + xa + (((4 * (s & 1) + gq) ^ (tk & 7)) * 16));
            const int tp = (p >> 1) & 3, cg = (p >> 3) * 2 + (p & 1);
#pragma unroll
            for (int i = 0; i < 3; ++i) {
                const int tt = 2 * tp + (i < 2 ? i : (wave & 1)), ct = 9 * cg + (i < 2 ? wave : 8);
                const u32x2 r = *(const LAS u32x2*)((LAS unsigned char*)smem + tt * TKT + (ct >> 2) * 2048 + xa + (((2 * (ct & 3) + (gq >> 1)) ^ (tk & 7)) * 16) + (gq & 1) * 8);
                res[i] = (f32x4){f_of(r.x << 16), f_of(r.x & 0xffff0000u), f_of(r.y << 16), f_of(r.y & 0xffff0000u)};
            }
        }
        if (wave == 3) wait_flags(flag_at(P.ws, FL_IA, p * 64), (lane < 48 ? lane : 0) * 128, C.tag);
        BARRIER();
        const unsigned char* img = P.ws + WS_IMA + p * IMG_BYTES;
        unsigned fo[4];
#pragma unroll
        for (int i = 0; i < 4; ++i) { const int j = i * 8 + wave, jt = j / 6; fo[i] = (j < 30 ? jt * 12 + j : 60 + j) * 1024; }
        const unsigned st3 = wave < 6 ? 6144 : 2048;
        auto piece = [&](int c, int i) {
            unsigned f = c < 3 ? fo[i] + c * (i < 3 ? 6144 : st3) : (96 + i * 8 + wave) * 1024;
            if (c == 3 && i == 3) f = f < 125 * 1024 ? f : 125 * 1024;
            dma1k(img + f, lane * 16, f);
        };
#pragma unroll
        for (int i = 0; i < 8; ++i) asm volatile("" :: "v"(lraw[i]));
#pragma unroll
        for (int i = 0; i < 8; ++i) piece(i >> 2, i & 3);
        if (wave < 3) {
            const u32x4 bm[2] = {im, im}, bl[2] = {ix, ix};
            unit_tile(lraw, bm, bl, 0);
            if ((g >> 1) == 1) unit_tile(lraw + 4, bm, bl, 7168);
        } else {
            wdst = __builtin_amdgcn_readfirstlane(wdst);
            if (wdst) {
                const f32x4 a0 = mfma32(lraw[0], im, Z4), a1 = mfma32(lraw[1], im, Z4), c0 = mfma32(lraw[0], ix, Z4), c1 = mfma32(lraw[1], ix, Z4);
                st128c(wsr, lane * 16, wdst, (u32x4){pk2(a0[0], a0[1]), pk2(a0[2], a0[3]), pk2(a1[0], a1[1]), pk2(a1[2], a1[3])});
                st128c(wsr, lane * 16, wdst + TKT, (u32x4){pk2(c0[0], c0[1]), pk2(c0[2], c0[3]), pk2(c1[0], c1[1]), pk2(c1[2], c1[3])});
            }
            if (wave >= 5) arm_unit(wsr, lane * 16, wave - 5, b);
        }
#pragma unroll
        for (int t = 0; t < 7; ++t) acc[t] = Z4;
        sq = Z4;
        const unsigned lb0[3] = {(unsigned)lane * 16, (unsigned)lane * 16 + 6144, (unsigned)lane * 16 + 12288};
        if (wave < 3) asm volatile("s_waitcnt vmcnt(4)" ::: "memory"); else if (wave == 5) asm volatile("s_waitcnt vmcnt(5)" ::: "memory");
        else if (wave < 5) asm volatile("s_waitcnt vmcnt(6)" ::: "memory"); else asm volatile("s_waitcnt vmcnt(5)" ::: "memory");
        BARRIER();
        gemm_img<0, 5, 0, 6, true, 2>(acc, xf, lb0, sq, [&](int s) {
            if (s < 2 && wave < 7 && (s == 0 || (g >> 1) == 1)) put_frag<true>(wsr, WS_IMB + p * IMG_BYTES + wave * TKT + (s ? 14 + g : g) * 1024, (16 + p) * 18 + (s ? 14 + g : g), wave, lds_r128(L_PST + s * 7168 + wave * 1024 + lane * 16), lane);
            if (s >= 2) piece(2, s - 2); });
        asm volatile("s_waitcnt vmcnt(4)" ::: "memory"); BARRIER();
        if (tid == 0) set_flag(P.ws, FL_W1, g * 16 + p, C.tag);
        gemm_img<0, 5, 6, 12, true, 2>(acc, xf, lb0, sq, [&](int s) { if (s < 10) piece(3, s - 6); if (s >= 10) arm_unit(wsr, lane * 16, 3 + (s - 10) * 8 + wave, b); });
        asm volatile("s_waitcnt vmcnt(6)" ::: "memory"); BARRIER();
        gemm_img<0, 5, 12, 18, true, 2>(acc, xf, lb0, sq, [&](int s) { if (s < 17 || wave < 4) arm_unit(wsr, lane * 16, 19 + (s - 12) * 8 + wave, b); });
        asm volatile("s_nop 7" : "+v"(sq));
        const float d0 = (tk & 1) ? sq[1] : sq[0], d1 = (tk & 1) ? sq[3] : sq[2], dg = (tk & 2) ? d1 : d0;
        ssq = gq == (tk >> 2) ? dg : 0.f;
        if (wave < 4) colsum_tile(wave, L_CS, lane);
        if (wave == 0) colsum_tile(4, L_CS, lane);
        BARRIER();
    }
#pragma unroll 1
    for (int it = 0; it < 2; ++it) {
        layer_fn<false>(P, wsr, lane, C, it, xf, res, acc, ssq);
        layer_fn<true>(P, wsr, lane, C, it, xf, res, acc, ssq);
    }
}

extern "C" void kernel_launch(void* const* d_in, const int* in_sizes, int n_in, void* d_out, int out_size, void* d_ws, size_t ws_size, hipStream_t stream) {
    Params P;
    P.x = (const u16*)d_in[0]; P.wqkvA = (const u16*)d_in[1]; P.wphi = (const u16*)d_in[2]; P.woA = (const u16*)d_in[3];
    P.wqkvB = (const u16*)d_in[5]; P.woB = (const u16*)d_in[6];
    P.out = (u16*)d_out; P.ws = (unsigned char*)d_ws;
    (void)hipFuncSetAttribute((const void*)fused_kernel, hipFuncAttributeMaxDynamicSharedMemorySize, LDS_BYTES);
    hipLaunchKernelGGL(fused_kernel, dim3(256), dim3(512), LDS_BYTES, stream, P);
}
```
